```python
import torch

def _softmax(x, axis=-1):
    return torch.softmax(x, dim=axis)
def _gelu(x):
    return torch.nn.functional.gelu(x, approximate="tanh")
def _silu(x):
    return torch.nn.functional.silu(x)
def _sigmoid(x):
    return torch.sigmoid(x)
def _layernorm(x, axis=-1, eps=1e-5):
    m = x.mean(dim=axis, keepdim=True)
    v = x.var(dim=axis, keepdim=True, unbiased=False)
    return (x - m) / torch.sqrt(v + eps)
def _scatter_add(tgt, idx, src, axis=0):
    return tgt.clone().index_add_(axis, idx, src)
def _segment_sum(src, seg, n):
    out = torch.zeros((n,) + src.shape[1:], dtype=src.dtype)
    return out.index_add_(0, seg, src)
def _pad(x, pw):
    flat = tuple(v for pair in reversed(pw) for v in pair)
    return torch.nn.functional.pad(x, flat)

kernel_name = 'recurrent_scan__spectral_gfnet__dense_activation_squared_relu__prompt'

def setup_inputs():

    _w = (lambda a: (lambda t: t.to(torch.bfloat16) if t.is_floating_point() else t)(torch.as_tensor(a)))
    def _A_setup():
        B, S, D = 1, 2048, 1024
        Df = D // 2 + 1
        return {
            "x": _w((lambda *s: torch.randn(*s, dtype=torch.bfloat16) * 0.05)(B, S, D)),
            "w_r": _w((lambda *s: torch.randn(*s, dtype=torch.bfloat16) * 0.05)(S, Df) * 0.02),
            "w_i": _w((lambda *s: torch.randn(*s, dtype=torch.bfloat16) * 0.05)(S, Df) * 0.02),
        }
    _A_in = _A_setup()

    def _B_setup():
        B, S, D, F = 1, 2048, 1024, 3584
        return {
            "x": _w((lambda *s: torch.randn(*s, dtype=torch.bfloat16) * 0.05)(B, S, D)),
            "w1": _w((lambda *s: torch.randn(*s, dtype=torch.bfloat16) * 0.05)(D, F)),
            "b1": _w((lambda *s: torch.randn(*s, dtype=torch.bfloat16) * 0.4)(F)),
            "w2": _w((lambda *s: torch.randn(*s, dtype=torch.bfloat16) * 0.05)(F, D)),
            "b2": _w((lambda *s: torch.randn(*s, dtype=torch.bfloat16) * 3.2)(D)),
        }
    _B_in = _B_setup()

    return {
        "A_x": _A_in["x"],
        "A_w_r": _A_in["w_r"],
        "A_w_i": _A_in["w_i"],
        "B_x": _B_in["x"],
        "B_w1": _B_in["w1"],
        "B_b1": _B_in["b1"],
        "B_w2": _B_in["w2"],
        "B_b2": _B_in["b2"],
    }

def reference(A_x, A_w_r, A_w_i, B_x, B_w1, B_b1, B_w2, B_b2):
    def _A_ref(x, w_r, w_i):
        B, S, D = x.shape
        X = (lambda x, axes=(-2,-1): torch.fft.rfft2(x.float(), dim=axes))(x, axes=(-2, -1))
        W = w_r + 1j * w_i
        X = X * W
        y = (lambda x, s, axes=(-2,-1): torch.fft.irfft2(x, s=s, dim=axes).to(torch.bfloat16))(X, (S, D), axes=(-2, -1))
        return y

    def _B_ref(x, w1, b1, w2, b2):
        h = torch.einsum("bsd,df->bsf", x, w1) + b1
        zero = torch.zeros_like(h)
        r = torch.where(h > zero, h, zero)
        h = r * r
        out = torch.einsum("bsf,fd->bsd", h, w2) + b2
        return out

    x = A_x
    xn = _layernorm(x, axis=-1)
    s = _A_ref(xn, A_w_r, A_w_i)
    g = _silu(_B_ref(xn, B_w1, B_b1, B_w2, B_b2))
    return x + s * g
```

```cpp
#include <hip/hip_runtime.h>
#include <hip/hip_bf16.h>

__global__ __launch_bounds__(256, 1) void fused_kernel(const void* x, const void* wr, const void* wi, const void* w1, const void* b1,
                                                       const void* w2, const void* b2, void* out, void* ws) {
    __shared__ __attribute__((aligned(16))) char lds_mem[153088];
    unsigned tid = threadIdx.x;
    const unsigned lds_base = (unsigned)(size_t)(__attribute__((address_space(3))) char*)lds_mem;
    asm volatile(
    "s_load_dwordx16 s[4:19], s[0:1], 0x0\n\t"
    "s_load_dwordx2 s[20:21], s[0:1], 0x40\n\t"
    "v_readfirstlane_b32 s22, v0\n\t"
    "v_and_b32 v245, 63, v0\n\t"
    "s_and_b32 s23, s2, 7\n\t"
    "s_lshr_b32 s24, s2, 3\n\t"
    "s_lshr_b32 s22, s22, 6\n\t"
    "s_getpc_b64 s[42:43]\n\t"
    "Lpc_%=:\n\t"
    "s_and_b32 s38, s24, 7\n\t"
    "s_lshl_b32 s38, s38, 2\n\t"
    "s_add_u32 s38, s38, s22\n\t"
    "s_lshl_b32 s38, s38, 11\n\t"
    "v_and_b32 v1, 15, v0\n\t"
    "v_lshl_add_u32 v1, v1, 7, s38\n\t"
    "v_min_u32 v1, Lend_%=-Lpc_%=-4, v1\n\t"
    "global_load_dword a127, v1, s[42:43]\n\t"
    "s_lshl_b32 s28, s23, 5\n\t"
    "s_add_u32 s28, s28, s24\n\t"
    "v_bfe_u32 v3, v0, 2, 3\n\t"
    "v_bfe_u32 v4, v0, 5, 1\n\t"
    "v_and_b32 v5, 3, v0\n\t"
    "v_lshlrev_b32 v6, 4, v5\n\t"
    "v_lshl_or_b32 v6, v4, 8, v6\n\t"
    "v_lshl_or_b32 v247, v3, 11, v6\n\t"
    "s_lshl_b32 s36, s28, 14\n\t"
    "s_lshl_b32 s38, s22, 9\n\t"
    "s_add_u32 s36, s36, s38\n\t"
    "v_and_b32 v248, 7, v0\n\t"
    "v_lshlrev_b32 v248, 3, v248\n\t"
    "s_and_b32 s25, s23, 3\n\t"
    "s_and_b32 s26, s24, 7\n\t"
    "s_lshr_b32 s38, s23, 2\n\t"
    "s_lshl_b32 s38, s38, 2\n\t"
    "s_lshr_b32 s39, s24, 3\n\t"
    "s_add_u32 s38, s38, s39\n\t"
    "s_lshl_b32 s27, s38, 2\n\t"
    "s_add_u32 s27, s27, s22\n\t"
    "s_xor_b32 s29, s28, 7\n\t"
    "s_lshl_b32 s30, s23, 11\n\t"
    "s_and_b32 s30, s30, 0x2000\n\t"
    "s_add_u32 s23, s26, s38\n\t"
    "s_and_b32 s23, s23, 7\n\t"
    "v_lshlrev_b32 v240, 4, v245\n\t"
    "v_bfe_u32 v7, v0, 4, 2\n\t"
    "v_bfe_u32 v8, v0, 2, 2\n\t"
    "v_bfe_u32 v2, v0, 3, 3\n\t"
    "v_and_b32 v9, 7, v0\n\t"
    "v_xor_b32 v10, v2, v9\n\t"
    "v_lshlrev_b32 v10, 4, v10\n\t"
    "v_lshl_or_b32 v1, v2, 11, v10\n\t"
    "s_lshl_b32 s40, s22, 12\n\t"
    "s_add_u32 s40, s40, 115200\n\t"
    "s_mov_b32 m0, s40\n\t"
    "s_waitcnt lgkmcnt(0)\n\t"
    "s_add_u32 s44, s4, s36\n\t"
    "s_addc_u32 s45, s5, 0\n\t"
    "global_load_lds_dwordx4 v1, s[44:45] offset:0\n\t"
    "s_add_u32 m0, m0, 896\n\t"
    "s_nop 0\n\t"
    "global_load_lds_dwordx4 v1, s[44:45] offset:128\n\t"
    "s_add_u32 m0, m0, 896\n\t"
    "s_nop 0\n\t"
    "global_load_lds_dwordx4 v1, s[44:45] offset:256\n\t"
    "s_add_u32 m0, m0, 896\n\t"
    "s_nop 0\n\t"
    "global_load_lds_dwordx4 v1, s[44:45] offset:384\n\t"
    "v_lshl_or_b32 v10, v9, 7, v10\n\t"
    "v_add_u32 v208, s40, v10\n\t"
    "v_xor_b32 v10, v3, v5\n\t"
    "v_lshlrev_b32 v10, 4, v10\n\t"
    "v_lshl_or_b32 v10, v3, 7, v10\n\t"
    "v_lshl_or_b32 v10, v4, 11, v10\n\t"
    "v_add_u32 v209, s40, v10\n\t"
    "v_xor_b32 v210, 64, v209\n\t"
    "s_add_u32 s20, s20, 0x200000\n\t"
    "s_addc_u32 s21, s21, 0\n\t"
    "s_and_b32 s20, s20, 0xffe00000\n\t"
    "global_load_dwordx2 v[250:251], v248, s[20:21] sc1\n\t"
    "s_lshl_b32 s38, s2, 2\n\t"
    "s_add_u32 s38, s38, s22\n\t"
    "s_lshl_b32 s38, s38, 9\n\t"
    "s_add_u32 s38, s38, 0x40000\n\t"
    "s_add_u32 s4, s20, s38\n\t"
    "s_addc_u32 s5, s21, 0\n\t"
    "v_and_b32 v10, 31, v0\n\t"
    "v_lshlrev_b32 v10, 3, v10\n\t"
    "v_lshl_or_b32 v10, v4, 21, v10\n\t"
    "v_add_u32 v10, s38, v10\n\t"
    "s_lshl_b32 s39, s29, 6\n\t"
    "s_add_u32 s39, s39, 114688\n\t"
    "v_add_u32 v11, s39, v248\n\t"
    "v_cmp_lt_u32 vcc, 55, v245\n\t"
    "v_mov_b32 v12, -1\n\t"
    "v_mov_b32 v13, -1\n\t"
    "v_cndmask_b32 v10, v10, v11, vcc\n\t"
    "global_store_dwordx2 v10, v[12:13], s[20:21] sc1\n\t"
    "v_and_b32 v64, 31, v0\n\t"
    "v_cvt_f32_u32 v64, v64\n\t"
    "s_lshl_b32 s38, s22, 3\n\t"
    "v_lshl_add_u32 v65, v4, 2, s38\n\t"
    "v_cvt_f32_u32 v65, v65\n\t"
    "v_mul_f32 v66, 0x3a800000, v64\n\t"
    "v_mul_f32 v67, v66, v65\n\t"
    "v_mul_f32 v64, 0x3a000000, v64\n\t"
    "v_fmamk_f32 v64, v65, 0x3c800000, v64\n\t"
    "s_mov_b32 s72, 0x28002800\n\t"
    "s_lshl_b32 s38, s22, 10\n\t"
    "s_add_u32 s38, s38, 94208\n\t"
    "v_add_u32 v105, s38, v240\n\t"
    "v_add_u32 v106, 94208, v240\n\t"
    "s_lshr_b32 s38, s22, 1\n\t"
    "s_lshl_b32 s38, s38, 12\n\t"
    "v_add_u32 v15, s38, v106\n\t"
    "v_mul_f32 v68, 0x42000000, v67\n\t"
    "v_cos_f32 v69, v68\n\t"
    "v_sin_f32 v70, v68\n\t"
    "v_cos_f32 v92, v67\n\t"
    "v_sin_f32 v93, v67\n\t"
    "v_sin_f32 v71, v64\n\t"
    "v_cos_f32 v68, v64\n\t"
    "v_cvt_pk_f16_f32 v72, v69, v70\n\t"
    "v_cvt_pk_f16_f32 v76, -v70, v69\n\t"
    "v_cvt_pk_f16_f32 v80, v92, v93\n\t"
    "v_cvt_pk_f16_f32 v84, -v71, v68\n\t"
    "v_cvt_pk_f16_f32 v88, v71, v68\n\t"
    "v_pk_mul_f16 v80, v80, s72\n\t"
    "v_add_f32 v67, v67, v66\n\t"
    "v_add_f32 v64, 0x3c800000, v64\n\t"
    "v_mul_f32 v68, 0x42000000, v67\n\t"
    "v_cos_f32 v69, v68\n\t"
    "v_sin_f32 v70, v68\n\t"
    "v_cos_f32 v92, v67\n\t"
    "v_sin_f32 v93, v67\n\t"
    "v_sin_f32 v71, v64\n\t"
    "v_cos_f32 v68, v64\n\t"
    "v_cvt_pk_f16_f32 v73, v69, v70\n\t"
    "v_cvt_pk_f16_f32 v77, -v70, v69\n\t"
    "v_cvt_pk_f16_f32 v81, v92, v93\n\t"
    "v_cvt_pk_f16_f32 v85, -v71, v68\n\t"
    "v_cvt_pk_f16_f32 v89, v71, v68\n\t"
    "v_pk_mul_f16 v81, v81, s72\n\t"
    "v_add_f32 v67, v67, v66\n\t"
    "v_add_f32 v64, 0x3c800000, v64\n\t"
    "v_mul_f32 v68, 0x42000000, v67\n\t"
    "v_cos_f32 v69, v68\n\t"
    "v_sin_f32 v70, v68\n\t"
    "v_cos_f32 v92, v67\n\t"
    "v_sin_f32 v93, v67\n\t"
    "v_sin_f32 v71, v64\n\t"
    "v_cos_f32 v68, v64\n\t"
    "v_cvt_pk_f16_f32 v74, v69, v70\n\t"
    "v_cvt_pk_f16_f32 v78, -v70, v69\n\t"
    "v_cvt_pk_f16_f32 v82, v92, v93\n\t"
    "v_cvt_pk_f16_f32 v86, -v71, v68\n\t"
    "v_cvt_pk_f16_f32 v90, v71, v68\n\t"
    "v_pk_mul_f16 v82, v82, s72\n\t"
    "v_add_f32 v67, v67, v66\n\t"
    "v_add_f32 v64, 0x3c800000, v64\n\t"
    "v_mul_f32 v68, 0x42000000, v67\n\t"
    "v_cos_f32 v69, v68\n\t"
    "v_sin_f32 v70, v68\n\t"
    "v_cos_f32 v92, v67\n\t"
    "v_sin_f32 v93, v67\n\t"
    "v_sin_f32 v71, v64\n\t"
    "v_cos_f32 v68, v64\n\t"
    "v_cvt_pk_f16_f32 v75, v69, v70\n\t"
    "v_cvt_pk_f16_f32 v79, -v70, v69\n\t"
    "v_cvt_pk_f16_f32 v83, v92, v93\n\t"
    "v_cvt_pk_f16_f32 v87, -v71, v68\n\t"
    "v_cvt_pk_f16_f32 v91, v71, v68\n\t"
    "v_pk_mul_f16 v83, v83, s72\n\t"
    "ds_write_b128 v105, v[72:75] offset:0\n\t"
    "ds_write_b128 v105, v[76:79] offset:4096\n\t"
    "ds_write_b128 v105, v[80:83] offset:8192\n\t"
    "ds_write_b128 v105, v[84:87] offset:12288\n\t"
    "ds_write_b128 v105, v[88:91] offset:16384\n\t"
    "s_bfe_u32 s38, s28, 0x40003\n\t"
    "s_lshl_b32 s38, s38, 2\n\t"
    "s_add_u32 s38, s38, s22\n\t"
    "s_lshl_b32 s38, s38, 16\n\t"
    "s_lshr_b32 s39, s38, 5\n\t"
    "s_xor_b32 s39, s39, s30\n\t"
    "s_and_b32 s39, s39, 0x2000\n\t"
    "s_or_b32 s38, s38, s39\n\t"
    "s_bfe_u32 s39, s28, 0x20001\n\t"
    "s_lshl_b32 s39, s39, 10\n\t"
    "s_add_u32 s38, s38, s39\n\t"
    "s_and_b32 s39, s28, 1\n\t"
    "s_lshl_b32 s39, s39, 7\n\t"
    "s_add_u32 s38, s38, s39\n\t"
    "s_add_u32 s38, s38, 0x400000\n\t"
    "s_add_u32 s46, s20, s38\n\t"
    "s_addc_u32 s47, s21, 0\n\t"
    "s_add_u32 s48, s46, 0x8000\n\t"
    "s_addc_u32 s49, s47, 0\n\t"
    "v_lshlrev_b32 v96, 12, v4\n\t"
    "v_and_b32 v10, 3, v2\n\t"
    "v_lshl_or_b32 v96, v10, 8, v96\n\t"
    "v_lshl_or_b32 v96, v9, 4, v96\n\t"
    "v_add_u32 v211, 16384, v96\n\t"
    "s_lshl_b32 s38, s22, 3\n\t"
    "s_add_u32 s38, s38, 152064\n\t"
    "v_lshl_add_u32 v98, v9, 5, s38\n\t"
    "v_lshlrev_b32 v99, 5, v9\n\t"
    "v_add_u32 v99, 152064, v99\n\t"
    "s_mul_i32 s38, s28, 4104\n\t"
    "s_lshr_b32 s39, s22, 1\n\t"
    "s_mul_i32 s39, s39, 1050624\n\t"
    "s_add_u32 s39, s39, s38\n\t"
    "s_bitcmp1_b32 s22, 0\n\t"
    "s_cselect_b64 s[44:45], s[8:9], s[6:7]\n\t"
    "s_add_u32 s44, s44, s39\n\t"
    "s_addc_u32 s45, s45, 0\n\t"
    "s_lshl_b32 s39, s22, 8\n\t"
    "s_add_u32 s38, s38, s39\n\t"
    "s_add_u32 s54, s6, s38\n\t"
    "s_addc_u32 s55, s7, 0\n\t"
    "s_add_u32 s56, s8, s38\n\t"
    "s_addc_u32 s57, s9, 0\n\t"
    "v_mul_u32_u24 v108, 1026, v7\n\t"
    "v_and_b32 v14, 15, v0\n\t"
    "v_lshl_add_u32 v109, v14, 4, v108\n\t"
    "v_add_u32 v110, 1050624, v109\n\t"
    "s_mul_i32 s31, s22, 2056\n\t"
    "v_xor_b32 v13, v7, v8\n\t"
    "v_lshlrev_b32 v13, 6, v13\n\t"
    "v_lshl_or_b32 v243, v5, 4, v13\n\t"
    "v_mul_u32_u24 v241, 7168, v7\n\t"
    "v_add_u32 v241, v241, v243\n\t"
    "v_add_u32 v242, 27648, v241\n\t"
    "s_mul_i32 s40, s25, 7\n\t"
    "s_min_u32 s38, s26, 6\n\t"
    "s_add_u32 s40, s40, s38\n\t"
    "s_lshl_b32 s40, s40, 8\n\t"
    "s_mul_i32 s38, s22, 57344\n\t"
    "s_add_u32 s38, s38, s40\n\t"
    "s_add_u32 s68, s10, s38\n\t"
    "s_addc_u32 s69, s11, 0\n\t"
    "s_and_b32 s69, s69, 0xffff\n\t"
    "s_mov_b32 s70, 0x6d4500\n\t"
    "s_mov_b32 s71, 0x20000\n\t"
    "s_add_u32 s12, s12, s40\n\t"
    "s_addc_u32 s13, s13, 0\n\t"
    "s_add_u32 m0, s31, 0\n\t"
    "s_mov_b32 s35, 0\n\t"
    "buffer_load_dwordx4 v241, s[68:71], s35 offen sc1 lds\n\t"
    "buffer_load_dwordx4 v242, s[68:71], s35 offen offset:1024 sc1 lds\n\t"
    "s_add_u32 m0, s31, 8224\n\t"
    "s_mov_b32 s35, 229376\n\t"
    "buffer_load_dwordx4 v241, s[68:71], s35 offen sc1 lds\n\t"
    "buffer_load_dwordx4 v242, s[68:71], s35 offen offset:1024 sc1 lds\n\t"
    "s_lshl_b32 s38, s22, 9\n\t"
    "s_add_u32 s38, s38, 133632\n\t"
    "v_mul_u32_u24 v97, 2064, v9\n\t"
    "v_lshl_add_u32 v97, v2, 4, v97\n\t"
    "v_add_u32 v97, s38, v97\n\t"
    "s_mul_i32 s38, s22, 4128\n\t"
    "s_add_u32 s38, s38, 133632\n\t"
    "v_lshl_add_u32 v100, v4, 2, v8\n\t"
    "v_lshlrev_b32 v100, 6, v100\n\t"
    "v_and_b32 v11, 1, v7\n\t"
    "v_lshl_or_b32 v100, v11, 5, v100\n\t"
    "v_lshl_or_b32 v100, v5, 3, v100\n\t"
    "v_add_u32 v100, s38, v100\n\t"
    "s_mov_b32 s60, 0x05040100\n\t"
    "s_mov_b32 s61, 0x07060302\n\t"
    "s_mul_i32 s38, s22, 4160\n\t"
    "s_add_u32 s38, s38, 115200\n\t"
    "v_and_b32 v11, 31, v0\n\t"
    "v_lshrrev_b32 v12, 1, v11\n\t"
    "v_and_b32 v13, 1, v11\n\t"
    "v_lshl_add_u32 v12, v12, 2, v13\n\t"
    "v_lshl_add_u32 v12, v4, 8, v12\n\t"
    "v_lshl_add_u32 v101, v12, 2, s38\n\t"
    "v_sub_u32 v11, 32, v11\n\t"
    "v_lshrrev_b32 v12, 1, v11\n\t"
    "v_lshl_add_u32 v12, v12, 2, v13\n\t"
    "v_lshlrev_b32 v13, 8, v4\n\t"
    "v_sub_u32 v12, v12, v13\n\t"
    "v_add_u32 v13, 258, v12\n\t"
    "v_lshl_add_u32 v102, v13, 2, s38\n\t"
    "v_add_u32 v13, 962, v12\n\t"
    "v_and_b32 v13, 1023, v13\n\t"
    "v_lshl_add_u32 v103, v13, 2, s38\n\t"
    "s_lshl_b32 s38, s22, 10\n\t"
    "s_add_u32 s38, s38, 115200\n\t"
    "v_mul_u32_u24 v104, 4160, v5\n\t"
    "v_lshrrev_b32 v11, 2, v245\n\t"
    "v_lshl_add_u32 v104, v11, 4, v104\n\t"
    "v_add_u32 v104, s38, v104\n\t"
    "v_lshlrev_b32 v12, 4, v5\n\t"
    "v_lshl_or_b32 v231, v11, 7, v12\n\t"
    "v_lshl_or_b32 v230, v11, 9, v12\n\t"
    "s_lshl_b32 s38, s22, 12\n\t"
    "s_add_u32 s38, s38, 94208\n\t"
    "v_lshlrev_b32 v12, 2, v7\n\t"
    "v_xor_b32 v12, v12, v14\n\t"
    "v_lshl_or_b32 v12, v7, 4, v12\n\t"
    "v_lshl_add_u32 v228, v12, 4, s38\n\t"
    "v_lshlrev_b32 v12, 2, v5\n\t"
    "v_xor_b32 v12, v12, v11\n\t"
    "v_lshl_or_b32 v12, v5, 4, v12\n\t"
    "v_lshl_add_u32 v227, v12, 4, s38\n\t"
    "v_lshl_add_u32 v229, v7, 1, v248\n\t"
    "s_lshl_b32 s38, s28, 2\n\t"
    "s_add_u32 s38, s38, s22\n\t"
    "s_lshl_b32 s38, s38, 13\n\t"
    "s_lshl_b32 s39, s28, 6\n\t"
    "s_add_u32 s38, s38, 0x801000\n\t"
    "s_add_u32 s50, s20, s38\n\t"
    "s_addc_u32 s51, s21, 0\n\t"
    "s_add_u32 s39, s39, 0x20000\n\t"
    "s_add_u32 s42, s20, s39\n\t"
    "s_addc_u32 s43, s21, 0\n\t"
    "s_cmp_eq_u32 s22, 0\n\t"
    "s_cselect_b64 s[58:59], 0xff, 0\n\t"
    "s_lshl_b32 s38, s28, 6\n\t"
    "s_lshl_b32 s39, s22, 14\n\t"
    "s_add_u32 s38, s38, s39\n\t"
    "s_add_u32 s38, s38, 0x1f0000\n\t"
    "s_add_u32 s94, s20, s38\n\t"
    "s_addc_u32 s95, s21, 0\n\t"
    "s_mov_b64 exec, 0\n\t"
    "s_cmp_eq_u32 s22, 0\n\t"
    "s_cbranch_scc1 Lpad1_%=\n\t"
    "s_cmp_eq_u32 s22, 1\n\t"
    "s_cbranch_scc1 Lpad2_%=\n\t"
    "s_cmp_eq_u32 s22, 2\n\t"
    "s_cbranch_scc1 Lpad3_%=\n\t"
    "s_branch Lpad4_%=\n\t"
    "LretA_%=:\n\t"
    "s_mov_b64 exec, -1\n\t"
    "s_waitcnt vmcnt(9)\n\t"
    "ds_read_b128 v[112:115], v208 offset:0\n\t"
    "s_waitcnt vmcnt(8)\n\t"
    "ds_read_b128 v[116:119], v208 offset:1024\n\t"
    "s_waitcnt vmcnt(7)\n\t"
    "ds_read_b128 v[120:123], v208 offset:2048\n\t"
    "s_waitcnt vmcnt(6)\n\t"
    "ds_read_b128 v[124:127], v208 offset:3072\n\t"
    "global_load_dwordx4 v[128:131], v109, s[54:55]\n\t"
    "global_load_dwordx4 v[132:135], v109, s[56:57]\n\t"
    "global_load_dwordx4 v[136:139], v110, s[54:55]\n\t"
    "global_load_dwordx4 v[140:143], v110, s[56:57]\n\t"
    "global_load_ushort v111, v108, s[44:45] offset:1024\n\t"
    "ds_read_b128 v[192:195], v209 offset:0\n\t"
    "ds_read_b128 v[196:199], v210 offset:0\n\t"
    "ds_read_b128 v[200:203], v209 offset:1024\n\t"
    "ds_read_b128 v[204:207], v210 offset:1024\n\t"
    "s_waitcnt lgkmcnt(7)\n\t"
    "v_lshlrev_b32 v16, 16, v112\n\t"
    "v_and_b32 v17, 0xffff0000, v112\n\t"
    "v_lshlrev_b32 v18, 16, v113\n\t"
    "v_and_b32 v19, 0xffff0000, v113\n\t"
    "v_lshlrev_b32 v20, 16, v114\n\t"
    "v_and_b32 v21, 0xffff0000, v114\n\t"
    "v_lshlrev_b32 v22, 16, v115\n\t"
    "v_and_b32 v23, 0xffff0000, v115\n\t"
    "v_pk_add_f32 v[48:49], v[18:19], v[16:17]\n\t"
    "v_pk_add_f32 v[48:49], v[20:21], v[48:49]\n\t"
    "v_pk_add_f32 v[48:49], v[22:23], v[48:49]\n\t"
    "s_waitcnt lgkmcnt(6)\n\t"
    "v_lshlrev_b32 v24, 16, v116\n\t"
    "v_and_b32 v25, 0xffff0000, v116\n\t"
    "v_lshlrev_b32 v26, 16, v117\n\t"
    "v_and_b32 v27, 0xffff0000, v117\n\t"
    "v_lshlrev_b32 v28, 16, v118\n\t"
    "v_and_b32 v29, 0xffff0000, v118\n\t"
    "v_lshlrev_b32 v30, 16, v119\n\t"
    "v_and_b32 v31, 0xffff0000, v119\n\t"
    "v_pk_add_f32 v[48:49], v[24:25], v[48:49]\n\t"
    "v_pk_add_f32 v[48:49], v[26:27], v[48:49]\n\t"
    "v_pk_add_f32 v[48:49], v[28:29], v[48:49]\n\t"
    "v_pk_add_f32 v[48:49], v[30:31], v[48:49]\n\t"
    "s_waitcnt lgkmcnt(5)\n\t"
    "v_lshlrev_b32 v32, 16, v120\n\t"
    "v_and_b32 v33, 0xffff0000, v120\n\t"
    "v_lshlrev_b32 v34, 16, v121\n\t"
    "v_and_b32 v35, 0xffff0000, v121\n\t"
    "v_lshlrev_b32 v36, 16, v122\n\t"
    "v_and_b32 v37, 0xffff0000, v122\n\t"
    "v_lshlrev_b32 v38, 16, v123\n\t"
    "v_and_b32 v39, 0xffff0000, v123\n\t"
    "v_pk_add_f32 v[48:49], v[32:33], v[48:49]\n\t"
    "v_pk_add_f32 v[48:49], v[34:35], v[48:49]\n\t"
    "v_pk_add_f32 v[48:49], v[36:37], v[48:49]\n\t"
    "v_pk_add_f32 v[48:49], v[38:39], v[48:49]\n\t"
    "s_waitcnt lgkmcnt(4)\n\t"
    "v_lshlrev_b32 v40, 16, v124\n\t"
    "v_and_b32 v41, 0xffff0000, v124\n\t"
    "v_lshlrev_b32 v42, 16, v125\n\t"
    "v_and_b32 v43, 0xffff0000, v125\n\t"
    "v_lshlrev_b32 v44, 16, v126\n\t"
    "v_and_b32 v45, 0xffff0000, v126\n\t"
    "v_lshlrev_b32 v46, 16, v127\n\t"
    "v_and_b32 v47, 0xffff0000, v127\n\t"
    "v_pk_add_f32 v[48:49], v[40:41], v[48:49]\n\t"
    "v_pk_add_f32 v[48:49], v[42:43], v[48:49]\n\t"
    "v_pk_add_f32 v[48:49], v[44:45], v[48:49]\n\t"
    "v_pk_add_f32 v[48:49], v[46:47], v[48:49]\n\t"
    "v_add_f32 v48, v48, v49\n\t"
    "s_nop 1\n\t"
    "v_add_f32_dpp v48, v48, v48 row_ror:8 row_mask:0xf bank_mask:0xf\n\t"
    "v_mov_b32 v49, v48\n\t"
    "s_nop 1\n\t"
    "v_permlane16_swap_b32 v48, v49\n\t"
    "v_add_f32 v48, v48, v49\n\t"
    "v_mov_b32 v49, v48\n\t"
    "s_nop 1\n\t"
    "v_permlane32_swap_b32 v48, v49\n\t"
    "v_add_f32 v48, v48, v49\n\t"
    "v_mul_f32 v50, 0x3b800000, v48\n\t"
    "v_pk_add_f32 v[16:17], v[16:17], v[50:51] op_sel_hi:[1,0] neg_lo:[0,1] neg_hi:[0,1]\n\t"
    "v_pk_mul_f32 v[48:49], v[16:17], v[16:17]\n\t"
    "v_pk_add_f32 v[18:19], v[18:19], v[50:51] op_sel_hi:[1,0] neg_lo:[0,1] neg_hi:[0,1]\n\t"
    "v_pk_fma_f32 v[48:49], v[18:19], v[18:19], v[48:49]\n\t"
    "v_pk_add_f32 v[20:21], v[20:21], v[50:51] op_sel_hi:[1,0] neg_lo:[0,1] neg_hi:[0,1]\n\t"
    "v_pk_fma_f32 v[48:49], v[20:21], v[20:21], v[48:49]\n\t"
    "v_pk_add_f32 v[22:23], v[22:23], v[50:51] op_sel_hi:[1,0] neg_lo:[0,1] neg_hi:[0,1]\n\t"
    "v_pk_fma_f32 v[48:49], v[22:23], v[22:23], v[48:49]\n\t"
    "v_pk_add_f32 v[24:25], v[24:25], v[50:51] op_sel_hi:[1,0] neg_lo:[0,1] neg_hi:[0,1]\n\t"
    "v_pk_fma_f32 v[48:49], v[24:25], v[24:25], v[48:49]\n\t"
    "v_pk_add_f32 v[26:27], v[26:27], v[50:51] op_sel_hi:[1,0] neg_lo:[0,1] neg_hi:[0,1]\n\t"
    "v_pk_fma_f32 v[48:49], v[26:27], v[26:27], v[48:49]\n\t"
    "v_pk_add_f32 v[28:29], v[28:29], v[50:51] op_sel_hi:[1,0] neg_lo:[0,1] neg_hi:[0,1]\n\t"
    "v_pk_fma_f32 v[48:49], v[28:29], v[28:29], v[48:49]\n\t"
    "v_pk_add_f32 v[30:31], v[30:31], v[50:51] op_sel_hi:[1,0] neg_lo:[0,1] neg_hi:[0,1]\n\t"
    "v_pk_fma_f32 v[48:49], v[30:31], v[30:31], v[48:49]\n\t"
    "v_pk_add_f32 v[32:33], v[32:33], v[50:51] op_sel_hi:[1,0] neg_lo:[0,1] neg_hi:[0,1]\n\t"
    "v_pk_fma_f32 v[48:49], v[32:33], v[32:33], v[48:49]\n\t"
    "v_pk_add_f32 v[34:35], v[34:35], v[50:51] op_sel_hi:[1,0] neg_lo:[0,1] neg_hi:[0,1]\n\t"
    "v_pk_fma_f32 v[48:49], v[34:35], v[34:35], v[48:49]\n\t"
    "v_pk_add_f32 v[36:37], v[36:37], v[50:51] op_sel_hi:[1,0] neg_lo:[0,1] neg_hi:[0,1]\n\t"
    "v_pk_fma_f32 v[48:49], v[36:37], v[36:37], v[48:49]\n\t"
    "v_pk_add_f32 v[38:39], v[38:39], v[50:51] op_sel_hi:[1,0] neg_lo:[0,1] neg_hi:[0,1]\n\t"
    "v_pk_fma_f32 v[48:49], v[38:39], v[38:39], v[48:49]\n\t"
    "v_pk_add_f32 v[40:41], v[40:41], v[50:51] op_sel_hi:[1,0] neg_lo:[0,1] neg_hi:[0,1]\n\t"
    "v_pk_fma_f32 v[48:49], v[40:41], v[40:41], v[48:49]\n\t"
    "v_pk_add_f32 v[42:43], v[42:43], v[50:51] op_sel_hi:[1,0] neg_lo:[0,1] neg_hi:[0,1]\n\t"
    "v_pk_fma_f32 v[48:49], v[42:43], v[42:43], v[48:49]\n\t"
    "v_pk_add_f32 v[44:45], v[44:45], v[50:51] op_sel_hi:[1,0] neg_lo:[0,1] neg_hi:[0,1]\n\t"
    "v_pk_fma_f32 v[48:49], v[44:45], v[44:45], v[48:49]\n\t"
    "v_pk_add_f32 v[46:47], v[46:47], v[50:51] op_sel_hi:[1,0] neg_lo:[0,1] neg_hi:[0,1]\n\t"
    "v_pk_fma_f32 v[48:49], v[46:47], v[46:47], v[48:49]\n\t"
    "v_add_f32 v48, v48, v49\n\t"
    "s_nop 1\n\t"
    "v_add_f32_dpp v48, v48, v48 row_ror:8 row_mask:0xf bank_mask:0xf\n\t"
    "v_mov_b32 v49, v48\n\t"
    "s_nop 1\n\t"
    "v_permlane16_swap_b32 v48, v49\n\t"
    "v_add_f32 v48, v48, v49\n\t"
    "v_mov_b32 v49, v48\n\t"
    "s_nop 1\n\t"
    "v_permlane32_swap_b32 v48, v49\n\t"
    "v_add_f32 v51, v48, v49\n\t"
    "ds_write_b64 v98, v[50:51]\n\t"
    "s_waitcnt vmcnt(10)\n\t"
    "v_readfirstlane_b32 s74, v250\n\t"
    "v_readfirstlane_b32 s75, v251\n\t"
    "s_nop 0\n\t"
    "s_and_b32 s74, s74, 0xffff0000\n\t"
    "s_add_u32 s74, s74, 0x10000\n\t"
    "s_addc_u32 s75, s75, 0\n\t"
    "s_waitcnt lgkmcnt(0)\n\t"
    "s_barrier\n\t"
    "ds_read_b128 v[52:55], v99\n\t"
    "ds_read_b128 v[56:59], v99 offset:16\n\t"
    "ds_read_b128 a[224:227], v106 offset:0\n\t"
    "ds_read_b128 a[228:231], v106 offset:1024\n\t"
    "ds_read_b128 a[232:235], v106 offset:2048\n\t"
    "ds_read_b128 a[236:239], v106 offset:3072\n\t"
    "ds_read_b128 a[240:243], v106 offset:4096\n\t"
    "ds_read_b128 a[244:247], v106 offset:5120\n\t"
    "ds_read_b128 a[248:251], v106 offset:6144\n\t"
    "ds_read_b128 a[252:255], v106 offset:7168\n\t"
    "ds_read_b128 v[160:163], v106 offset:8192\n\t"
    "ds_read_b128 v[164:167], v106 offset:9216\n\t"
    "ds_read_b128 v[168:171], v106 offset:10240\n\t"
    "ds_read_b128 v[172:175], v106 offset:11264\n\t"
    "s_waitcnt lgkmcnt(13)\n\t"
    "v_pk_add_f32 v[60:61], v[52:53], v[54:55]\n\t"
    "s_waitcnt lgkmcnt(12)\n\t"
    "v_pk_add_f32 v[62:63], v[56:57], v[58:59]\n\t"
    "v_pk_add_f32 v[60:61], v[60:61], v[62:63]\n\t"
    "v_mul_f32 v62, 0x3e800000, v60\n\t"
    "v_sub_f32 v52, v52, v62\n\t"
    "v_sub_f32 v54, v54, v62\n\t"
    "v_sub_f32 v56, v56, v62\n\t"
    "v_sub_f32 v58, v58, v62\n\t"
    "v_mul_f32 v63, v52, v52\n\t"
    "v_fmac_f32 v63, v54, v54\n\t"
    "v_fmac_f32 v63, v56, v56\n\t"
    "v_fmac_f32 v63, v58, v58\n\t"
    "v_fmamk_f32 v61, v63, 0x43800000, v61\n\t"
    "v_mul_f32 v61, 0x3a800000, v61\n\t"
    "v_cvt_pk_bf16_f32 v60, v61, v62\n\t"
    "v_lshlrev_b32 v61, 16, v60\n\t"
    "v_and_b32 v62, 0xffff0000, v60\n\t"
    "v_add_f32 v61, 0x3727c5ac, v61\n\t"
    "v_cvt_pk_bf16_f32 v61, v61, v61\n\t"
    "v_lshlrev_b32 v61, 16, v61\n\t"
    "v_sqrt_f32 v61, v61\n\t"
    "v_sub_f32 v62, v50, v62\n\t"
    "v_cvt_pk_bf16_f32 v61, v61, v61\n\t"
    "v_lshlrev_b32 v61, 16, v61\n\t"
    "v_rcp_f32 v60, v61\n\t"
    "s_nop 0\n\t"
    "v_mul_f32 v61, v62, v60\n\t"
    "v_pk_fma_f32 v[16:17], v[16:17], v[60:61], v[60:61] op_sel:[0,0,1] op_sel_hi:[1,0,1]\n\t"
    "v_pk_fma_f32 v[18:19], v[18:19], v[60:61], v[60:61] op_sel:[0,0,1] op_sel_hi:[1,0,1]\n\t"
    "v_pk_fma_f32 v[20:21], v[20:21], v[60:61], v[60:61] op_sel:[0,0,1] op_sel_hi:[1,0,1]\n\t"
    "v_pk_fma_f32 v[22:23], v[22:23], v[60:61], v[60:61] op_sel:[0,0,1] op_sel_hi:[1,0,1]\n\t"
    "v_cvt_pk_bf16_f32 v64, v16, v17\n\t"
    "v_cvt_pk_bf16_f32 v65, v18, v19\n\t"
    "v_cvt_pk_bf16_f32 v66, v20, v21\n\t"
    "v_cvt_pk_bf16_f32 v67, v22, v23\n\t"
    "global_store_dwordx4 v96, v[64:67], s[46:47] sc1\n\t"
    "v_pk_fma_f32 v[24:25], v[24:25], v[60:61], v[60:61] op_sel:[0,0,1] op_sel_hi:[1,0,1]\n\t"
    "v_pk_fma_f32 v[26:27], v[26:27], v[60:61], v[60:61] op_sel:[0,0,1] op_sel_hi:[1,0,1]\n\t"
    "v_pk_fma_f32 v[28:29], v[28:29], v[60:61], v[60:61] op_sel:[0,0,1] op_sel_hi:[1,0,1]\n\t"
    "v_pk_fma_f32 v[30:31], v[30:31], v[60:61], v[60:61] op_sel:[0,0,1] op_sel_hi:[1,0,1]\n\t"
    "v_cvt_pk_bf16_f32 v68, v24, v25\n\t"
    "v_cvt_pk_bf16_f32 v69, v26, v27\n\t"
    "v_cvt_pk_bf16_f32 v70, v28, v29\n\t"
    "v_cvt_pk_bf16_f32 v71, v30, v31\n\t"
    "global_store_dwordx4 v211, v[68:71], s[46:47] sc1\n\t"
    "v_pk_fma_f32 v[32:33], v[32:33], v[60:61], v[60:61] op_sel:[0,0,1] op_sel_hi:[1,0,1]\n\t"
    "v_pk_fma_f32 v[34:35], v[34:35], v[60:61], v[60:61] op_sel:[0,0,1] op_sel_hi:[1,0,1]\n\t"
    "v_pk_fma_f32 v[36:37], v[36:37], v[60:61], v[60:61] op_sel:[0,0,1] op_sel_hi:[1,0,1]\n\t"
    "v_pk_fma_f32 v[38:39], v[38:39], v[60:61], v[60:61] op_sel:[0,0,1] op_sel_hi:[1,0,1]\n\t"
    "v_cvt_pk_bf16_f32 v72, v32, v33\n\t"
    "v_cvt_pk_bf16_f32 v73, v34, v35\n\t"
    "v_cvt_pk_bf16_f32 v74, v36, v37\n\t"
    "v_cvt_pk_bf16_f32 v75, v38, v39\n\t"
    "global_store_dwordx4 v96, v[72:75], s[48:49] sc1\n\t"
    "v_pk_fma_f32 v[40:41], v[40:41], v[60:61], v[60:61] op_sel:[0,0,1] op_sel_hi:[1,0,1]\n\t"
    "v_pk_fma_f32 v[42:43], v[42:43], v[60:61], v[60:61] op_sel:[0,0,1] op_sel_hi:[1,0,1]\n\t"
    "v_pk_fma_f32 v[44:45], v[44:45], v[60:61], v[60:61] op_sel:[0,0,1] op_sel_hi:[1,0,1]\n\t"
    "v_pk_fma_f32 v[46:47], v[46:47], v[60:61], v[60:61] op_sel:[0,0,1] op_sel_hi:[1,0,1]\n\t"
    "v_cvt_pk_bf16_f32 v76, v40, v41\n\t"
    "v_cvt_pk_bf16_f32 v77, v42, v43\n\t"
    "v_cvt_pk_bf16_f32 v78, v44, v45\n\t"
    "v_cvt_pk_bf16_f32 v79, v46, v47\n\t"
    "global_store_dwordx4 v211, v[76:79], s[48:49] sc1\n\t"
    "ds_read_b128 v[176:179], v15 offset:12288\n\t"
    "ds_read_b128 v[180:183], v15 offset:13312\n\t"
    "ds_read_b128 v[184:187], v15 offset:14336\n\t"
    "ds_read_b128 v[188:191], v15 offset:15360\n\t"
    "v_cvt_pk_f16_f32 v80, v16, v17\n\t"
    "v_cvt_pk_f16_f32 v81, v18, v19\n\t"
    "v_cvt_pk_f16_f32 v82, v20, v21\n\t"
    "v_cvt_pk_f16_f32 v83, v22, v23\n\t"
    "ds_write_b128 v97, v[80:83] offset:0\n\t"
    "v_cvt_pk_f16_f32 v84, v24, v25\n\t"
    "v_cvt_pk_f16_f32 v85, v26, v27\n\t"
    "v_cvt_pk_f16_f32 v86, v28, v29\n\t"
    "v_cvt_pk_f16_f32 v87, v30, v31\n\t"
    "ds_write_b128 v97, v[84:87] offset:128\n\t"
    "v_cvt_pk_f16_f32 v88, v32, v33\n\t"
    "v_cvt_pk_f16_f32 v89, v34, v35\n\t"
    "v_cvt_pk_f16_f32 v90, v36, v37\n\t"
    "v_cvt_pk_f16_f32 v91, v38, v39\n\t"
    "ds_write_b128 v97, v[88:91] offset:256\n\t"
    "v_cvt_pk_f16_f32 v92, v40, v41\n\t"
    "v_cvt_pk_f16_f32 v93, v42, v43\n\t"
    "v_cvt_pk_f16_f32 v94, v44, v45\n\t"
    "v_cvt_pk_f16_f32 v95, v46, v47\n\t"
    "ds_write_b128 v97, v[92:95] offset:384\n\t"
    "v_mov_b32 v254, s74\n\t"
    "v_mov_b32 v255, s75\n\t"
    "s_waitcnt lgkmcnt(0)\n\t"
    "s_barrier\n\t"
    "ds_read_b64_tr_b16 v[112:113], v100 offset:0\n\t"
    "ds_read_b64_tr_b16 v[114:115], v100 offset:2064\n\t"
    "ds_read_b64_tr_b16 v[116:117], v100 offset:512\n\t"
    "ds_read_b64_tr_b16 v[118:119], v100 offset:2576\n\t"
    "ds_read_b64_tr_b16 v[120:121], v100 offset:1024\n\t"
    "ds_read_b64_tr_b16 v[122:123], v100 offset:3088\n\t"
    "ds_read_b64_tr_b16 v[124:125], v100 offset:1536\n\t"
    "ds_read_b64_tr_b16 v[126:127], v100 offset:3600\n\t"
    "s_waitcnt lgkmcnt(6)\n\t"
    "v_perm_b32 v208, v114, v112, s60\n\t"
    "v_perm_b32 v209, v114, v112, s61\n\t"
    "v_perm_b32 v210, v115, v113, s60\n\t"
    "v_perm_b32 v211, v115, v113, s61\n\t"
    "s_nop 1\n\t"
    "v_mfma_f32_32x32x16_f16 v[0:15], v[208:211], a[224:227], 0\n\t"
    "v_lshrrev_b32 v88, 4, v245\n\t"
    "v_bfe_u32 v89, v245, 2, 2\n\t"
    "v_and_b32 v90, 48, v240\n\t"
    "v_lshl_add_u32 v243, v88, 11, v243\n\t"
    "s_waitcnt lgkmcnt(4)\n\t"
    "v_perm_b32 v212, v118, v116, s60\n\t"
    "v_perm_b32 v213, v118, v116, s61\n\t"
    "v_perm_b32 v214, v119, v117, s60\n\t"
    "v_mfma_f32_32x32x16_f16 v[16:31], v[208:211], a[240:243], 0\n\t"
    "v_add_u32 v244, 7168, v243\n\t"
    "v_lshlrev_b32 v107, 4, v88\n\t"
    "v_mul_u32_u24 v88, 2056, v88\n\t"
    "v_lshl_add_u32 v88, v89, 8, v88\n\t"
    "v_perm_b32 v215, v119, v117, s61\n\t"
    "s_nop 1\n\t"
    "v_mfma_f32_32x32x16_f16 v[0:15], v[212:215], a[228:231], v[0:15]\n\t"
    "v_add_u32 v88, v88, v90\n\t"
    "v_xor_b32 v90, 0, v89\n\t"
    "v_lshl_add_u32 v232, v90, 6, v88\n\t"
    "v_add_u32 v236, 57568, v232\n\t"
    "s_waitcnt lgkmcnt(2)\n\t"
    "v_perm_b32 v216, v122, v120, s60\n\t"
    "v_perm_b32 v217, v122, v120, s61\n\t"
    "v_perm_b32 v218, v123, v121, s60\n\t"
    "v_mfma_f32_32x32x16_f16 v[16:31], v[212:215], a[244:247], v[16:31]\n\t"
    "v_xor_b32 v90, 1, v89\n\t"
    "v_lshl_add_u32 v233, v90, 6, v88\n\t"
    "v_add_u32 v237, 57568, v233\n\t"
    "v_perm_b32 v219, v123, v121, s61\n\t"
    "s_nop 1\n\t"
    "v_mfma_f32_32x32x16_f16 v[0:15], v[216:219], a[232:235], v[0:15]\n\t"
    "s_waitcnt lgkmcnt(0)\n\t"
    "v_perm_b32 v220, v126, v124, s60\n\t"
    "v_perm_b32 v221, v126, v124, s61\n\t"
    "v_perm_b32 v222, v127, v125, s60\n\t"
    "v_mfma_f32_32x32x16_f16 v[16:31], v[216:219], a[248:251], v[16:31]\n\t"
    "v_perm_b32 v223, v127, v125, s61\n\t"
    "s_nop 1\n\t"
    "v_mfma_f32_32x32x16_f16 v[0:15], v[220:223], a[236:239], v[0:15]\n\t"
    "v_mfma_f32_32x32x16_f16 v[16:31], v[220:223], a[252:255], v[16:31]\n\t"
    "s_nop 11\n\t"
    "v_cvt_pk_f16_f32 v80, v0, v16\n\t"
    "v_cvt_pk_f16_f32 v81, v1, v17\n\t"
    "v_pk_mul_f16 v32, v80, v160 op_sel_hi:[1,0]\n\t"
    "v_pk_mul_f16 v33, v81, v161 op_sel_hi:[1,0]\n\t"
    "v_pk_fma_f16 v32, v80, v160, v32 op_sel:[1,1,0] op_sel_hi:[0,1,1] neg_hi:[1,0,0]\n\t"
    "v_pk_fma_f16 v33, v81, v161, v33 op_sel:[1,1,0] op_sel_hi:[0,1,1] neg_hi:[1,0,0]\n\t"
    "v_cvt_pk_f16_f32 v82, v2, v18\n\t"
    "v_cvt_pk_f16_f32 v83, v3, v19\n\t"
    "v_pk_mul_f16 v34, v82, v162 op_sel_hi:[1,0]\n\t"
    "v_pk_mul_f16 v35, v83, v163 op_sel_hi:[1,0]\n\t"
    "v_pk_fma_f16 v34, v82, v162, v34 op_sel:[1,1,0] op_sel_hi:[0,1,1] neg_hi:[1,0,0]\n\t"
    "v_pk_fma_f16 v35, v83, v163, v35 op_sel:[1,1,0] op_sel_hi:[0,1,1] neg_hi:[1,0,0]\n\t"
    "v_cvt_pk_f16_f32 v80, v4, v20\n\t"
    "v_cvt_pk_f16_f32 v81, v5, v21\n\t"
    "v_mfma_f32_32x32x16_f16 v[48:63], a[224:227], v[32:35], 0\n\t"
    "v_pk_mul_f16 v36, v80, v164 op_sel_hi:[1,0]\n\t"
    "v_pk_mul_f16 v37, v81, v165 op_sel_hi:[1,0]\n\t"
    "v_pk_fma_f16 v36, v80, v164, v36 op_sel:[1,1,0] op_sel_hi:[0,1,1] neg_hi:[1,0,0]\n\t"
    "v_pk_fma_f16 v37, v81, v165, v37 op_sel:[1,1,0] op_sel_hi:[0,1,1] neg_hi:[1,0,0]\n\t"
    "v_cvt_pk_f16_f32 v82, v6, v22\n\t"
    "v_cvt_pk_f16_f32 v83, v7, v23\n\t"
    "v_mfma_f32_32x32x16_f16 v[64:79], a[240:243], v[32:35], 0\n\t"
    "v_pk_mul_f16 v38, v82, v166 op_sel_hi:[1,0]\n\t"
    "v_pk_mul_f16 v39, v83, v167 op_sel_hi:[1,0]\n\t"
    "Lpad1_%=:\n\t"
    "s_cbranch_execz LretA_%=\n\t"
    "v_pk_fma_f16 v38, v82, v166, v38 op_sel:[1,1,0] op_sel_hi:[0,1,1] neg_hi:[1,0,0]\n\t"
    "v_pk_fma_f16 v39, v83, v167, v39 op_sel:[1,1,0] op_sel_hi:[0,1,1] neg_hi:[1,0,0]\n\t"
    "v_cvt_pk_f16_f32 v80, v8, v24\n\t"
    "v_cvt_pk_f16_f32 v81, v9, v25\n\t"
    "v_mfma_f32_32x32x16_f16 v[48:63], a[228:231], v[36:39], v[48:63]\n\t"
    "v_pk_mul_f16 v40, v80, v168 op_sel_hi:[1,0]\n\t"
    "v_pk_mul_f16 v41, v81, v169 op_sel_hi:[1,0]\n\t"
    "v_pk_fma_f16 v40, v80, v168, v40 op_sel:[1,1,0] op_sel_hi:[0,1,1] neg_hi:[1,0,0]\n\t"
    "v_pk_fma_f16 v41, v81, v169, v41 op_sel:[1,1,0] op_sel_hi:[0,1,1] neg_hi:[1,0,0]\n\t"
    "v_cvt_pk_f16_f32 v82, v10, v26\n\t"
    "v_cvt_pk_f16_f32 v83, v11, v27\n\t"
    "v_mfma_f32_32x32x16_f16 v[64:79], a[244:247], v[36:39], v[64:79]\n\t"
    "v_pk_mul_f16 v42, v82, v170 op_sel_hi:[1,0]\n\t"
    "v_pk_mul_f16 v43, v83, v171 op_sel_hi:[1,0]\n\t"
    "v_pk_fma_f16 v42, v82, v170, v42 op_sel:[1,1,0] op_sel_hi:[0,1,1] neg_hi:[1,0,0]\n\t"
    "v_pk_fma_f16 v43, v83, v171, v43 op_sel:[1,1,0] op_sel_hi:[0,1,1] neg_hi:[1,0,0]\n\t"
    "v_cvt_pk_f16_f32 v80, v12, v28\n\t"
    "v_cvt_pk_f16_f32 v81, v13, v29\n\t"
    "v_mfma_f32_32x32x16_f16 v[48:63], a[232:235], v[40:43], v[48:63]\n\t"
    "v_pk_mul_f16 v44, v80, v172 op_sel_hi:[1,0]\n\t"
    "v_pk_mul_f16 v45, v81, v173 op_sel_hi:[1,0]\n\t"
    "v_pk_fma_f16 v44, v80, v172, v44 op_sel:[1,1,0] op_sel_hi:[0,1,1] neg_hi:[1,0,0]\n\t"
    "v_pk_fma_f16 v45, v81, v173, v45 op_sel:[1,1,0] op_sel_hi:[0,1,1] neg_hi:[1,0,0]\n\t"
    "v_cvt_pk_f16_f32 v82, v14, v30\n\t"
    "v_cvt_pk_f16_f32 v83, v15, v31\n\t"
    "v_mfma_f32_32x32x16_f16 v[64:79], a[248:251], v[40:43], v[64:79]\n\t"
    "v_pk_mul_f16 v46, v82, v174 op_sel_hi:[1,0]\n\t"
    "v_pk_mul_f16 v47, v83, v175 op_sel_hi:[1,0]\n\t"
    "v_pk_fma_f16 v46, v82, v174, v46 op_sel:[1,1,0] op_sel_hi:[0,1,1] neg_hi:[1,0,0]\n\t"
    "v_pk_fma_f16 v47, v83, v175, v47 op_sel:[1,1,0] op_sel_hi:[0,1,1] neg_hi:[1,0,0]\n\t"
    "s_nop 1\n\t"
    "v_mfma_f32_32x32x16_f16 v[48:63], a[236:239], v[44:47], v[48:63]\n\t"
    "v_xor_b32 v90, 2, v89\n\t"
    "v_lshl_add_u32 v234, v90, 6, v88\n\t"
    "v_add_u32 v238, 57568, v234\n\t"
    "v_xor_b32 v90, 3, v89\n\t"
    "v_lshl_add_u32 v235, v90, 6, v88\n\t"
    "v_add_u32 v239, 57568, v235\n\t"
    "v_mfma_f32_32x32x16_f16 v[64:79], a[252:255], v[44:47], v[64:79]\n\t"
    "s_and_b32 s38, s27, 15\n\t"
    "s_lshl_b32 s38, s38, 18\n\t"
    "s_lshr_b32 s39, s38, 5\n\t"
    "s_xor_b32 s39, s39, s30\n\t"
    "s_and_b32 s39, s39, 0x2000\n\t"
    "s_or_b32 s38, s38, s39\n\t"
    "s_add_u32 s38, s38, 0x400000\n\t"
    "s_add_u32 s64, s20, s38\n\t"
    "s_addc_u32 s65, s21, 0\n\t"
    "s_and_b32 s65, s65, 0xffff\n\t"
    "s_mov_b32 s66, 0x3e000\n\t"
    "s_mov_b32 s67, 0x20000\n\t"
    "v_cvt_pk_f16_f32 v80, v48, v64\n\t"
    "ds_write_b32 v101, v80 offset:0\n\t"
    "v_cvt_pk_f16_f32 v81, v49, v65\n\t"
    "ds_write_b32 v101, v81 offset:256\n\t"
    "v_cvt_pk_f16_f32 v82, v50, v66\n\t"
    "ds_write_b32 v101, v82 offset:512\n\t"
    "v_cvt_pk_f16_f32 v83, v51, v67\n\t"
    "ds_write_b32 v101, v83 offset:768\n\t"
    "v_cvt_pk_f16_f32 v84, v52, v68\n\t"
    "ds_write_b32 v101, v84 offset:2048\n\t"
    "v_cvt_pk_f16_f32 v85, v53, v69\n\t"
    "ds_write_b32 v101, v85 offset:2304\n\t"
    "v_cvt_pk_f16_f32 v86, v54, v70\n\t"
    "ds_write_b32 v101, v86 offset:2560\n\t"
    "v_cvt_pk_f16_f32 v87, v55, v71\n\t"
    "ds_write_b32 v101, v87 offset:2816\n\t"
    "v_cvt_pk_f16_f32 v88, v72, v56\n\t"
    "ds_write_b32 v103, v88\n\t"
    "v_cvt_pk_f16_f32 v89, v73, v57\n\t"
    "ds_write_b32 v102, v89 offset:2560\n\t"
    "v_cvt_pk_f16_f32 v90, v74, v58\n\t"
    "ds_write_b32 v102, v90 offset:2304\n\t"
    "v_cvt_pk_f16_f32 v91, v75, v59\n\t"
    "ds_write_b32 v102, v91 offset:2048\n\t"
    "v_cvt_pk_f16_f32 v92, v76, v60\n\t"
    "ds_write_b32 v102, v92 offset:768\n\t"
    "v_cvt_pk_f16_f32 v93, v77, v61\n\t"
    "ds_write_b32 v102, v93 offset:512\n\t"
    "v_cvt_pk_f16_f32 v94, v78, v62\n\t"
    "ds_write_b32 v102, v94 offset:256\n\t"
    "v_cvt_pk_f16_f32 v95, v79, v63\n\t"
    "ds_write_b32 v102, v95 offset:0\n\t"
    "s_waitcnt lgkmcnt(0)\n\t"
    "s_barrier\n\t"
    "ds_read_b128 v[208:211], v104 offset:0\n\t"
    "ds_read_b128 v[212:215], v104 offset:256\n\t"
    "ds_read_b128 v[216:219], v104 offset:512\n\t"
    "ds_read_b128 v[220:223], v104 offset:768\n\t"
    "s_waitcnt lgkmcnt(3)\n\t"
    "global_store_dwordx4 v231, v[208:211], s[50:51] offset:-4096 sc1\n\t"
    "s_waitcnt lgkmcnt(2)\n\t"
    "global_store_dwordx4 v231, v[212:215], s[50:51] offset:-2048 sc1\n\t"
    "s_waitcnt lgkmcnt(1)\n\t"
    "global_store_dwordx4 v231, v[216:219], s[50:51] offset:0 sc1\n\t"
    "s_waitcnt lgkmcnt(0)\n\t"
    "global_store_dwordx4 v231, v[220:223], s[50:51] offset:2048 sc1\n\t"
    "s_waitcnt vmcnt(4)\n\t"
    "s_barrier\n\t"
    "s_mov_b64 exec, s[58:59]\n\t"
    "global_store_dwordx2 v248, v[254:255], s[4:5] offset:192 sc1\n\t"
    "s_mov_b64 exec, -1\n\t"
    "v_perm_b32 v144, v132, v128, s60\n\t"
    "v_perm_b32 v145, v140, v136, s60\n\t"
    "v_perm_b32 v146, v132, v128, s61\n\t"
    "v_perm_b32 v147, v140, v136, s61\n\t"
    "ds_write_b128 v228, v[144:147] offset:0\n\t"
    "v_perm_b32 v148, v133, v129, s60\n\t"
    "v_perm_b32 v149, v141, v137, s60\n\t"
    "v_perm_b32 v150, v133, v129, s61\n\t"
    "v_perm_b32 v151, v141, v137, s61\n\t"
    "ds_write_b128 v228, v[148:151] offset:1024\n\t"
    "v_perm_b32 v152, v134, v130, s60\n\t"
    "v_perm_b32 v153, v142, v138, s60\n\t"
    "v_perm_b32 v154, v134, v130, s61\n\t"
    "v_perm_b32 v155, v142, v138, s61\n\t"
    "ds_write_b128 v228, v[152:155] offset:2048\n\t"
    "v_perm_b32 v156, v135, v131, s60\n\t"
    "v_perm_b32 v157, v143, v139, s60\n\t"
    "v_perm_b32 v158, v135, v131, s61\n\t"
    "v_perm_b32 v159, v143, v139, s61\n\t"
    "ds_write_b128 v228, v[156:159] offset:3072\n\t"
    "ds_read_b128 v[144:147], v227 offset:0\n\t"
    "ds_read_b128 v[148:151], v227 offset:1024\n\t"
    "ds_read_b128 v[152:155], v227 offset:2048\n\t"
    "ds_read_b128 v[156:159], v227 offset:3072\n\t"
    "v_mov_b32 v228, v111\n\t"
    "s_waitcnt lgkmcnt(3)\n\t"
    "global_store_dwordx4 v230, v[144:147], s[50:51] offset:-4032 sc1\n\t"
    "s_waitcnt lgkmcnt(2)\n\t"
    "global_store_dwordx4 v230, v[148:151], s[50:51] offset:-3904 sc1\n\t"
    "s_waitcnt lgkmcnt(1)\n\t"
    "global_store_dwordx4 v230, v[152:155], s[50:51] offset:-3776 sc1\n\t"
    "s_waitcnt lgkmcnt(0)\n\t"
    "global_store_dwordx4 v230, v[156:159], s[50:51] offset:-3648 sc1\n\t"
    "global_store_short v229, v228, s[94:95] sc1\n\t"
    "v_and_b32 v249, 7, v245\n\t"
    "s_and_b32 s38, s27, 3\n\t"
    "s_lshl_b32 s38, s38, 6\n\t"
    "s_lshr_b32 s39, s27, 2\n\t"
    "s_add_u32 s38, s38, s39\n\t"
    "s_lshl_b32 s38, s38, 11\n\t"
    "s_add_u32 s38, s38, 262336\n\t"
    "v_lshl_add_u32 v249, v249, 14, s38\n\t"
    "s_cmp_eq_u32 s26, 7\n\t"
    "s_cbranch_scc1 Lspare_%=\n\t"
    "v_mov_b64 v[250:251], 0\n\t"
    "s_mov_b64 exec, 0xff\n\t"
    "global_atomic_or_x2 v[250:251], v249, v[250:251], s[20:21] sc0 sc1\n\t"
    "s_mov_b64 exec, -1\n\t"
    "global_load_dwordx4 v[0:3], v107, s[12:13] offset:0\n\t"
    "global_load_dwordx4 v[4:7], v107, s[12:13] offset:64\n\t"
    "global_load_dwordx4 v[8:11], v107, s[12:13] offset:128\n\t"
    "global_load_dwordx4 v[12:15], v107, s[12:13] offset:192\n\t"
    "s_add_u32 m0, s31, 16448\n\t"
    "s_mov_b32 s35, 458752\n\t"
    "buffer_load_dwordx4 v241, s[68:71], s35 offen sc1 lds\n\t"
    "buffer_load_dwordx4 v242, s[68:71], s35 offen offset:1024 sc1 lds\n\t"
    "v_mov_b32 v110, v111\n\t"
    "v_pk_maximum3_f16 v110, v128, v129, v110\n\t"
    "v_pk_minimum3_f16 v111, v128, v129, v111\n\t"
    "v_pk_maximum3_f16 v110, v130, v131, v110\n\t"
    "v_pk_minimum3_f16 v111, v130, v131, v111\n\t"
    "v_pk_maximum3_f16 v110, v132, v133, v110\n\t"
    "v_pk_minimum3_f16 v111, v132, v133, v111\n\t"
    "v_pk_maximum3_f16 v110, v134, v135, v110\n\t"
    "v_pk_minimum3_f16 v111, v134, v135, v111\n\t"
    "v_pk_maximum3_f16 v110, v136, v137, v110\n\t"
    "v_pk_minimum3_f16 v111, v136, v137, v111\n\t"
    "v_pk_maximum3_f16 v110, v138, v139, v110\n\t"
    "v_pk_minimum3_f16 v111, v138, v139, v111\n\t"
    "v_pk_maximum3_f16 v110, v140, v141, v110\n\t"
    "v_pk_minimum3_f16 v111, v140, v141, v111\n\t"
    "v_pk_maximum3_f16 v110, v142, v143, v110\n\t"
    "v_pk_minimum3_f16 v111, v142, v143, v111\n\t"
    "v_and_b32 v110, 0x7fff7fff, v110\n\t"
    "v_and_b32 v111, 0x7fff7fff, v111\n\t"
    "v_pk_max_u16 v110, v110, v111\n\t"
    "v_lshrrev_b32 v111, 16, v110\n\t"
    "v_and_b32 v110, 0xffff, v110\n\t"
    "v_max_u32 v110, v110, v111\n\t"
    "s_nop 1\n\t"
    "v_max_u32_dpp v110, v110, v110 quad_perm:[1,0,3,2] row_mask:0xf bank_mask:0xf\n\t"
    "s_nop 1\n\t"
    "v_max_u32_dpp v110, v110, v110 quad_perm:[2,3,0,1] row_mask:0xf bank_mask:0xf\n\t"
    "s_nop 1\n\t"
    "v_max_u32_dpp v110, v110, v110 row_half_mirror row_mask:0xf bank_mask:0xf\n\t"
    "s_nop 1\n\t"
    "v_max_u32_dpp v110, v110, v110 row_mirror row_mask:0xf bank_mask:0xf\n\t"
    "v_mov_b32 v111, v110\n\t"
    "s_nop 1\n\t"
    "v_permlane16_swap_b32 v110, v111\n\t"
    "v_max_u32 v110, v110, v111\n\t"
    "v_mov_b32 v111, v110\n\t"
    "s_nop 1\n\t"
    "v_permlane32_swap_b32 v110, v111\n\t"
    "v_max_u32 v110, v110, v111\n\t"
    "v_lshrrev_b32 v110, 7, v110\n\t"
    "s_lshl_b32 s38, s22, 2\n\t"
    "s_add_u32 s38, s38, 152320\n\t"
    "v_mov_b32 v111, s38\n\t"
    "ds_write_b32 v111, v110\n\t"
    "s_waitcnt lgkmcnt(0)\n\t"
    "ds_read_b64_tr_b16 a[192:193], v232 offset:0\n\t"
    "ds_read_b64_tr_b16 a[194:195], v232 offset:1024\n\t"
    "ds_read_b64_tr_b16 a[196:197], v232 offset:8\n\t"
    "ds_read_b64_tr_b16 a[198:199], v232 offset:1032\n\t"
    "ds_read_b64_tr_b16 a[200:201], v233 offset:0\n\t"
    "ds_read_b64_tr_b16 a[202:203], v233 offset:1024\n\t"
    "ds_read_b64_tr_b16 a[204:205], v233 offset:8\n\t"
    "ds_read_b64_tr_b16 a[206:207], v233 offset:1032\n\t"
    "ds_read_b64_tr_b16 a[208:209], v234 offset:0\n\t"
    "ds_read_b64_tr_b16 a[210:211], v234 offset:1024\n\t"
    "ds_read_b64_tr_b16 a[212:213], v234 offset:8\n\t"
    "ds_read_b64_tr_b16 a[214:215], v234 offset:1032\n\t"
    "ds_read_b64_tr_b16 a[216:217], v235 offset:0\n\t"
    "ds_read_b64_tr_b16 a[218:219], v235 offset:1024\n\t"
    "ds_read_b64_tr_b16 a[220:221], v235 offset:8\n\t"
    "s_mov_b64 exec, 0\n\t"
    "s_cmp_eq_u32 s22, 0\n\t"
    "s_cbranch_scc1 Lpad5_%=\n\t"
    "s_cmp_eq_u32 s22, 1\n\t"
    "s_cbranch_scc1 Lpad6_%=\n\t"
    "s_cmp_eq_u32 s22, 2\n\t"
    "s_cbranch_scc1 Lpad7_%=\n\t"
    "s_branch Lpad8_%=\n\t"
    "LretB_%=:\n\t"
    "s_mov_b64 exec, -1\n\t"
    "s_cmp_eq_u32 s26, 7\n\t"
    "s_cbranch_scc1 Lspare2_%=\n\t"
    "s_waitcnt vmcnt(6)\n\t"
    "v_cmp_ne_u64 vcc, s[74:75], v[250:251]\n\t"
    "s_nop 1\n\t"
    "s_and_b64 vcc, vcc, 0xff\n\t"
    "s_cbranch_scc1 Ls1_%=\n\t"
    "Lb2_%=:\n\t"
    "s_mov_b32 s34, 0\n\t"
    "buffer_load_dwordx4 a[0:3], v240, s[64:67], s34 offen offset:0 sc1\n\t"
    "buffer_load_dwordx4 a[4:7], v240, s[64:67], s34 offen offset:1024 sc1\n\t"
    "buffer_load_dwordx4 a[8:11], v240, s[64:67], s34 offen offset:2048 sc1\n\t"
    "buffer_load_dwordx4 a[12:15], v240, s[64:67], s34 offen offset:3072 sc1\n\t"
    "s_mov_b32 s34, 4096\n\t"
    "buffer_load_dwordx4 a[16:19], v240, s[64:67], s34 offen offset:0 sc1\n\t"
    "buffer_load_dwordx4 a[20:23], v240, s[64:67], s34 offen offset:1024 sc1\n\t"
    "buffer_load_dwordx4 a[24:27], v240, s[64:67], s34 offen offset:2048 sc1\n\t"
    "buffer_load_dwordx4 a[28:31], v240, s[64:67], s34 offen offset:3072 sc1\n\t"
    "s_mov_b32 s34, 16384\n\t"
    "buffer_load_dwordx4 a[32:35], v240, s[64:67], s34 offen offset:0 sc1\n\t"
    "buffer_load_dwordx4 a[36:39], v240, s[64:67], s34 offen offset:1024 sc1\n\t"
    "buffer_load_dwordx4 a[40:43], v240, s[64:67], s34 offen offset:2048 sc1\n\t"
    "buffer_load_dwordx4 a[44:47], v240, s[64:67], s34 offen offset:3072 sc1\n\t"
    "s_waitcnt vmcnt(17)\n\t"
    "v_lshlrev_b32 v24, 16, v0\n\t"
    "v_and_b32 v25, 0xffff0000, v0\n\t"
    "v_lshlrev_b32 v26, 16, v1\n\t"
    "v_and_b32 v27, 0xffff0000, v1\n\t"
    "v_lshlrev_b32 v28, 16, v2\n\t"
    "v_and_b32 v29, 0xffff0000, v2\n\t"
    "v_lshlrev_b32 v30, 16, v3\n\t"
    "v_and_b32 v31, 0xffff0000, v3\n\t"
    "s_waitcnt vmcnt(16)\n\t"
    "v_lshlrev_b32 v56, 16, v4\n\t"
    "v_and_b32 v57, 0xffff0000, v4\n\t"
    "v_lshlrev_b32 v58, 16, v5\n\t"
    "v_and_b32 v59, 0xffff0000, v5\n\t"
    "v_lshlrev_b32 v60, 16, v6\n\t"
    "v_and_b32 v61, 0xffff0000, v6\n\t"
    "v_lshlrev_b32 v62, 16, v7\n\t"
    "v_and_b32 v63, 0xffff0000, v7\n\t"
    "s_waitcnt vmcnt(15)\n\t"
    "v_lshlrev_b32 v88, 16, v8\n\t"
    "v_and_b32 v89, 0xffff0000, v8\n\t"
    "v_lshlrev_b32 v90, 16, v9\n\t"
    "v_and_b32 v91, 0xffff0000, v9\n\t"
    "v_lshlrev_b32 v92, 16, v10\n\t"
    "v_and_b32 v93, 0xffff0000, v10\n\t"
    "v_lshlrev_b32 v94, 16, v11\n\t"
    "v_and_b32 v95, 0xffff0000, v11\n\t"
    "s_waitcnt vmcnt(14)\n\t"
    "v_lshlrev_b32 v120, 16, v12\n\t"
    "v_and_b32 v121, 0xffff0000, v12\n\t"
    "v_lshlrev_b32 v122, 16, v13\n\t"
    "v_and_b32 v123, 0xffff0000, v13\n\t"
    "v_lshlrev_b32 v124, 16, v14\n\t"
    "v_and_b32 v125, 0xffff0000, v14\n\t"
    "v_lshlrev_b32 v126, 16, v15\n\t"
    "v_and_b32 v127, 0xffff0000, v15\n\t"
    "s_waitcnt vmcnt(12)\n\t"
    "s_waitcnt vmcnt(11) lgkmcnt(13)\n\t"
    "v_mfma_f32_16x16x32_bf16 v[0:3], a[192:195], a[0:3], v[24:27]\n\t"
    "s_barrier\n\t"
    "s_waitcnt vmcnt(10)\n\t"
    "v_mfma_f32_16x16x32_bf16 v[8:11], a[192:195], a[4:7], v[24:27]\n\t"
    "s_add_u32 m0, s31, 24672\n\t"
    "ds_read_b64_tr_b16 a[222:223], v235 offset:1032\n\t"
    "s_waitcnt vmcnt(9)\n\t"
    "v_mfma_f32_16x16x32_bf16 v[16:19], a[192:195], a[8:11], v[24:27]\n\t"
    "s_mov_b32 s35, 688128\n\t"
    "buffer_load_dwordx4 v241, s[68:71], s35 offen sc1 lds\n\t"
    "s_waitcnt vmcnt(9)\n\t"
    "v_mfma_f32_16x16x32_bf16 v[24:27], a[192:195], a[12:15], v[24:27]\n\t"
    "s_waitcnt lgkmcnt(12)\n\t"
    "ds_read_b64_tr_b16 a[192:193], v232 offset:8224\n\t"
    "v_mfma_f32_16x16x32_bf16 v[4:7], a[196:199], a[0:3], v[28:31]\n\t"
    "ds_read_b64_tr_b16 a[194:195], v232 offset:9248\n\t"
    "v_mov_b32 v128, 152320\n\t"
    "v_mfma_f32_16x16x32_bf16 v[12:15], a[196:199], a[4:7], v[28:31]\n\t"
    "ds_read_b128 v[128:131], v128\n\t"
    "v_mfma_f32_16x16x32_bf16 v[20:23], a[196:199], a[8:11], v[28:31]\n\t"
    "buffer_load_dwordx4 v242, s[68:71], s35 offen offset:1024 sc1 lds\n\t"
    "v_mfma_f32_16x16x32_bf16 v[28:31], a[196:199], a[12:15], v[28:31]\n\t"
    "s_waitcnt lgkmcnt(13)\n\t"
    "ds_read_b64_tr_b16 a[196:197], v232 offset:8232\n\t"
    "v_mfma_f32_16x16x32_bf16 v[32:35], a[200:203], a[0:3], v[56:59]\n\t"
    "ds_read_b64_tr_b16 a[198:199], v232 offset:9256\n\t"
    "v_mfma_f32_16x16x32_bf16 v[40:43], a[200:203], a[4:7], v[56:59]\n\t"
    "s_add_u32 m0, s31, 32896\n\t"
    "v_mfma_f32_16x16x32_bf16 v[48:51], a[200:203], a[8:11], v[56:59]\n\t"
    "s_mov_b32 s35, 917504\n\t"
    "buffer_load_dwordx4 v241, s[68:71], s35 offen sc1 lds\n\t"
    "v_mfma_f32_16x16x32_bf16 v[56:59], a[200:203], a[12:15], v[56:59]\n\t"
    "s_waitcnt lgkmcnt(13)\n\t"
    "ds_read_b64_tr_b16 a[200:201], v233 offset:8224\n\t"
    "v_mfma_f32_16x16x32_bf16 v[36:39], a[204:207], a[0:3], v[60:63]\n\t"
    "ds_read_b64_tr_b16 a[202:203], v233 offset:9248\n\t"
    "s_waitcnt lgkmcnt(4)\n\t"
    "v_max3_u32 v128, v128, v129, v130\n\t"
    "v_mfma_f32_16x16x32_bf16 v[44:47], a[204:207], a[4:7], v[60:63]\n\t"
    "v_max_u32 v128, v128, v131\n\t"
    "v_mfma_f32_16x16x32_bf16 v[52:55], a[204:207], a[8:11], v[60:63]\n\t"
    "v_mfma_f32_16x16x32_bf16 v[60:63], a[204:207], a[12:15], v[60:63]\n\t"
    "ds_read_b64_tr_b16 a[204:205], v233 offset:8232\n\t"
    "v_mfma_f32_16x16x32_bf16 v[64:67], a[208:211], a[0:3], v[88:91]\n\t"
    "ds_read_b64_tr_b16 a[206:207], v233 offset:9256\n\t"
    "v_mfma_f32_16x16x32_bf16 v[72:75], a[208:211], a[4:7], v[88:91]\n\t"
    "v_or_b32 v128, v128, v254\n\t"
    "v_mfma_f32_16x16x32_bf16 v[80:83], a[208:211], a[8:11], v[88:91]\n\t"
    "buffer_load_dwordx4 v242, s[68:71], s35 offen offset:1024 sc1 lds\n\t"
    "v_mfma_f32_16x16x32_bf16 v[88:91], a[208:211], a[12:15], v[88:91]\n\t"
    "ds_read_b64_tr_b16 a[208:209], v234 offset:8224\n\t"
    "v_mfma_f32_16x16x32_bf16 v[68:71], a[212:215], a[0:3], v[92:95]\n\t"
    "ds_read_b64_tr_b16 a[210:211], v234 offset:9248\n\t"
    "v_mfma_f32_16x16x32_bf16 v[76:79], a[212:215], a[4:7], v[92:95]\n\t"
    "v_mov_b32 v129, v255\n\t"
    "v_mfma_f32_16x16x32_bf16 v[84:87], a[212:215], a[8:11], v[92:95]\n\t"
    "s_mov_b32 s34, 20480\n\t"
    "buffer_load_dwordx4 a[48:51], v240, s[64:67], s34 offen offset:0 sc1\n\t"
    "v_mfma_f32_16x16x32_bf16 v[92:95], a[212:215], a[12:15], v[92:95]\n\t"
    "ds_read_b64_tr_b16 a[212:213], v234 offset:8232\n\t"
    "v_mfma_f32_16x16x32_bf16 v[96:99], a[216:219], a[0:3], v[120:123]\n\t"
    "ds_read_b64_tr_b16 a[214:215], v234 offset:9256\n\t"
    "v_mfma_f32_16x16x32_bf16 v[104:107], a[216:219], a[4:7], v[120:123]\n\t"
    "s_mov_b64 exec, s[58:59]\n\t"
    "global_store_dwordx2 v248, v[128:129], s[42:43] offset:0 sc1\n\t"
    "s_mov_b64 exec, -1\n\t"
    "v_mfma_f32_16x16x32_bf16 v[112:115], a[216:219], a[8:11], v[120:123]\n\t"
    "buffer_load_dwordx4 a[52:55], v240, s[64:67], s34 offen offset:1024 sc1\n\t"
    "v_mfma_f32_16x16x32_bf16 v[120:123], a[216:219], a[12:15], v[120:123]\n\t"
    "ds_read_b64_tr_b16 a[216:217], v235 offset:8224\n\t"
    "v_mfma_f32_16x16x32_bf16 v[100:103], a[220:223], a[0:3], v[124:127]\n\t"
    "ds_read_b64_tr_b16 a[218:219], v235 offset:9248\n\t"
    "v_mfma_f32_16x16x32_bf16 v[108:111], a[220:223], a[4:7], v[124:127]\n\t"
    "v_mfma_f32_16x16x32_bf16 v[116:119], a[220:223], a[8:11], v[124:127]\n\t"
    "buffer_load_dwordx4 a[56:59], v240, s[64:67], s34 offen offset:2048 sc1\n\t"
    "v_mfma_f32_16x16x32_bf16 v[124:127], a[220:223], a[12:15], v[124:127]\n\t"
    "ds_read_b64_tr_b16 a[220:221], v235 offset:8232\n\t"
    "s_waitcnt vmcnt(14)\n\t"
    "v_mfma_f32_16x16x32_bf16 v[0:3], a[192:195], a[16:19], v[0:3]\n\t"
    "ds_read_b64_tr_b16 a[222:223], v235 offset:9256\n\t"
    "s_waitcnt vmcnt(13)\n\t"
    "v_mfma_f32_16x16x32_bf16 v[8:11], a[192:195], a[20:23], v[8:11]\n\t"
    "s_waitcnt vmcnt(12)\n\t"
    "v_mfma_f32_16x16x32_bf16 v[16:19], a[192:195], a[24:27], v[16:19]\n\t"
    "buffer_load_dwordx4 a[60:63], v240, s[64:67], s34 offen offset:3072 sc1\n\t"
    "s_waitcnt vmcnt(12)\n\t"
    "v_mfma_f32_16x16x32_bf16 v[24:27], a[192:195], a[28:31], v[24:27]\n\t"
    "s_waitcnt lgkmcnt(12)\n\t"
    "ds_read_b64_tr_b16 a[192:193], v232 offset:16448\n\t"
    "v_mfma_f32_16x16x32_bf16 v[28:31], a[196:199], a[28:31], v[28:31]\n\t"
    "ds_read_b64_tr_b16 a[194:195], v232 offset:17472\n\t"
    "v_mfma_f32_16x16x32_bf16 v[20:23], a[196:199], a[24:27], v[20:23]\n\t"
    "s_add_u32 m0, s31, 41120\n\t"
    "v_mfma_f32_16x16x32_bf16 v[12:15], a[196:199], a[20:23], v[12:15]\n\t"
    "s_mov_b32 s35, 1146880\n\t"
    "buffer_load_dwordx4 v241, s[68:71], s35 offen sc1 lds\n\t"
    "v_mfma_f32_16x16x32_bf16 v[4:7], a[196:199], a[16:19], v[4:7]\n\t"
    "s_waitcnt lgkmcnt(12)\n\t"
    "ds_read_b64_tr_b16 a[196:197], v232 offset:16456\n\t"
    "v_mfma_f32_16x16x32_bf16 v[32:35], a[200:203], a[16:19], v[32:35]\n\t"
    "ds_read_b64_tr_b16 a[198:199], v232 offset:17480\n\t"
    "v_mfma_f32_16x16x32_bf16 v[40:43], a[200:203], a[20:23], v[40:43]\n\t"
    "v_mfma_f32_16x16x32_bf16 v[48:51], a[200:203], a[24:27], v[48:51]\n\t"
    "buffer_load_dwordx4 v242, s[68:71], s35 offen offset:1024 sc1 lds\n\t"
    "v_mfma_f32_16x16x32_bf16 v[56:59], a[200:203], a[28:31], v[56:59]\n\t"
    "s_waitcnt lgkmcnt(12)\n\t"
    "ds_read_b64_tr_b16 a[200:201], v233 offset:16448\n\t"
    "v_mfma_f32_16x16x32_bf16 v[60:63], a[204:207], a[28:31], v[60:63]\n\t"
    "ds_read_b64_tr_b16 a[202:203], v233 offset:17472\n\t"
    "v_mfma_f32_16x16x32_bf16 v[52:55], a[204:207], a[24:27], v[52:55]\n\t"
    "s_add_u32 m0, s31, 49344\n\t"
    "v_mfma_f32_16x16x32_bf16 v[44:47], a[204:207], a[20:23], v[44:47]\n\t"
    "s_mov_b32 s35, 1376256\n\t"
    "buffer_load_dwordx4 v241, s[68:71], s35 offen sc1 lds\n\t"
    "v_mfma_f32_16x16x32_bf16 v[36:39], a[204:207], a[16:19], v[36:39]\n\t"
    "s_waitcnt lgkmcnt(12)\n\t"
    "ds_read_b64_tr_b16 a[204:205], v233 offset:16456\n\t"
    "v_mfma_f32_16x16x32_bf16 v[64:67], a[208:211], a[16:19], v[64:67]\n\t"
    "ds_read_b64_tr_b16 a[206:207], v233 offset:17480\n\t"
    "v_mfma_f32_16x16x32_bf16 v[72:75], a[208:211], a[20:23], v[72:75]\n\t"
    "v_mfma_f32_16x16x32_bf16 v[80:83], a[208:211], a[24:27], v[80:83]\n\t"
    "buffer_load_dwordx4 v242, s[68:71], s35 offen offset:1024 sc1 lds\n\t"
    "v_mfma_f32_16x16x32_bf16 v[88:91], a[208:211], a[28:31], v[88:91]\n\t"
    "s_waitcnt lgkmcnt(12)\n\t"
    "ds_read_b64_tr_b16 a[208:209], v234 offset:16448\n\t"
    "v_mfma_f32_16x16x32_bf16 v[92:95], a[212:215], a[28:31], v[92:95]\n\t"
    "ds_read_b64_tr_b16 a[210:211], v234 offset:17472\n\t"
    "v_mfma_f32_16x16x32_bf16 v[84:87], a[212:215], a[24:27], v[84:87]\n\t"
    "v_mfma_f32_16x16x32_bf16 v[76:79], a[212:215], a[20:23], v[76:79]\n\t"
    "s_mov_b32 s34, 32768\n\t"
    "buffer_load_dwordx4 a[64:67], v240, s[64:67], s34 offen offset:0 sc1\n\t"
    "v_mfma_f32_16x16x32_bf16 v[68:71], a[212:215], a[16:19], v[68:71]\n\t"
    "s_waitcnt lgkmcnt(12)\n\t"
    "ds_read_b64_tr_b16 a[212:213], v234 offset:16456\n\t"
    "v_mfma_f32_16x16x32_bf16 v[96:99], a[216:219], a[16:19], v[96:99]\n\t"
    "ds_read_b64_tr_b16 a[214:215], v234 offset:17480\n\t"
    "v_mfma_f32_16x16x32_bf16 v[104:107], a[216:219], a[20:23], v[104:107]\n\t"
    "v_mfma_f32_16x16x32_bf16 v[112:115], a[216:219], a[24:27], v[112:115]\n\t"
    "buffer_load_dwordx4 a[68:71], v240, s[64:67], s34 offen offset:1024 sc1\n\t"
    "v_mfma_f32_16x16x32_bf16 v[120:123], a[216:219], a[28:31], v[120:123]\n\t"
    "s_waitcnt lgkmcnt(12)\n\t"
    "ds_read_b64_tr_b16 a[216:217], v235 offset:16448\n\t"
    "v_mfma_f32_16x16x32_bf16 v[124:127], a[220:223], a[28:31], v[124:127]\n\t"
    "ds_read_b64_tr_b16 a[218:219], v235 offset:17472\n\t"
    "v_mfma_f32_16x16x32_bf16 v[116:119], a[220:223], a[24:27], v[116:119]\n\t"
    "v_mfma_f32_16x16x32_bf16 v[108:111], a[220:223], a[20:23], v[108:111]\n\t"
    "buffer_load_dwordx4 a[72:75], v240, s[64:67], s34 offen offset:2048 sc1\n\t"
    "v_mfma_f32_16x16x32_bf16 v[100:103], a[220:223], a[16:19], v[100:103]\n\t"
    "s_waitcnt vmcnt(11) lgkmcnt(12)\n\t"
    "ds_read_b64_tr_b16 a[220:221], v235 offset:16456\n\t"
    "v_mfma_f32_16x16x32_bf16 v[0:3], a[192:195], a[32:35], v[0:3]\n\t"
    "s_barrier\n\t"
    "v_mfma_f32_16x16x32_bf16 v[8:11], a[192:195], a[36:39], v[8:11]\n\t"
    "ds_read_b64_tr_b16 a[222:223], v235 offset:17480\n\t"
    "v_mfma_f32_16x16x32_bf16 v[16:19], a[192:195], a[40:43], v[16:19]\n\t"
    "buffer_load_dwordx4 a[76:79], v240, s[64:67], s34 offen offset:3072 sc1\n\t"
    "v_mfma_f32_16x16x32_bf16 v[24:27], a[192:195], a[44:47], v[24:27]\n\t"
    "s_waitcnt lgkmcnt(12)\n\t"
    "ds_read_b64_tr_b16 a[192:193], v232 offset:24672\n\t"
    "v_mfma_f32_16x16x32_bf16 v[28:31], a[196:199], a[44:47], v[28:31]\n\t"
    "ds_read_b64_tr_b16 a[194:195], v232 offset:25696\n\t"
    "v_mfma_f32_16x16x32_bf16 v[20:23], a[196:199], a[40:43], v[20:23]\n\t"
    "v_mfma_f32_16x16x32_bf16 v[12:15], a[196:199], a[36:39], v[12:15]\n\t"
    "s_mov_b32 s34, 36864\n\t"
    "buffer_load_dwordx4 a[80:83], v240, s[64:67], s34 offen offset:0 sc1\n\t"
    "v_mfma_f32_16x16x32_bf16 v[4:7], a[196:199], a[32:35], v[4:7]\n\t"
    "s_waitcnt lgkmcnt(12)\n\t"
    "ds_read_b64_tr_b16 a[196:197], v232 offset:24680\n\t"
    "v_mfma_f32_16x16x32_bf16 v[32:35], a[200:203], a[32:35], v[32:35]\n\t"
    "ds_read_b64_tr_b16 a[198:199], v232 offset:25704\n\t"
    "v_mfma_f32_16x16x32_bf16 v[40:43], a[200:203], a[36:39], v[40:43]\n\t"
    "v_mfma_f32_16x16x32_bf16 v[48:51], a[200:203], a[40:43], v[48:51]\n\t"
    "buffer_load_dwordx4 a[84:87], v240, s[64:67], s34 offen offset:1024 sc1\n\t"
    "v_mfma_f32_16x16x32_bf16 v[56:59], a[200:203], a[44:47], v[56:59]\n\t"
    "s_waitcnt lgkmcnt(12)\n\t"
    "ds_read_b64_tr_b16 a[200:201], v233 offset:24672\n\t"
    "v_mfma_f32_16x16x32_bf16 v[60:63], a[204:207], a[44:47], v[60:63]\n\t"
    "ds_read_b64_tr_b16 a[202:203], v233 offset:25696\n\t"
    "v_mfma_f32_16x16x32_bf16 v[52:55], a[204:207], a[40:43], v[52:55]\n\t"
    "v_mfma_f32_16x16x32_bf16 v[44:47], a[204:207], a[36:39], v[44:47]\n\t"
    "v_mfma_f32_16x16x32_bf16 v[36:39], a[204:207], a[32:35], v[36:39]\n\t"
    "s_waitcnt lgkmcnt(12)\n\t"
    "ds_read_b64_tr_b16 a[204:205], v233 offset:24680\n\t"
    "v_mfma_f32_16x16x32_bf16 v[64:67], a[208:211], a[32:35], v[64:67]\n\t"
    "ds_read_b64_tr_b16 a[206:207], v233 offset:25704\n\t"
    "v_mfma_f32_16x16x32_bf16 v[72:75], a[208:211], a[36:39], v[72:75]\n\t"
    "v_mfma_f32_16x16x32_bf16 v[80:83], a[208:211], a[40:43], v[80:83]\n\t"
    "buffer_load_dwordx4 a[88:91], v240, s[64:67], s34 offen offset:2048 sc1\n\t"
    "v_mfma_f32_16x16x32_bf16 v[88:91], a[208:211], a[44:47], v[88:91]\n\t"
    "s_waitcnt lgkmcnt(12)\n\t"
    "ds_read_b64_tr_b16 a[208:209], v234 offset:24672\n\t"
    "v_mfma_f32_16x16x32_bf16 v[92:95], a[212:215], a[44:47], v[92:95]\n\t"
    "ds_read_b64_tr_b16 a[210:211], v234 offset:25696\n\t"
    "v_mfma_f32_16x16x32_bf16 v[84:87], a[212:215], a[40:43], v[84:87]\n\t"
    "v_mfma_f32_16x16x32_bf16 v[76:79], a[212:215], a[36:39], v[76:79]\n\t"
    "buffer_load_dwordx4 a[92:95], v240, s[64:67], s34 offen offset:3072 sc1\n\t"
    "v_mfma_f32_16x16x32_bf16 v[68:71], a[212:215], a[32:35], v[68:71]\n\t"
    "s_waitcnt lgkmcnt(12)\n\t"
    "ds_read_b64_tr_b16 a[212:213], v234 offset:24680\n\t"
    "v_mfma_f32_16x16x32_bf16 v[96:99], a[216:219], a[32:35], v[96:99]\n\t"
    "ds_read_b64_tr_b16 a[214:215], v234 offset:25704\n\t"
    "v_mfma_f32_16x16x32_bf16 v[104:107], a[216:219], a[36:39], v[104:107]\n\t"
    "s_add_u32 m0, s31, 57568\n\t"
    "v_mfma_f32_16x16x32_bf16 v[112:115], a[216:219], a[40:43], v[112:115]\n\t"
    "s_mov_b32 s35, 1605632\n\t"
    "buffer_load_dwordx4 v241, s[68:71], s35 offen sc1 lds\n\t"
    "v_mfma_f32_16x16x32_bf16 v[120:123], a[216:219], a[44:47], v[120:123]\n\t"
    "s_waitcnt lgkmcnt(12)\n\t"
    "ds_read_b64_tr_b16 a[216:217], v235 offset:24672\n\t"
    "v_mfma_f32_16x16x32_bf16 v[124:127], a[220:223], a[44:47], v[124:127]\n\t"
    "ds_read_b64_tr_b16 a[218:219], v235 offset:25696\n\t"
    "v_mfma_f32_16x16x32_bf16 v[116:119], a[220:223], a[40:43], v[116:119]\n\t"
    "v_mfma_f32_16x16x32_bf16 v[108:111], a[220:223], a[36:39], v[108:111]\n\t"
    "buffer_load_dwordx4 v242, s[68:71], s35 offen offset:1024 sc1 lds\n\t"
    "v_mfma_f32_16x16x32_bf16 v[100:103], a[220:223], a[32:35], v[100:103]\n\t"
    "s_waitcnt vmcnt(14) lgkmcnt(12)\n\t"
    "ds_read_b64_tr_b16 a[220:221], v235 offset:24680\n\t"
    "v_mfma_f32_16x16x32_bf16 v[0:3], a[192:195], a[48:51], v[0:3]\n\t"
    "ds_read_b64_tr_b16 a[222:223], v235 offset:25704\n\t"
    "v_mfma_f32_16x16x32_bf16 v[8:11], a[192:195], a[52:55], v[8:11]\n\t"
    "s_add_u32 m0, s31, 65792\n\t"
    "v_mfma_f32_16x16x32_bf16 v[16:19], a[192:195], a[56:59], v[16:19]\n\t"
    "s_mov_b32 s35, 1835008\n\t"
    "buffer_load_dwordx4 v241, s[68:71], s35 offen sc1 lds\n\t"
    "v_mfma_f32_16x16x32_bf16 v[24:27], a[192:195], a[60:63], v[24:27]\n\t"
    "s_waitcnt lgkmcnt(12)\n\t"
    "ds_read_b64_tr_b16 a[192:193], v232 offset:32896\n\t"
    "v_mfma_f32_16x16x32_bf16 v[28:31], a[196:199], a[60:63], v[28:31]\n\t"
    "ds_read_b64_tr_b16 a[194:195], v232 offset:33920\n\t"
    "v_mfma_f32_16x16x32_bf16 v[20:23], a[196:199], a[56:59], v[20:23]\n\t"
    "v_mfma_f32_16x16x32_bf16 v[12:15], a[196:199], a[52:55], v[12:15]\n\t"
    "buffer_load_dwordx4 v242, s[68:71], s35 offen offset:1024 sc1 lds\n\t"
    "v_mfma_f32_16x16x32_bf16 v[4:7], a[196:199], a[48:51], v[4:7]\n\t"
    "s_waitcnt lgkmcnt(12)\n\t"
    "ds_read_b64_tr_b16 a[196:197], v232 offset:32904\n\t"
    "v_mfma_f32_16x16x32_bf16 v[32:35], a[200:203], a[48:51], v[32:35]\n\t"
    "ds_read_b64_tr_b16 a[198:199], v232 offset:33928\n\t"
    "v_mfma_f32_16x16x32_bf16 v[40:43], a[200:203], a[52:55], v[40:43]\n\t"
    "v_mfma_f32_16x16x32_bf16 v[48:51], a[200:203], a[56:59], v[48:51]\n\t"
    "s_mov_b32 s34, 49152\n\t"
    "buffer_load_dwordx4 a[96:99], v240, s[64:67], s34 offen offset:0 sc1\n\t"
    "v_mfma_f32_16x16x32_bf16 v[56:59], a[200:203], a[60:63], v[56:59]\n\t"
    "s_waitcnt lgkmcnt(12)\n\t"
    "ds_read_b64_tr_b16 a[200:201], v233 offset:32896\n\t"
    "v_mfma_f32_16x16x32_bf16 v[60:63], a[204:207], a[60:63], v[60:63]\n\t"
    "ds_read_b64_tr_b16 a[202:203], v233 offset:33920\n\t"
    "v_mfma_f32_16x16x32_bf16 v[52:55], a[204:207], a[56:59], v[52:55]\n\t"
    "v_mfma_f32_16x16x32_bf16 v[44:47], a[204:207], a[52:55], v[44:47]\n\t"
    "v_mfma_f32_16x16x32_bf16 v[36:39], a[204:207], a[48:51], v[36:39]\n\t"
    "s_waitcnt lgkmcnt(12)\n\t"
    "ds_read_b64_tr_b16 a[204:205], v233 offset:32904\n\t"
    "v_mfma_f32_16x16x32_bf16 v[64:67], a[208:211], a[48:51], v[64:67]\n\t"
    "ds_read_b64_tr_b16 a[206:207], v233 offset:33928\n\t"
    "v_mfma_f32_16x16x32_bf16 v[72:75], a[208:211], a[52:55], v[72:75]\n\t"
    "v_mfma_f32_16x16x32_bf16 v[80:83], a[208:211], a[56:59], v[80:83]\n\t"
    "buffer_load_dwordx4 a[100:103], v240, s[64:67], s34 offen offset:1024 sc1\n\t"
    "v_mfma_f32_16x16x32_bf16 v[88:91], a[208:211], a[60:63], v[88:91]\n\t"
    "s_waitcnt lgkmcnt(12)\n\t"
    "ds_read_b64_tr_b16 a[208:209], v234 offset:32896\n\t"
    "v_mfma_f32_16x16x32_bf16 v[92:95], a[212:215], a[60:63], v[92:95]\n\t"
    "ds_read_b64_tr_b16 a[210:211], v234 offset:33920\n\t"
    "v_mfma_f32_16x16x32_bf16 v[84:87], a[212:215], a[56:59], v[84:87]\n\t"
    "v_mfma_f32_16x16x32_bf16 v[76:79], a[212:215], a[52:55], v[76:79]\n\t"
    "buffer_load_dwordx4 a[104:107], v240, s[64:67], s34 offen offset:2048 sc1\n\t"
    "v_mfma_f32_16x16x32_bf16 v[68:71], a[212:215], a[48:51], v[68:71]\n\t"
    "s_waitcnt lgkmcnt(12)\n\t"
    "ds_read_b64_tr_b16 a[212:213], v234 offset:32904\n\t"
    "v_mfma_f32_16x16x32_bf16 v[96:99], a[216:219], a[48:51], v[96:99]\n\t"
    "ds_read_b64_tr_b16 a[214:215], v234 offset:33928\n\t"
    "v_mfma_f32_16x16x32_bf16 v[104:107], a[216:219], a[52:55], v[104:107]\n\t"
    "v_mfma_f32_16x16x32_bf16 v[112:115], a[216:219], a[56:59], v[112:115]\n\t"
    "buffer_load_dwordx4 a[108:111], v240, s[64:67], s34 offen offset:3072 sc1\n\t"
    "v_mfma_f32_16x16x32_bf16 v[120:123], a[216:219], a[60:63], v[120:123]\n\t"
    "s_waitcnt lgkmcnt(12)\n\t"
    "ds_read_b64_tr_b16 a[216:217], v235 offset:32896\n\t"
    "v_mfma_f32_16x16x32_bf16 v[124:127], a[220:223], a[60:63], v[124:127]\n\t"
    "ds_read_b64_tr_b16 a[218:219], v235 offset:33920\n\t"
    "v_mfma_f32_16x16x32_bf16 v[116:119], a[220:223], a[56:59], v[116:119]\n\t"
    "v_mfma_f32_16x16x32_bf16 v[108:111], a[220:223], a[52:55], v[108:111]\n\t"
    "s_mov_b32 s34, 53248\n\t"
    "buffer_load_dwordx4 a[112:115], v240, s[64:67], s34 offen offset:0 sc1\n\t"
    "v_mfma_f32_16x16x32_bf16 v[100:103], a[220:223], a[48:51], v[100:103]\n\t"
    "s_waitcnt vmcnt(13) lgkmcnt(12)\n\t"
    "ds_read_b64_tr_b16 a[220:221], v235 offset:32904\n\t"
    "v_mfma_f32_16x16x32_bf16 v[0:3], a[192:195], a[64:67], v[0:3]\n\t"
    "s_barrier\n\t"
    "v_mfma_f32_16x16x32_bf16 v[8:11], a[192:195], a[68:71], v[8:11]\n\t"
    "ds_read_b64_tr_b16 a[222:223], v235 offset:33928\n\t"
    "v_mfma_f32_16x16x32_bf16 v[16:19], a[192:195], a[72:75], v[16:19]\n\t"
    "buffer_load_dwordx4 a[116:119], v240, s[64:67], s34 offen offset:1024 sc1\n\t"
    "v_mfma_f32_16x16x32_bf16 v[24:27], a[192:195], a[76:79], v[24:27]\n\t"
    "s_waitcnt lgkmcnt(12)\n\t"
    "ds_read_b64_tr_b16 a[192:193], v232 offset:41120\n\t"
    "v_mfma_f32_16x16x32_bf16 v[28:31], a[196:199], a[76:79], v[28:31]\n\t"
    "Lpad2_%=:\n\t"
    "s_cbranch_execz LretA_%=\n\t"
    "ds_read_b64_tr_b16 a[194:195], v232 offset:42144\n\t"
    "v_mfma_f32_16x16x32_bf16 v[20:23], a[196:199], a[72:75], v[20:23]\n\t"
    "v_mfma_f32_16x16x32_bf16 v[12:15], a[196:199], a[68:71], v[12:15]\n\t"
    "buffer_load_dwordx4 a[120:123], v240, s[64:67], s34 offen offset:2048 sc1\n\t"
    "v_mfma_f32_16x16x32_bf16 v[4:7], a[196:199], a[64:67], v[4:7]\n\t"
    "s_waitcnt lgkmcnt(12)\n\t"
    "ds_read_b64_tr_b16 a[196:197], v232 offset:41128\n\t"
    "v_mfma_f32_16x16x32_bf16 v[32:35], a[200:203], a[64:67], v[32:35]\n\t"
    "ds_read_b64_tr_b16 a[198:199], v232 offset:42152\n\t"
    "v_mfma_f32_16x16x32_bf16 v[40:43], a[200:203], a[68:71], v[40:43]\n\t"
    "v_mfma_f32_16x16x32_bf16 v[48:51], a[200:203], a[72:75], v[48:51]\n\t"
    "buffer_load_dwordx4 a[124:127], v240, s[64:67], s34 offen offset:3072 sc1\n\t"
    "v_mfma_f32_16x16x32_bf16 v[56:59], a[200:203], a[76:79], v[56:59]\n\t"
    "s_waitcnt lgkmcnt(12)\n\t"
    "ds_read_b64_tr_b16 a[200:201], v233 offset:41120\n\t"
    "v_mfma_f32_16x16x32_bf16 v[60:63], a[204:207], a[76:79], v[60:63]\n\t"
    "ds_read_b64_tr_b16 a[202:203], v233 offset:42144\n\t"
    "v_mfma_f32_16x16x32_bf16 v[52:55], a[204:207], a[72:75], v[52:55]\n\t"
    "v_mfma_f32_16x16x32_bf16 v[44:47], a[204:207], a[68:71], v[44:47]\n\t"
    "v_mfma_f32_16x16x32_bf16 v[36:39], a[204:207], a[64:67], v[36:39]\n\t"
    "s_waitcnt lgkmcnt(12)\n\t"
    "ds_read_b64_tr_b16 a[204:205], v233 offset:41128\n\t"
    "v_mfma_f32_16x16x32_bf16 v[64:67], a[208:211], a[64:67], v[64:67]\n\t"
    "ds_read_b64_tr_b16 a[206:207], v233 offset:42152\n\t"
    "v_mfma_f32_16x16x32_bf16 v[72:75], a[208:211], a[68:71], v[72:75]\n\t"
    "s_add_u32 m0, s31, 74016\n\t"
    "v_mfma_f32_16x16x32_bf16 v[80:83], a[208:211], a[72:75], v[80:83]\n\t"
    "s_mov_b32 s35, 2064384\n\t"
    "buffer_load_dwordx4 v241, s[68:71], s35 offen sc1 lds\n\t"
    "v_mfma_f32_16x16x32_bf16 v[88:91], a[208:211], a[76:79], v[88:91]\n\t"
    "s_waitcnt lgkmcnt(12)\n\t"
    "ds_read_b64_tr_b16 a[208:209], v234 offset:41120\n\t"
    "v_mfma_f32_16x16x32_bf16 v[92:95], a[212:215], a[76:79], v[92:95]\n\t"
    "ds_read_b64_tr_b16 a[210:211], v234 offset:42144\n\t"
    "v_mfma_f32_16x16x32_bf16 v[84:87], a[212:215], a[72:75], v[84:87]\n\t"
    "v_mfma_f32_16x16x32_bf16 v[76:79], a[212:215], a[68:71], v[76:79]\n\t"
    "buffer_load_dwordx4 v242, s[68:71], s35 offen offset:1024 sc1 lds\n\t"
    "v_mfma_f32_16x16x32_bf16 v[68:71], a[212:215], a[64:67], v[68:71]\n\t"
    "s_waitcnt lgkmcnt(12)\n\t"
    "ds_read_b64_tr_b16 a[212:213], v234 offset:41128\n\t"
    "v_mfma_f32_16x16x32_bf16 v[96:99], a[216:219], a[64:67], v[96:99]\n\t"
    "ds_read_b64_tr_b16 a[214:215], v234 offset:42152\n\t"
    "v_mfma_f32_16x16x32_bf16 v[104:107], a[216:219], a[68:71], v[104:107]\n\t"
    "s_add_u32 m0, s31, 82240\n\t"
    "v_mfma_f32_16x16x32_bf16 v[112:115], a[216:219], a[72:75], v[112:115]\n\t"
    "s_mov_b32 s35, 2293760\n\t"
    "buffer_load_dwordx4 v241, s[68:71], s35 offen sc1 lds\n\t"
    "v_mfma_f32_16x16x32_bf16 v[120:123], a[216:219], a[76:79], v[120:123]\n\t"
    "s_waitcnt lgkmcnt(12)\n\t"
    "ds_read_b64_tr_b16 a[216:217], v235 offset:41120\n\t"
    "v_mfma_f32_16x16x32_bf16 v[124:127], a[220:223], a[76:79], v[124:127]\n\t"
    "ds_read_b64_tr_b16 a[218:219], v235 offset:42144\n\t"
    "v_mfma_f32_16x16x32_bf16 v[116:119], a[220:223], a[72:75], v[116:119]\n\t"
    "v_mfma_f32_16x16x32_bf16 v[108:111], a[220:223], a[68:71], v[108:111]\n\t"
    "buffer_load_dwordx4 v242, s[68:71], s35 offen offset:1024 sc1 lds\n\t"
    "v_mfma_f32_16x16x32_bf16 v[100:103], a[220:223], a[64:67], v[100:103]\n\t"
    "s_waitcnt vmcnt(16) lgkmcnt(12)\n\t"
    "ds_read_b64_tr_b16 a[220:221], v235 offset:41128\n\t"
    "v_mfma_f32_16x16x32_bf16 v[0:3], a[192:195], a[80:83], v[0:3]\n\t"
    "ds_read_b64_tr_b16 a[222:223], v235 offset:42152\n\t"
    "v_mfma_f32_16x16x32_bf16 v[8:11], a[192:195], a[84:87], v[8:11]\n\t"
    "s_mul_i32 s38, s25, 896\n\t"
    "v_mfma_f32_16x16x32_bf16 v[16:19], a[192:195], a[88:91], v[16:19]\n\t"
    "s_mov_b32 s34, 65536\n\t"
    "buffer_load_dwordx4 a[0:3], v240, s[64:67], s34 offen offset:0 sc1\n\t"
    "v_mfma_f32_16x16x32_bf16 v[24:27], a[192:195], a[92:95], v[24:27]\n\t"
    "s_waitcnt lgkmcnt(12)\n\t"
    "ds_read_b64_tr_b16 a[192:193], v232 offset:49344\n\t"
    "v_mfma_f32_16x16x32_bf16 v[28:31], a[196:199], a[92:95], v[28:31]\n\t"
    "s_lshl_b32 s39, s22, 3\n\t"
    "ds_read_b64_tr_b16 a[194:195], v232 offset:50368\n\t"
    "v_mfma_f32_16x16x32_bf16 v[20:23], a[196:199], a[88:91], v[20:23]\n\t"
    "s_add_u32 s38, s38, s39\n\t"
    "v_mfma_f32_16x16x32_bf16 v[12:15], a[196:199], a[84:87], v[12:15]\n\t"
    "buffer_load_dwordx4 a[4:7], v240, s[64:67], s34 offen offset:1024 sc1\n\t"
    "v_mfma_f32_16x16x32_bf16 v[4:7], a[196:199], a[80:83], v[4:7]\n\t"
    "s_waitcnt lgkmcnt(12)\n\t"
    "ds_read_b64_tr_b16 a[196:197], v232 offset:49352\n\t"
    "v_mfma_f32_16x16x32_bf16 v[32:35], a[200:203], a[80:83], v[32:35]\n\t"
    "s_lshl_b32 s38, s38, 11\n\t"
    "ds_read_b64_tr_b16 a[198:199], v232 offset:50376\n\t"
    "v_mfma_f32_16x16x32_bf16 v[40:43], a[200:203], a[84:87], v[40:43]\n\t"
    "s_lshl_b32 s39, s23, 8\n\t"
    "v_mfma_f32_16x16x32_bf16 v[48:51], a[200:203], a[88:91], v[48:51]\n\t"
    "buffer_load_dwordx4 a[8:11], v240, s[64:67], s34 offen offset:2048 sc1\n\t"
    "v_mfma_f32_16x16x32_bf16 v[56:59], a[200:203], a[92:95], v[56:59]\n\t"
    "s_waitcnt lgkmcnt(12)\n\t"
    "ds_read_b64_tr_b16 a[200:201], v233 offset:49344\n\t"
    "v_mfma_f32_16x16x32_bf16 v[60:63], a[204:207], a[92:95], v[60:63]\n\t"
    "s_add_u32 s38, s38, s39\n\t"
    "ds_read_b64_tr_b16 a[202:203], v233 offset:50368\n\t"
    "v_mfma_f32_16x16x32_bf16 v[52:55], a[204:207], a[88:91], v[52:55]\n\t"
    "s_add_u32 s96, s14, s38\n\t"
    "s_addc_u32 s97, s15, 0\n\t"
    "v_mfma_f32_16x16x32_bf16 v[44:47], a[204:207], a[84:87], v[44:47]\n\t"
    "v_mfma_f32_16x16x32_bf16 v[36:39], a[204:207], a[80:83], v[36:39]\n\t"
    "s_waitcnt lgkmcnt(12)\n\t"
    "ds_read_b64_tr_b16 a[204:205], v233 offset:49352\n\t"
    "v_mfma_f32_16x16x32_bf16 v[64:67], a[208:211], a[80:83], v[64:67]\n\t"
    "s_and_b32 s97, s97, 0xffff\n\t"
    "ds_read_b64_tr_b16 a[206:207], v233 offset:50376\n\t"
    "v_mfma_f32_16x16x32_bf16 v[72:75], a[208:211], a[84:87], v[72:75]\n\t"
    "s_mov_b32 s98, 0x1b3900\n\t"
    "v_mfma_f32_16x16x32_bf16 v[80:83], a[208:211], a[88:91], v[80:83]\n\t"
    "buffer_load_dwordx4 a[12:15], v240, s[64:67], s34 offen offset:3072 sc1\n\t"
    "v_mfma_f32_16x16x32_bf16 v[88:91], a[208:211], a[92:95], v[88:91]\n\t"
    "s_waitcnt lgkmcnt(12)\n\t"
    "ds_read_b64_tr_b16 a[208:209], v234 offset:49344\n\t"
    "v_mfma_f32_16x16x32_bf16 v[92:95], a[212:215], a[92:95], v[92:95]\n\t"
    "s_mov_b32 s99, 0x20000\n\t"
    "ds_read_b64_tr_b16 a[210:211], v234 offset:50368\n\t"
    "v_mfma_f32_16x16x32_bf16 v[84:87], a[212:215], a[88:91], v[84:87]\n\t"
    "v_and_b32 v227, 31, v245\n\t"
    "v_mfma_f32_16x16x32_bf16 v[76:79], a[212:215], a[84:87], v[76:79]\n\t"
    "s_mov_b32 s34, 69632\n\t"
    "buffer_load_dwordx4 a[16:19], v240, s[64:67], s34 offen offset:0 sc1\n\t"
    "v_mfma_f32_16x16x32_bf16 v[68:71], a[212:215], a[80:83], v[68:71]\n\t"
    "s_waitcnt lgkmcnt(12)\n\t"
    "ds_read_b64_tr_b16 a[212:213], v234 offset:49352\n\t"
    "v_mfma_f32_16x16x32_bf16 v[96:99], a[216:219], a[80:83], v[96:99]\n\t"
    "ds_read_b64_tr_b16 a[214:215], v234 offset:50376\n\t"
    "v_subrev_u32 v224, 4, v227\n\t"
    "v_mfma_f32_16x16x32_bf16 v[104:107], a[216:219], a[84:87], v[104:107]\n\t"
    "v_mul_u32_u24 v225, 43, v224\n\t"
    "v_mfma_f32_16x16x32_bf16 v[112:115], a[216:219], a[88:91], v[112:115]\n\t"
    "buffer_load_dwordx4 a[20:23], v240, s[64:67], s34 offen offset:1024 sc1\n\t"
    "v_mfma_f32_16x16x32_bf16 v[120:123], a[216:219], a[92:95], v[120:123]\n\t"
    "s_waitcnt lgkmcnt(12)\n\t"
    "ds_read_b64_tr_b16 a[216:217], v235 offset:49344\n\t"
    "v_mfma_f32_16x16x32_bf16 v[124:127], a[220:223], a[92:95], v[124:127]\n\t"
    "ds_read_b64_tr_b16 a[218:219], v235 offset:50368\n\t"
    "v_lshrrev_b32 v225, 8, v225\n\t"
    "v_mfma_f32_16x16x32_bf16 v[116:119], a[220:223], a[88:91], v[116:119]\n\t"
    "v_mfma_f32_16x16x32_bf16 v[108:111], a[220:223], a[84:87], v[108:111]\n\t"
    "buffer_load_dwordx4 a[24:27], v240, s[64:67], s34 offen offset:2048 sc1\n\t"
    "v_mfma_f32_16x16x32_bf16 v[100:103], a[220:223], a[80:83], v[100:103]\n\t"
    "s_waitcnt vmcnt(15) lgkmcnt(12)\n\t"
    "ds_read_b64_tr_b16 a[220:221], v235 offset:49352\n\t"
    "v_mfma_f32_16x16x32_bf16 v[0:3], a[192:195], a[96:99], v[0:3]\n\t"
    "s_barrier\n\t"
    "v_mfma_f32_16x16x32_bf16 v[8:11], a[192:195], a[100:103], v[8:11]\n\t"
    "ds_read_b64_tr_b16 a[222:223], v235 offset:50376\n\t"
    "v_mul_u32_u24 v226, 6, v225\n\t"
    "v_mfma_f32_16x16x32_bf16 v[16:19], a[192:195], a[104:107], v[16:19]\n\t"
    "buffer_load_dwordx4 a[28:31], v240, s[64:67], s34 offen offset:3072 sc1\n\t"
    "v_sub_u32 v224, v224, v226\n\t"
    "v_mfma_f32_16x16x32_bf16 v[24:27], a[192:195], a[108:111], v[24:27]\n\t"
    "s_waitcnt lgkmcnt(12)\n\t"
    "ds_read_b64_tr_b16 a[192:193], v236 offset:0\n\t"
    "v_mfma_f32_16x16x32_bf16 v[28:31], a[196:199], a[108:111], v[28:31]\n\t"
    "ds_read_b64_tr_b16 a[194:195], v236 offset:1024\n\t"
    "v_add3_u32 v224, v224, s26, 1\n\t"
    "v_mfma_f32_16x16x32_bf16 v[20:23], a[196:199], a[104:107], v[20:23]\n\t"
    "s_add_u32 m0, s31, 90464\n\t"
    "v_subrev_u32 v226, 7, v224\n\t"
    "v_mfma_f32_16x16x32_bf16 v[12:15], a[196:199], a[100:103], v[12:15]\n\t"
    "s_mov_b32 s35, 2523136\n\t"
    "buffer_load_dwordx4 v241, s[68:71], s35 offen sc1 lds\n\t"
    "v_mfma_f32_16x16x32_bf16 v[4:7], a[196:199], a[96:99], v[4:7]\n\t"
    "s_waitcnt lgkmcnt(12)\n\t"
    "ds_read_b64_tr_b16 a[196:197], v236 offset:8\n\t"
    "v_mfma_f32_16x16x32_bf16 v[32:35], a[200:203], a[96:99], v[32:35]\n\t"
    "ds_read_b64_tr_b16 a[198:199], v236 offset:1032\n\t"
    "v_min_u32 v224, v224, v226\n\t"
    "v_mfma_f32_16x16x32_bf16 v[40:43], a[200:203], a[100:103], v[40:43]\n\t"
    "v_lshl_add_u32 v224, v224, 2, v225\n\t"
    "v_lshl_add_u32 v225, s26, 2, v227\n\t"
    "v_mfma_f32_16x16x32_bf16 v[48:51], a[200:203], a[104:107], v[48:51]\n\t"
    "buffer_load_dwordx4 v242, s[68:71], s35 offen offset:1024 sc1 lds\n\t"
    "v_mfma_f32_16x16x32_bf16 v[56:59], a[200:203], a[108:111], v[56:59]\n\t"
    "s_waitcnt lgkmcnt(12)\n\t"
    "ds_read_b64_tr_b16 a[200:201], v237 offset:0\n\t"
    "v_mfma_f32_16x16x32_bf16 v[60:63], a[204:207], a[108:111], v[60:63]\n\t"
    "ds_read_b64_tr_b16 a[202:203], v237 offset:1024\n\t"
    "v_mfma_f32_16x16x32_bf16 v[52:55], a[204:207], a[104:107], v[52:55]\n\t"
    "v_cmp_gt_u32 vcc, 4, v227\n\t"
    "s_nop 1\n\t"
    "v_cndmask_b32 v228, v224, v225, vcc\n\t"
    "v_mfma_f32_16x16x32_bf16 v[44:47], a[204:207], a[100:103], v[44:47]\n\t"
    "v_mul_u32_u24 v225, 37, v227\n\t"
    "v_lshrrev_b32 v225, 8, v225\n\t"
    "v_mfma_f32_16x16x32_bf16 v[36:39], a[204:207], a[96:99], v[36:39]\n\t"
    "s_waitcnt lgkmcnt(12)\n\t"
    "ds_read_b64_tr_b16 a[204:205], v237 offset:8\n\t"
    "v_mfma_f32_16x16x32_bf16 v[64:67], a[208:211], a[96:99], v[64:67]\n\t"
    "ds_read_b64_tr_b16 a[206:207], v237 offset:1032\n\t"
    "v_mul_u32_u24 v226, 7, v225\n\t"
    "v_mfma_f32_16x16x32_bf16 v[72:75], a[208:211], a[100:103], v[72:75]\n\t"
    "s_add_u32 m0, s31, 98688\n\t"
    "v_sub_u32 v224, v227, v226\n\t"
    "v_mfma_f32_16x16x32_bf16 v[80:83], a[208:211], a[104:107], v[80:83]\n\t"
    "s_mov_b32 s35, 2752512\n\t"
    "buffer_load_dwordx4 v241, s[68:71], s35 offen sc1 lds\n\t"
    "v_mfma_f32_16x16x32_bf16 v[88:91], a[208:211], a[108:111], v[88:91]\n\t"
    "s_waitcnt lgkmcnt(12)\n\t"
    "ds_read_b64_tr_b16 a[208:209], v238 offset:0\n\t"
    "v_mfma_f32_16x16x32_bf16 v[92:95], a[212:215], a[108:111], v[92:95]\n\t"
    "ds_read_b64_tr_b16 a[210:211], v238 offset:1024\n\t"
    "v_lshl_add_u32 v224, v224, 2, v225\n\t"
    "v_mfma_f32_16x16x32_bf16 v[84:87], a[212:215], a[104:107], v[84:87]\n\t"
    "s_cmp_eq_u32 s26, 7\n\t"
    "s_cselect_b64 vcc, -1, 0\n\t"
    "v_cndmask_b32 v228, v228, v224, vcc\n\t"
    "v_mfma_f32_16x16x32_bf16 v[76:79], a[212:215], a[100:103], v[76:79]\n\t"
    "buffer_load_dwordx4 v242, s[68:71], s35 offen offset:1024 sc1 lds\n\t"
    "v_lshlrev_b32 v228, 12, v228\n\t"
    "v_mfma_f32_16x16x32_bf16 v[68:71], a[212:215], a[96:99], v[68:71]\n\t"
    "s_waitcnt lgkmcnt(12)\n\t"
    "ds_read_b64_tr_b16 a[212:213], v238 offset:8\n\t"
    "v_mfma_f32_16x16x32_bf16 v[96:99], a[216:219], a[96:99], v[96:99]\n\t"
    "ds_read_b64_tr_b16 a[214:215], v238 offset:1032\n\t"
    "v_and_b32 v224, 0xffffe000, v228\n\t"
    "v_mfma_f32_16x16x32_bf16 v[104:107], a[216:219], a[100:103], v[104:107]\n\t"
    "v_add_u32 v224, v224, v228\n\t"
    "v_mfma_f32_16x16x32_bf16 v[112:115], a[216:219], a[104:107], v[112:115]\n\t"
    "s_mov_b32 s34, 81920\n\t"
    "buffer_load_dwordx4 a[32:35], v240, s[64:67], s34 offen offset:0 sc1\n\t"
    "v_mfma_f32_16x16x32_bf16 v[120:123], a[216:219], a[108:111], v[120:123]\n\t"
    "s_waitcnt lgkmcnt(12)\n\t"
    "ds_read_b64_tr_b16 a[216:217], v239 offset:0\n\t"
    "v_mfma_f32_16x16x32_bf16 v[124:127], a[220:223], a[108:111], v[124:127]\n\t"
    "ds_read_b64_tr_b16 a[218:219], v239 offset:1024\n\t"
    "v_mfma_f32_16x16x32_bf16 v[116:119], a[220:223], a[104:107], v[116:119]\n\t"
    "v_cmp_gt_u32 vcc, 32, v245\n\t"
    "s_nop 1\n\t"
    "v_cndmask_b32 v246, v228, v224, vcc\n\t"
    "v_mfma_f32_16x16x32_bf16 v[108:111], a[220:223], a[100:103], v[108:111]\n\t"
    "buffer_load_dwordx4 a[36:39], v240, s[64:67], s34 offen offset:1024 sc1\n\t"
    "v_mfma_f32_16x16x32_bf16 v[100:103], a[220:223], a[96:99], v[100:103]\n\t"
    "s_waitcnt vmcnt(18) lgkmcnt(12)\n\t"
    "ds_read_b64_tr_b16 a[220:221], v239 offset:8\n\t"
    "v_mfma_f32_16x16x32_bf16 v[0:3], a[192:195], a[112:115], v[0:3]\n\t"
    "ds_read_b64_tr_b16 a[222:223], v239 offset:1032\n\t"
    "v_mfma_f32_16x16x32_bf16 v[8:11], a[192:195], a[116:119], v[8:11]\n\t"
    "v_mfma_f32_16x16x32_bf16 v[16:19], a[192:195], a[120:123], v[16:19]\n\t"
    "buffer_load_dwordx4 a[40:43], v240, s[64:67], s34 offen offset:2048 sc1\n\t"
    "v_mfma_f32_16x16x32_bf16 v[24:27], a[192:195], a[124:127], v[24:27]\n\t"
    "s_waitcnt lgkmcnt(12)\n\t"
    "ds_read_b64_tr_b16 a[192:193], v236 offset:8224\n\t"
    "v_mfma_f32_16x16x32_bf16 v[28:31], a[196:199], a[124:127], v[28:31]\n\t"
    "ds_read_b64_tr_b16 a[194:195], v236 offset:9248\n\t"
    "v_mfma_f32_16x16x32_bf16 v[20:23], a[196:199], a[120:123], v[20:23]\n\t"
    "v_mfma_f32_16x16x32_bf16 v[12:15], a[196:199], a[116:119], v[12:15]\n\t"
    "buffer_load_dwordx4 a[44:47], v240, s[64:67], s34 offen offset:3072 sc1\n\t"
    "v_mfma_f32_16x16x32_bf16 v[4:7], a[196:199], a[112:115], v[4:7]\n\t"
    "s_waitcnt lgkmcnt(12)\n\t"
    "ds_read_b64_tr_b16 a[196:197], v236 offset:8232\n\t"
    "v_mfma_f32_16x16x32_bf16 v[32:35], a[200:203], a[112:115], v[32:35]\n\t"
    "ds_read_b64_tr_b16 a[198:199], v236 offset:9256\n\t"
    "v_mfma_f32_16x16x32_bf16 v[40:43], a[200:203], a[116:119], v[40:43]\n\t"
    "v_mfma_f32_16x16x32_bf16 v[48:51], a[200:203], a[120:123], v[48:51]\n\t"
    "s_mov_b32 s34, 86016\n\t"
    "buffer_load_dwordx4 a[48:51], v240, s[64:67], s34 offen offset:0 sc1\n\t"
    "v_mfma_f32_16x16x32_bf16 v[56:59], a[200:203], a[124:127], v[56:59]\n\t"
    "s_waitcnt lgkmcnt(12)\n\t"
    "ds_read_b64_tr_b16 a[200:201], v237 offset:8224\n\t"
    "v_mfma_f32_16x16x32_bf16 v[60:63], a[204:207], a[124:127], v[60:63]\n\t"
    "ds_read_b64_tr_b16 a[202:203], v237 offset:9248\n\t"
    "v_mfma_f32_16x16x32_bf16 v[52:55], a[204:207], a[120:123], v[52:55]\n\t"
    "v_mfma_f32_16x16x32_bf16 v[44:47], a[204:207], a[116:119], v[44:47]\n\t"
    "v_mfma_f32_16x16x32_bf16 v[36:39], a[204:207], a[112:115], v[36:39]\n\t"
    "s_waitcnt lgkmcnt(12)\n\t"
    "ds_read_b64_tr_b16 a[204:205], v237 offset:8232\n\t"
    "v_mfma_f32_16x16x32_bf16 v[64:67], a[208:211], a[112:115], v[64:67]\n\t"
    "ds_read_b64_tr_b16 a[206:207], v237 offset:9256\n\t"
    "v_mfma_f32_16x16x32_bf16 v[72:75], a[208:211], a[116:119], v[72:75]\n\t"
    "v_mfma_f32_16x16x32_bf16 v[80:83], a[208:211], a[120:123], v[80:83]\n\t"
    "buffer_load_dwordx4 a[52:55], v240, s[64:67], s34 offen offset:1024 sc1\n\t"
    "v_mfma_f32_16x16x32_bf16 v[88:91], a[208:211], a[124:127], v[88:91]\n\t"
    "s_waitcnt lgkmcnt(12)\n\t"
    "ds_read_b64_tr_b16 a[208:209], v238 offset:8224\n\t"
    "v_mfma_f32_16x16x32_bf16 v[92:95], a[212:215], a[124:127], v[92:95]\n\t"
    "ds_read_b64_tr_b16 a[210:211], v238 offset:9248\n\t"
    "v_mfma_f32_16x16x32_bf16 v[84:87], a[212:215], a[120:123], v[84:87]\n\t"
    "v_mfma_f32_16x16x32_bf16 v[76:79], a[212:215], a[116:119], v[76:79]\n\t"
    "buffer_load_dwordx4 a[56:59], v240, s[64:67], s34 offen offset:2048 sc1\n\t"
    "v_mfma_f32_16x16x32_bf16 v[68:71], a[212:215], a[112:115], v[68:71]\n\t"
    "s_waitcnt lgkmcnt(12)\n\t"
    "ds_read_b64_tr_b16 a[212:213], v238 offset:8232\n\t"
    "v_mfma_f32_16x16x32_bf16 v[96:99], a[216:219], a[112:115], v[96:99]\n\t"
    "ds_read_b64_tr_b16 a[214:215], v238 offset:9256\n\t"
    "v_mfma_f32_16x16x32_bf16 v[104:107], a[216:219], a[116:119], v[104:107]\n\t"
    "v_mfma_f32_16x16x32_bf16 v[112:115], a[216:219], a[120:123], v[112:115]\n\t"
    "buffer_load_dwordx4 a[60:63], v240, s[64:67], s34 offen offset:3072 sc1\n\t"
    "v_mfma_f32_16x16x32_bf16 v[120:123], a[216:219], a[124:127], v[120:123]\n\t"
    "s_waitcnt lgkmcnt(12)\n\t"
    "ds_read_b64_tr_b16 a[216:217], v239 offset:8224\n\t"
    "v_mfma_f32_16x16x32_bf16 v[124:127], a[220:223], a[124:127], v[124:127]\n\t"
    "ds_read_b64_tr_b16 a[218:219], v239 offset:9248\n\t"
    "v_mfma_f32_16x16x32_bf16 v[116:119], a[220:223], a[120:123], v[116:119]\n\t"
    "s_add_u32 m0, s31, 106912\n\t"
    "v_mfma_f32_16x16x32_bf16 v[108:111], a[220:223], a[116:119], v[108:111]\n\t"
    "s_mov_b32 s35, 2981888\n\t"
    "buffer_load_dwordx4 v241, s[68:71], s35 offen sc1 lds\n\t"
    "v_mfma_f32_16x16x32_bf16 v[100:103], a[220:223], a[112:115], v[100:103]\n\t"
    "s_waitcnt vmcnt(17) lgkmcnt(12)\n\t"
    "ds_read_b64_tr_b16 a[220:221], v239 offset:8232\n\t"
    "v_mfma_f32_16x16x32_bf16 v[0:3], a[192:195], a[0:3], v[0:3]\n\t"
    "s_barrier\n\t"
    "v_mfma_f32_16x16x32_bf16 v[8:11], a[192:195], a[4:7], v[8:11]\n\t"
    "ds_read_b64_tr_b16 a[222:223], v239 offset:9256\n\t"
    "v_lshrrev_b32 v249, 1, v245\n\t"
    "v_mfma_f32_16x16x32_bf16 v[16:19], a[192:195], a[8:11], v[16:19]\n\t"
    "buffer_load_dwordx4 v242, s[68:71], s35 offen offset:1024 sc1 lds\n\t"
    "v_and_b32 v224, 1, v245\n\t"
    "v_mfma_f32_16x16x32_bf16 v[24:27], a[192:195], a[12:15], v[24:27]\n\t"
    "s_waitcnt lgkmcnt(12)\n\t"
    "ds_read_b64_tr_b16 a[192:193], v236 offset:16448\n\t"
    "v_mfma_f32_16x16x32_bf16 v[28:31], a[196:199], a[12:15], v[28:31]\n\t"
    "ds_read_b64_tr_b16 a[194:195], v236 offset:17472\n\t"
    "v_lshl_add_u32 v249, v249, 3, v224\n\t"
    "v_mfma_f32_16x16x32_bf16 v[20:23], a[196:199], a[8:11], v[20:23]\n\t"
    "s_lshl_b32 s38, s22, 7\n\t"
    "v_mfma_f32_16x16x32_bf16 v[12:15], a[196:199], a[4:7], v[12:15]\n\t"
    "s_add_u32 s38, s38, 131072\n\t"
    "v_mfma_f32_16x16x32_bf16 v[4:7], a[196:199], a[0:3], v[4:7]\n\t"
    "s_waitcnt lgkmcnt(12)\n\t"
    "ds_read_b64_tr_b16 a[196:197], v236 offset:16456\n\t"
    "v_mfma_f32_16x16x32_bf16 v[32:35], a[200:203], a[0:3], v[32:35]\n\t"
    "ds_read_b64_tr_b16 a[198:199], v236 offset:17480\n\t"
    "v_lshl_add_u32 v249, v249, 6, s38\n\t"
    "v_mfma_f32_16x16x32_bf16 v[40:43], a[200:203], a[4:7], v[40:43]\n\t"
    "s_add_u32 m0, s31, 0\n\t"
    "global_load_dwordx2 v[250:251], v249, s[20:21] sc1\n\t"
    "v_mfma_f32_16x16x32_bf16 v[48:51], a[200:203], a[8:11], v[48:51]\n\t"
    "s_mov_b32 s35, 3211264\n\t"
    "buffer_load_dwordx4 v241, s[68:71], s35 offen sc1 lds\n\t"
    "v_mfma_f32_16x16x32_bf16 v[56:59], a[200:203], a[12:15], v[56:59]\n\t"
    "s_waitcnt lgkmcnt(12)\n\t"
    "ds_read_b64_tr_b16 a[200:201], v237 offset:16448\n\t"
    "v_mfma_f32_16x16x32_bf16 v[60:63], a[204:207], a[12:15], v[60:63]\n\t"
    "ds_read_b64_tr_b16 a[202:203], v237 offset:17472\n\t"
    "v_lshrrev_b32 v225, 3, v245\n\t"
    "v_mfma_f32_16x16x32_bf16 v[52:55], a[204:207], a[8:11], v[52:55]\n\t"
    "v_and_b32 v227, 0x70, v240\n\t"
    "v_lshl_or_b32 v227, v225, 18, v227\n\t"
    "v_mfma_f32_16x16x32_bf16 v[44:47], a[204:207], a[4:7], v[44:47]\n\t"
    "buffer_load_dwordx4 v242, s[68:71], s35 offen offset:1024 sc1 lds\n\t"
    "v_and_b32 v226, 3, v245\n\t"
    "v_mfma_f32_16x16x32_bf16 v[36:39], a[204:207], a[0:3], v[36:39]\n\t"
    "s_waitcnt lgkmcnt(12)\n\t"
    "ds_read_b64_tr_b16 a[204:205], v237 offset:16456\n\t"
    "v_mfma_f32_16x16x32_bf16 v[64:67], a[208:211], a[0:3], v[64:67]\n\t"
    "ds_read_b64_tr_b16 a[206:207], v237 offset:17480\n\t"
    "v_mad_u32_u24 v228, v226, 36, v225\n\t"
    "v_mfma_f32_16x16x32_bf16 v[72:75], a[208:211], a[4:7], v[72:75]\n\t"
    "v_bfe_u32 v226, v245, 2, 1\n\t"
    "v_mul_u32_u24 v226, 4608, v226\n\t"
    "v_mfma_f32_16x16x32_bf16 v[80:83], a[208:211], a[8:11], v[80:83]\n\t"
    "s_mov_b32 s34, 98304\n\t"
    "buffer_load_dwordx4 a[64:67], v240, s[64:67], s34 offen offset:0 sc1\n\t"
    "v_mfma_f32_16x16x32_bf16 v[88:91], a[208:211], a[12:15], v[88:91]\n\t"
    "s_waitcnt lgkmcnt(12)\n\t"
    "ds_read_b64_tr_b16 a[208:209], v238 offset:16448\n\t"
    "v_mfma_f32_16x16x32_bf16 v[92:95], a[212:215], a[12:15], v[92:95]\n\t"
    "ds_read_b64_tr_b16 a[210:211], v238 offset:17472\n\t"
    "v_add_u32 v228, v228, v226\n\t"
    "v_mfma_f32_16x16x32_bf16 v[84:87], a[212:215], a[8:11], v[84:87]\n\t"
    "s_mul_i32 s38, s22, 1152\n\t"
    "s_add_u32 s38, s38, 115200\n\t"
    "v_mfma_f32_16x16x32_bf16 v[76:79], a[212:215], a[4:7], v[76:79]\n\t"
    "v_lshl_add_u32 v228, v228, 2, s38\n\t"
    "v_mfma_f32_16x16x32_bf16 v[68:71], a[212:215], a[0:3], v[68:71]\n\t"
    "s_waitcnt lgkmcnt(12)\n\t"
    "ds_read_b64_tr_b16 a[212:213], v238 offset:16456\n\t"
    "v_mfma_f32_16x16x32_bf16 v[96:99], a[216:219], a[0:3], v[96:99]\n\t"
    "s_lshl_b32 s38, s22, 16\n\t"
    "ds_read_b64_tr_b16 a[214:215], v238 offset:17480\n\t"
    "v_mfma_f32_16x16x32_bf16 v[104:107], a[216:219], a[4:7], v[104:107]\n\t"
    "s_lshl_b32 s39, s29, 7\n\t"
    "v_mfma_f32_16x16x32_bf16 v[112:115], a[216:219], a[8:11], v[112:115]\n\t"
    "s_add_u32 s38, s38, s39\n\t"
    "buffer_load_dwordx4 a[68:71], v240, s[64:67], s34 offen offset:1024 sc1\n\t"
    "v_mfma_f32_16x16x32_bf16 v[120:123], a[216:219], a[12:15], v[120:123]\n\t"
    "s_waitcnt lgkmcnt(12)\n\t"
    "ds_read_b64_tr_b16 a[216:217], v239 offset:16448\n\t"
    "v_mfma_f32_16x16x32_bf16 v[124:127], a[220:223], a[12:15], v[124:127]\n\t"
    "ds_read_b64_tr_b16 a[218:219], v239 offset:17472\n\t"
    "v_mfma_f32_16x16x32_bf16 v[116:119], a[220:223], a[8:11], v[116:119]\n\t"
    "s_add_u32 s38, s38, 0x800000\n\t"
    "s_add_u32 s42, s20, s38\n\t"
    "s_addc_u32 s43, s21, 0\n\t"
    "v_mfma_f32_16x16x32_bf16 v[108:111], a[220:223], a[4:7], v[108:111]\n\t"
    "buffer_load_dwordx4 a[72:75], v240, s[64:67], s34 offen offset:2048 sc1\n\t"
    "v_mfma_f32_16x16x32_bf16 v[100:103], a[220:223], a[0:3], v[100:103]\n\t"
    "s_waitcnt vmcnt(20) lgkmcnt(12)\n\t"
    "ds_read_b64_tr_b16 a[220:221], v239 offset:16456\n\t"
    "v_mfma_f32_16x16x32_bf16 v[0:3], a[192:195], a[16:19], v[0:3]\n\t"
    "ds_read_b64_tr_b16 a[222:223], v239 offset:17480\n\t"
    "v_mfma_f32_16x16x32_bf16 v[8:11], a[192:195], a[20:23], v[8:11]\n\t"
    "v_mfma_f32_16x16x32_bf16 v[16:19], a[192:195], a[24:27], v[16:19]\n\t"
    "buffer_load_dwordx4 a[76:79], v240, s[64:67], s34 offen offset:3072 sc1\n\t"
    "v_mfma_f32_16x16x32_bf16 v[24:27], a[192:195], a[28:31], v[24:27]\n\t"
    "s_waitcnt lgkmcnt(12)\n\t"
    "ds_read_b64_tr_b16 a[192:193], v236 offset:24672\n\t"
    "v_mfma_f32_16x16x32_bf16 v[28:31], a[196:199], a[28:31], v[28:31]\n\t"
    "ds_read_b64_tr_b16 a[194:195], v236 offset:25696\n\t"
    "v_mfma_f32_16x16x32_bf16 v[20:23], a[196:199], a[24:27], v[20:23]\n\t"
    "v_mfma_f32_16x16x32_bf16 v[12:15], a[196:199], a[20:23], v[12:15]\n\t"
    "s_mov_b32 s34, 102400\n\t"
    "buffer_load_dwordx4 a[80:83], v240, s[64:67], s34 offen offset:0 sc1\n\t"
    "v_mfma_f32_16x16x32_bf16 v[4:7], a[196:199], a[16:19], v[4:7]\n\t"
    "s_waitcnt lgkmcnt(12)\n\t"
    "ds_read_b64_tr_b16 a[196:197], v236 offset:24680\n\t"
    "v_mfma_f32_16x16x32_bf16 v[32:35], a[200:203], a[16:19], v[32:35]\n\t"
    "ds_read_b64_tr_b16 a[198:199], v236 offset:25704\n\t"
    "v_mfma_f32_16x16x32_bf16 v[40:43], a[200:203], a[20:23], v[40:43]\n\t"
    "v_mfma_f32_16x16x32_bf16 v[48:51], a[200:203], a[24:27], v[48:51]\n\t"
    "buffer_load_dwordx4 a[84:87], v240, s[64:67], s34 offen offset:1024 sc1\n\t"
    "v_mfma_f32_16x16x32_bf16 v[56:59], a[200:203], a[28:31], v[56:59]\n\t"
    "s_waitcnt lgkmcnt(12)\n\t"
    "ds_read_b64_tr_b16 a[200:201], v237 offset:24672\n\t"
    "v_mfma_f32_16x16x32_bf16 v[60:63], a[204:207], a[28:31], v[60:63]\n\t"
    "ds_read_b64_tr_b16 a[202:203], v237 offset:25696\n\t"
    "v_mfma_f32_16x16x32_bf16 v[52:55], a[204:207], a[24:27], v[52:55]\n\t"
    "v_mfma_f32_16x16x32_bf16 v[44:47], a[204:207], a[20:23], v[44:47]\n\t"
    "v_mfma_f32_16x16x32_bf16 v[36:39], a[204:207], a[16:19], v[36:39]\n\t"
    "s_waitcnt lgkmcnt(12)\n\t"
    "ds_read_b64_tr_b16 a[204:205], v237 offset:24680\n\t"
    "v_mfma_f32_16x16x32_bf16 v[64:67], a[208:211], a[16:19], v[64:67]\n\t"
    "ds_read_b64_tr_b16 a[206:207], v237 offset:25704\n\t"
    "v_mfma_f32_16x16x32_bf16 v[72:75], a[208:211], a[20:23], v[72:75]\n\t"
    "v_mfma_f32_16x16x32_bf16 v[80:83], a[208:211], a[24:27], v[80:83]\n\t"
    "buffer_load_dwordx4 a[88:91], v240, s[64:67], s34 offen offset:2048 sc1\n\t"
    "v_mfma_f32_16x16x32_bf16 v[88:91], a[208:211], a[28:31], v[88:91]\n\t"
    "s_waitcnt lgkmcnt(12)\n\t"
    "ds_read_b64_tr_b16 a[208:209], v238 offset:24672\n\t"
    "v_mfma_f32_16x16x32_bf16 v[92:95], a[212:215], a[28:31], v[92:95]\n\t"
    "ds_read_b64_tr_b16 a[210:211], v238 offset:25696\n\t"
    "v_mfma_f32_16x16x32_bf16 v[84:87], a[212:215], a[24:27], v[84:87]\n\t"
    "v_mfma_f32_16x16x32_bf16 v[76:79], a[212:215], a[20:23], v[76:79]\n\t"
    "buffer_load_dwordx4 a[92:95], v240, s[64:67], s34 offen offset:3072 sc1\n\t"
    "v_mfma_f32_16x16x32_bf16 v[68:71], a[212:215], a[16:19], v[68:71]\n\t"
    "s_waitcnt lgkmcnt(12)\n\t"
    "ds_read_b64_tr_b16 a[212:213], v238 offset:24680\n\t"
    "v_mfma_f32_16x16x32_bf16 v[96:99], a[216:219], a[16:19], v[96:99]\n\t"
    "ds_read_b64_tr_b16 a[214:215], v238 offset:25704\n\t"
    "v_mfma_f32_16x16x32_bf16 v[104:107], a[216:219], a[20:23], v[104:107]\n\t"
    "s_add_u32 m0, s31, 8224\n\t"
    "s_waitcnt vmcnt(10)\n\t"
    "v_mov_b32 v230, v250\n\t"
    "v_mfma_f32_16x16x32_bf16 v[112:115], a[216:219], a[24:27], v[112:115]\n\t"
    "s_mov_b32 s35, 3440640\n\t"
    "buffer_load_dwordx4 v241, s[68:71], s35 offen sc1 lds\n\t"
    "v_and_b32 v250, 0xffff0000, v250\n\t"
    "v_mfma_f32_16x16x32_bf16 v[120:123], a[216:219], a[28:31], v[120:123]\n\t"
    "s_waitcnt lgkmcnt(12)\n\t"
    "ds_read_b64_tr_b16 a[216:217], v239 offset:24672\n\t"
    "v_cmp_ne_u64 vcc, s[74:75], v[250:251]\n\t"
    "v_mfma_f32_16x16x32_bf16 v[124:127], a[220:223], a[28:31], v[124:127]\n\t"
    "ds_read_b64_tr_b16 a[218:219], v239 offset:25696\n\t"
    "v_mfma_f32_16x16x32_bf16 v[116:119], a[220:223], a[24:27], v[116:119]\n\t"
    "s_and_b64 vcc, vcc, -1\n\t"
    "s_cbranch_scc1 Ls4_%=\n\t"
    "Lb5_%=:\n\t"
    "v_mfma_f32_16x16x32_bf16 v[108:111], a[220:223], a[20:23], v[108:111]\n\t"
    "buffer_load_dwordx4 v242, s[68:71], s35 offen offset:1024 sc1 lds\n\t"
    "v_mfma_f32_16x16x32_bf16 v[100:103], a[220:223], a[16:19], v[100:103]\n\t"
    "s_waitcnt lgkmcnt(12)\n\t"
    "ds_read_b64_tr_b16 a[220:221], v239 offset:24680\n\t"
    "v_mfma_f32_16x16x32_bf16 v[0:3], a[192:195], a[32:35], v[0:3]\n\t"
    "s_barrier\n\t"
    "v_mfma_f32_16x16x32_bf16 v[8:11], a[192:195], a[36:39], v[8:11]\n\t"
    "s_add_u32 m0, s31, 16448\n\t"
    "ds_read_b64_tr_b16 a[222:223], v239 offset:25704\n\t"
    "v_mfma_f32_16x16x32_bf16 v[16:19], a[192:195], a[40:43], v[16:19]\n\t"
    "s_mov_b32 s35, 3670016\n\t"
    "buffer_load_dwordx4 v241, s[68:71], s35 offen sc1 lds\n\t"
    "v_mfma_f32_16x16x32_bf16 v[24:27], a[192:195], a[44:47], v[24:27]\n\t"
    "s_waitcnt lgkmcnt(12)\n\t"
    "ds_read_b64_tr_b16 a[192:193], v236 offset:32896\n\t"
    "v_mfma_f32_16x16x32_bf16 v[28:31], a[196:199], a[44:47], v[28:31]\n\t"
    "ds_read_b64_tr_b16 a[194:195], v236 offset:33920\n\t"
    "v_and_b32 v231, 0x100, v230\n\t"
    "v_mfma_f32_16x16x32_bf16 v[20:23], a[196:199], a[40:43], v[20:23]\n\t"
    "v_cmp_lt_u32 vcc, 0, v231\n\t"
    "s_nop 1\n\t"
    "s_bcnt1_i32_b64 s6, vcc\n\t"
    "v_mfma_f32_16x16x32_bf16 v[12:15], a[196:199], a[36:39], v[12:15]\n\t"
    "v_and_b32 v230, 0xff, v230\n\t"
    "s_nop 1\n\t"
    "v_max_u32_dpp v230, v230, v230 quad_perm:[1,0,3,2] row_mask:0xf bank_mask:0xf\n\t"
    "v_mfma_f32_16x16x32_bf16 v[4:7], a[196:199], a[32:35], v[4:7]\n\t"
    "s_waitcnt lgkmcnt(12)\n\t"
    "ds_read_b64_tr_b16 a[196:197], v236 offset:32904\n\t"
    "v_mfma_f32_16x16x32_bf16 v[32:35], a[200:203], a[32:35], v[32:35]\n\t"
    "ds_read_b64_tr_b16 a[198:199], v236 offset:33928\n\t"
    "v_max_u32_dpp v230, v230, v230 quad_perm:[2,3,0,1] row_mask:0xf bank_mask:0xf\n\t"
    "v_mfma_f32_16x16x32_bf16 v[40:43], a[200:203], a[36:39], v[40:43]\n\t"
    "s_nop 0\n\t"
    "v_max_u32_dpp v230, v230, v230 row_half_mirror row_mask:0xf bank_mask:0xf\n\t"
    "s_nop 1\n\t"
    "v_max_u32_dpp v230, v230, v230 row_mirror row_mask:0xf bank_mask:0xf\n\t"
    "v_mfma_f32_16x16x32_bf16 v[48:51], a[200:203], a[40:43], v[48:51]\n\t"
    "buffer_load_dwordx4 v242, s[68:71], s35 offen offset:1024 sc1 lds\n\t"
    "v_mov_b32 v231, v230\n\t"
    "v_mfma_f32_16x16x32_bf16 v[56:59], a[200:203], a[44:47], v[56:59]\n\t"
    "s_waitcnt lgkmcnt(12)\n\t"
    "ds_read_b64_tr_b16 a[200:201], v237 offset:32896\n\t"
    "v_mfma_f32_16x16x32_bf16 v[60:63], a[204:207], a[44:47], v[60:63]\n\t"
    "ds_read_b64_tr_b16 a[202:203], v237 offset:33920\n\t"
    "v_permlane16_swap_b32 v230, v231\n\t"
    "v_mfma_f32_16x16x32_bf16 v[52:55], a[204:207], a[40:43], v[52:55]\n\t"
    "v_max_u32 v230, v230, v231\n\t"
    "v_mov_b32 v231, v230\n\t"
    "v_mfma_f32_16x16x32_bf16 v[44:47], a[204:207], a[36:39], v[44:47]\n\t"
    "s_mov_b32 s34, 114688\n\t"
    "buffer_load_dwordx4 a[96:99], v240, s[64:67], s34 offen offset:0 sc1\n\t"
    "v_mfma_f32_16x16x32_bf16 v[36:39], a[204:207], a[32:35], v[36:39]\n\t"
    "s_waitcnt lgkmcnt(12)\n\t"
    "ds_read_b64_tr_b16 a[204:205], v237 offset:32904\n\t"
    "v_mfma_f32_16x16x32_bf16 v[64:67], a[208:211], a[32:35], v[64:67]\n\t"
    "ds_read_b64_tr_b16 a[206:207], v237 offset:33928\n\t"
    "v_permlane32_swap_b32 v230, v231\n\t"
    "v_mfma_f32_16x16x32_bf16 v[72:75], a[208:211], a[36:39], v[72:75]\n\t"
    "v_max_u32 v230, v230, v231\n\t"
    "v_lshl_or_b32 v230, v230, 8, s6\n\t"
    "v_mfma_f32_16x16x32_bf16 v[80:83], a[208:211], a[40:43], v[80:83]\n\t"
    "s_lshl_b32 s38, s22, 2\n\t"
    "buffer_load_dwordx4 a[100:103], v240, s[64:67], s34 offen offset:1024 sc1\n\t"
    "v_mfma_f32_16x16x32_bf16 v[88:91], a[208:211], a[44:47], v[88:91]\n\t"
    "s_waitcnt lgkmcnt(12)\n\t"
    "ds_read_b64_tr_b16 a[208:209], v238 offset:32896\n\t"
    "v_mfma_f32_16x16x32_bf16 v[92:95], a[212:215], a[44:47], v[92:95]\n\t"
    "s_add_u32 s38, s38, 152336\n\t"
    "ds_read_b64_tr_b16 a[210:211], v238 offset:33920\n\t"
    "v_mfma_f32_16x16x32_bf16 v[84:87], a[212:215], a[40:43], v[84:87]\n\t"
    "v_mov_b32 v231, s38\n\t"
    "v_mfma_f32_16x16x32_bf16 v[76:79], a[212:215], a[36:39], v[76:79]\n\t"
    "ds_write_b32 v231, v230\n\t"
    "v_mfma_f32_16x16x32_bf16 v[68:71], a[212:215], a[32:35], v[68:71]\n\t"
    "s_waitcnt lgkmcnt(13)\n\t"
    "ds_read_b64_tr_b16 a[212:213], v238 offset:32904\n\t"
    "v_mfma_f32_16x16x32_bf16 v[96:99], a[216:219], a[32:35], v[96:99]\n\t"
    "ds_read_b64_tr_b16 a[214:215], v238 offset:33928\n\t"
    "v_mfma_f32_16x16x32_bf16 v[104:107], a[216:219], a[36:39], v[104:107]\n\t"
    "global_load_dwordx4 v[128:131], v227, s[42:43] sc1\n\t"
    "v_mfma_f32_16x16x32_bf16 v[112:115], a[216:219], a[40:43], v[112:115]\n\t"
    "buffer_load_dwordx4 a[104:107], v240, s[64:67], s34 offen offset:2048 sc1\n\t"
    "v_mfma_f32_16x16x32_bf16 v[120:123], a[216:219], a[44:47], v[120:123]\n\t"
    "s_waitcnt lgkmcnt(13)\n\t"
    "ds_read_b64_tr_b16 a[216:217], v239 offset:32896\n\t"
    "v_mfma_f32_16x16x32_bf16 v[124:127], a[220:223], a[44:47], v[124:127]\n\t"
    "ds_read_b64_tr_b16 a[218:219], v239 offset:33920\n\t"
    "v_mfma_f32_16x16x32_bf16 v[116:119], a[220:223], a[40:43], v[116:119]\n\t"
    "s_add_u32 s42, s42, 0x200000\n\t"
    "s_addc_u32 s43, s43, 0\n\t"
    "v_mfma_f32_16x16x32_bf16 v[108:111], a[220:223], a[36:39], v[108:111]\n\t"
    "buffer_load_dwordx4 a[108:111], v240, s[64:67], s34 offen offset:3072 sc1\n\t"
    "v_mfma_f32_16x16x32_bf16 v[100:103], a[220:223], a[32:35], v[100:103]\n\t"
    "s_waitcnt lgkmcnt(13)\n\t"
    "ds_read_b64_tr_b16 a[220:221], v239 offset:32904\n\t"
    "v_mfma_f32_16x16x32_bf16 v[0:3], a[192:195], a[48:51], v[0:3]\n\t"
    "ds_read_b64_tr_b16 a[222:223], v239 offset:33928\n\t"
    "v_mfma_f32_16x16x32_bf16 v[8:11], a[192:195], a[52:55], v[8:11]\n\t"
    "global_load_dwordx4 v[132:135], v227, s[42:43] sc1\n\t"
    "v_mfma_f32_16x16x32_bf16 v[16:19], a[192:195], a[56:59], v[16:19]\n\t"
    "s_mov_b32 s34, 118784\n\t"
    "buffer_load_dwordx4 a[112:115], v240, s[64:67], s34 offen offset:0 sc1\n\t"
    "v_mfma_f32_16x16x32_bf16 v[24:27], a[192:195], a[60:63], v[24:27]\n\t"
    "s_waitcnt lgkmcnt(13)\n\t"
    "ds_read_b64_tr_b16 a[192:193], v236 offset:41120\n\t"
    "v_mfma_f32_16x16x32_bf16 v[28:31], a[196:199], a[60:63], v[28:31]\n\t"
    "ds_read_b64_tr_b16 a[194:195], v236 offset:42144\n\t"
    "v_mfma_f32_16x16x32_bf16 v[20:23], a[196:199], a[56:59], v[20:23]\n\t"
    "v_mfma_f32_16x16x32_bf16 v[12:15], a[196:199], a[52:55], v[12:15]\n\t"
    "v_mfma_f32_16x16x32_bf16 v[4:7], a[196:199], a[48:51], v[4:7]\n\t"
    "s_waitcnt lgkmcnt(13)\n\t"
    "ds_read_b64_tr_b16 a[196:197], v236 offset:41128\n\t"
    "v_mfma_f32_16x16x32_bf16 v[32:35], a[200:203], a[48:51], v[32:35]\n\t"
    "ds_read_b64_tr_b16 a[198:199], v236 offset:42152\n\t"
    "v_mfma_f32_16x16x32_bf16 v[40:43], a[200:203], a[52:55], v[40:43]\n\t"
    "Lpad3_%=:\n\t"
    "s_cbranch_execz LretA_%=\n\t"
    "v_mfma_f32_16x16x32_bf16 v[48:51], a[200:203], a[56:59], v[48:51]\n\t"
    "buffer_load_dwordx4 a[116:119], v240, s[64:67], s34 offen offset:1024 sc1\n\t"
    "v_mfma_f32_16x16x32_bf16 v[56:59], a[200:203], a[60:63], v[56:59]\n\t"
    "s_waitcnt lgkmcnt(13)\n\t"
    "ds_read_b64_tr_b16 a[200:201], v237 offset:41120\n\t"
    "v_mfma_f32_16x16x32_bf16 v[60:63], a[204:207], a[60:63], v[60:63]\n\t"
    "ds_read_b64_tr_b16 a[202:203], v237 offset:42144\n\t"
    "v_mfma_f32_16x16x32_bf16 v[52:55], a[204:207], a[56:59], v[52:55]\n\t"
    "v_mfma_f32_16x16x32_bf16 v[44:47], a[204:207], a[52:55], v[44:47]\n\t"
    "buffer_load_dwordx4 a[120:123], v240, s[64:67], s34 offen offset:2048 sc1\n\t"
    "v_mfma_f32_16x16x32_bf16 v[36:39], a[204:207], a[48:51], v[36:39]\n\t"
    "s_waitcnt lgkmcnt(13)\n\t"
    "ds_read_b64_tr_b16 a[204:205], v237 offset:41128\n\t"
    "v_mfma_f32_16x16x32_bf16 v[64:67], a[208:211], a[48:51], v[64:67]\n\t"
    "ds_read_b64_tr_b16 a[206:207], v237 offset:42152\n\t"
    "v_mfma_f32_16x16x32_bf16 v[72:75], a[208:211], a[52:55], v[72:75]\n\t"
    "s_add_u32 s42, s42, 0x200000\n\t"
    "s_addc_u32 s43, s43, 0\n\t"
    "v_mfma_f32_16x16x32_bf16 v[80:83], a[208:211], a[56:59], v[80:83]\n\t"
    "buffer_load_dwordx4 a[124:127], v240, s[64:67], s34 offen offset:3072 sc1\n\t"
    "v_mfma_f32_16x16x32_bf16 v[88:91], a[208:211], a[60:63], v[88:91]\n\t"
    "s_waitcnt lgkmcnt(12)\n\t"
    "ds_read_b64_tr_b16 a[208:209], v238 offset:41120\n\t"
    "v_mfma_f32_16x16x32_bf16 v[92:95], a[212:215], a[60:63], v[92:95]\n\t"
    "ds_read_b64_tr_b16 a[210:211], v238 offset:42144\n\t"
    "v_mfma_f32_16x16x32_bf16 v[84:87], a[212:215], a[56:59], v[84:87]\n\t"
    "v_mfma_f32_16x16x32_bf16 v[76:79], a[212:215], a[52:55], v[76:79]\n\t"
    "v_mfma_f32_16x16x32_bf16 v[68:71], a[212:215], a[48:51], v[68:71]\n\t"
    "s_waitcnt lgkmcnt(12)\n\t"
    "ds_read_b64_tr_b16 a[212:213], v238 offset:41128\n\t"
    "v_mfma_f32_16x16x32_bf16 v[96:99], a[216:219], a[48:51], v[96:99]\n\t"
    "ds_read_b64_tr_b16 a[214:215], v238 offset:42152\n\t"
    "v_mfma_f32_16x16x32_bf16 v[104:107], a[216:219], a[52:55], v[104:107]\n\t"
    "s_add_u32 m0, s31, 24672\n\t"
    "v_mfma_f32_16x16x32_bf16 v[112:115], a[216:219], a[56:59], v[112:115]\n\t"
    "s_mov_b32 s35, 3899392\n\t"
    "buffer_load_dwordx4 v241, s[68:71], s35 offen sc1 lds\n\t"
    "v_mfma_f32_16x16x32_bf16 v[120:123], a[216:219], a[60:63], v[120:123]\n\t"
    "s_waitcnt lgkmcnt(12)\n\t"
    "ds_read_b64_tr_b16 a[216:217], v239 offset:41120\n\t"
    "v_mfma_f32_16x16x32_bf16 v[124:127], a[220:223], a[60:63], v[124:127]\n\t"
    "ds_read_b64_tr_b16 a[218:219], v239 offset:42144\n\t"
    "v_mfma_f32_16x16x32_bf16 v[116:119], a[220:223], a[56:59], v[116:119]\n\t"
    "v_mfma_f32_16x16x32_bf16 v[108:111], a[220:223], a[52:55], v[108:111]\n\t"
    "buffer_load_dwordx4 v242, s[68:71], s35 offen offset:1024 sc1 lds\n\t"
    "v_mfma_f32_16x16x32_bf16 v[100:103], a[220:223], a[48:51], v[100:103]\n\t"
    "s_waitcnt vmcnt(20) lgkmcnt(12)\n\t"
    "ds_read_b64_tr_b16 a[220:221], v239 offset:41128\n\t"
    "v_mfma_f32_16x16x32_bf16 v[0:3], a[192:195], a[64:67], v[0:3]\n\t"
    "s_barrier\n\t"
    "v_mfma_f32_16x16x32_bf16 v[8:11], a[192:195], a[68:71], v[8:11]\n\t"
    "s_add_u32 m0, s31, 32896\n\t"
    "ds_read_b64_tr_b16 a[222:223], v239 offset:42152\n\t"
    "v_mfma_f32_16x16x32_bf16 v[16:19], a[192:195], a[72:75], v[16:19]\n\t"
    "s_mov_b32 s35, 4128768\n\t"
    "buffer_load_dwordx4 v241, s[68:71], s35 offen sc1 lds\n\t"
    "v_mfma_f32_16x16x32_bf16 v[24:27], a[192:195], a[76:79], v[24:27]\n\t"
    "s_waitcnt lgkmcnt(12)\n\t"
    "ds_read_b64_tr_b16 a[192:193], v236 offset:49344\n\t"
    "v_mfma_f32_16x16x32_bf16 v[28:31], a[196:199], a[76:79], v[28:31]\n\t"
    "ds_read_b64_tr_b16 a[194:195], v236 offset:50368\n\t"
    "v_mfma_f32_16x16x32_bf16 v[20:23], a[196:199], a[72:75], v[20:23]\n\t"
    "global_load_dwordx4 v[136:139], v227, s[42:43] sc1\n\t"
    "v_mfma_f32_16x16x32_bf16 v[12:15], a[196:199], a[68:71], v[12:15]\n\t"
    "v_mfma_f32_16x16x32_bf16 v[4:7], a[196:199], a[64:67], v[4:7]\n\t"
    "s_waitcnt lgkmcnt(12)\n\t"
    "ds_read_b64_tr_b16 a[196:197], v236 offset:49352\n\t"
    "v_mfma_f32_16x16x32_bf16 v[32:35], a[200:203], a[64:67], v[32:35]\n\t"
    "ds_read_b64_tr_b16 a[198:199], v236 offset:50376\n\t"
    "v_mfma_f32_16x16x32_bf16 v[40:43], a[200:203], a[68:71], v[40:43]\n\t"
    "v_mfma_f32_16x16x32_bf16 v[48:51], a[200:203], a[72:75], v[48:51]\n\t"
    "buffer_load_dwordx4 v242, s[68:71], s35 offen offset:1024 sc1 lds\n\t"
    "v_mfma_f32_16x16x32_bf16 v[56:59], a[200:203], a[76:79], v[56:59]\n\t"
    "s_waitcnt lgkmcnt(12)\n\t"
    "ds_read_b64_tr_b16 a[200:201], v237 offset:49344\n\t"
    "v_mfma_f32_16x16x32_bf16 v[60:63], a[204:207], a[76:79], v[60:63]\n\t"
    "ds_read_b64_tr_b16 a[202:203], v237 offset:50368\n\t"
    "v_mfma_f32_16x16x32_bf16 v[52:55], a[204:207], a[72:75], v[52:55]\n\t"
    "v_mfma_f32_16x16x32_bf16 v[44:47], a[204:207], a[68:71], v[44:47]\n\t"
    "s_mov_b32 s34, 131072\n\t"
    "buffer_load_dwordx4 a[0:3], v240, s[64:67], s34 offen offset:0 sc1\n\t"
    "v_mfma_f32_16x16x32_bf16 v[36:39], a[204:207], a[64:67], v[36:39]\n\t"
    "s_waitcnt lgkmcnt(12)\n\t"
    "ds_read_b64_tr_b16 a[204:205], v237 offset:49352\n\t"
    "v_mfma_f32_16x16x32_bf16 v[64:67], a[208:211], a[64:67], v[64:67]\n\t"
    "ds_read_b64_tr_b16 a[206:207], v237 offset:50376\n\t"
    "v_mfma_f32_16x16x32_bf16 v[72:75], a[208:211], a[68:71], v[72:75]\n\t"
    "s_add_u32 s42, s42, 0x200000\n\t"
    "s_addc_u32 s43, s43, 0\n\t"
    "v_mfma_f32_16x16x32_bf16 v[80:83], a[208:211], a[72:75], v[80:83]\n\t"
    "buffer_load_dwordx4 a[4:7], v240, s[64:67], s34 offen offset:1024 sc1\n\t"
    "v_mfma_f32_16x16x32_bf16 v[88:91], a[208:211], a[76:79], v[88:91]\n\t"
    "s_waitcnt lgkmcnt(12)\n\t"
    "ds_read_b64_tr_b16 a[208:209], v238 offset:49344\n\t"
    "v_mfma_f32_16x16x32_bf16 v[92:95], a[212:215], a[76:79], v[92:95]\n\t"
    "ds_read_b64_tr_b16 a[210:211], v238 offset:50368\n\t"
    "v_mfma_f32_16x16x32_bf16 v[84:87], a[212:215], a[72:75], v[84:87]\n\t"
    "v_mfma_f32_16x16x32_bf16 v[76:79], a[212:215], a[68:71], v[76:79]\n\t"
    "v_mfma_f32_16x16x32_bf16 v[68:71], a[212:215], a[64:67], v[68:71]\n\t"
    "s_waitcnt lgkmcnt(12)\n\t"
    "ds_read_b64_tr_b16 a[212:213], v238 offset:49352\n\t"
    "v_mfma_f32_16x16x32_bf16 v[96:99], a[216:219], a[64:67], v[96:99]\n\t"
    "ds_read_b64_tr_b16 a[214:215], v238 offset:50376\n\t"
    "v_mfma_f32_16x16x32_bf16 v[104:107], a[216:219], a[68:71], v[104:107]\n\t"
    "v_mfma_f32_16x16x32_bf16 v[112:115], a[216:219], a[72:75], v[112:115]\n\t"
    "buffer_load_dwordx4 a[8:11], v240, s[64:67], s34 offen offset:2048 sc1\n\t"
    "v_mfma_f32_16x16x32_bf16 v[120:123], a[216:219], a[76:79], v[120:123]\n\t"
    "s_waitcnt lgkmcnt(12)\n\t"
    "ds_read_b64_tr_b16 a[216:217], v239 offset:49344\n\t"
    "v_mfma_f32_16x16x32_bf16 v[124:127], a[220:223], a[76:79], v[124:127]\n\t"
    "ds_read_b64_tr_b16 a[218:219], v239 offset:50368\n\t"
    "v_mfma_f32_16x16x32_bf16 v[116:119], a[220:223], a[72:75], v[116:119]\n\t"
    "v_mfma_f32_16x16x32_bf16 v[108:111], a[220:223], a[68:71], v[108:111]\n\t"
    "buffer_load_dwordx4 a[12:15], v240, s[64:67], s34 offen offset:3072 sc1\n\t"
    "v_mfma_f32_16x16x32_bf16 v[100:103], a[220:223], a[64:67], v[100:103]\n\t"
    "s_waitcnt vmcnt(23) lgkmcnt(12)\n\t"
    "ds_read_b64_tr_b16 a[220:221], v239 offset:49352\n\t"
    "v_mfma_f32_16x16x32_bf16 v[0:3], a[192:195], a[80:83], v[0:3]\n\t"
    "ds_read_b64_tr_b16 a[222:223], v239 offset:50376\n\t"
    "v_mfma_f32_16x16x32_bf16 v[8:11], a[192:195], a[84:87], v[8:11]\n\t"
    "global_load_dwordx4 v[140:143], v227, s[42:43] sc1\n\t"
    "v_mfma_f32_16x16x32_bf16 v[16:19], a[192:195], a[88:91], v[16:19]\n\t"
    "s_mov_b32 s34, 135168\n\t"
    "buffer_load_dwordx4 a[16:19], v240, s[64:67], s34 offen offset:0 sc1\n\t"
    "v_mfma_f32_16x16x32_bf16 v[24:27], a[192:195], a[92:95], v[24:27]\n\t"
    "s_waitcnt lgkmcnt(12)\n\t"
    "ds_read_b64_tr_b16 a[192:193], v232 offset:0\n\t"
    "v_mfma_f32_16x16x32_bf16 v[28:31], a[196:199], a[92:95], v[28:31]\n\t"
    "ds_read_b64_tr_b16 a[194:195], v232 offset:1024\n\t"
    "v_mfma_f32_16x16x32_bf16 v[20:23], a[196:199], a[88:91], v[20:23]\n\t"
    "s_sub_u32 s42, s42, 0x5f8000\n\t"
    "s_subb_u32 s43, s43, 0\n\t"
    "v_mfma_f32_16x16x32_bf16 v[12:15], a[196:199], a[84:87], v[12:15]\n\t"
    "v_mfma_f32_16x16x32_bf16 v[4:7], a[196:199], a[80:83], v[4:7]\n\t"
    "s_waitcnt lgkmcnt(12)\n\t"
    "ds_read_b64_tr_b16 a[196:197], v232 offset:8\n\t"
    "v_mfma_f32_16x16x32_bf16 v[32:35], a[200:203], a[80:83], v[32:35]\n\t"
    "ds_read_b64_tr_b16 a[198:199], v232 offset:1032\n\t"
    "v_mfma_f32_16x16x32_bf16 v[40:43], a[200:203], a[84:87], v[40:43]\n\t"
    "v_mfma_f32_16x16x32_bf16 v[48:51], a[200:203], a[88:91], v[48:51]\n\t"
    "buffer_load_dwordx4 a[20:23], v240, s[64:67], s34 offen offset:1024 sc1\n\t"
    "v_mfma_f32_16x16x32_bf16 v[56:59], a[200:203], a[92:95], v[56:59]\n\t"
    "s_waitcnt lgkmcnt(12)\n\t"
    "ds_read_b64_tr_b16 a[200:201], v233 offset:0\n\t"
    "v_mfma_f32_16x16x32_bf16 v[60:63], a[204:207], a[92:95], v[60:63]\n\t"
    "ds_read_b64_tr_b16 a[202:203], v233 offset:1024\n\t"
    "v_mfma_f32_16x16x32_bf16 v[52:55], a[204:207], a[88:91], v[52:55]\n\t"
    "s_waitcnt vmcnt(19)\n\t"
    "ds_write_b32 v228, v128 offset:0\n\t"
    "v_mfma_f32_16x16x32_bf16 v[44:47], a[204:207], a[84:87], v[44:47]\n\t"
    "buffer_load_dwordx4 a[24:27], v240, s[64:67], s34 offen offset:2048 sc1\n\t"
    "v_mfma_f32_16x16x32_bf16 v[36:39], a[204:207], a[80:83], v[36:39]\n\t"
    "s_waitcnt lgkmcnt(13)\n\t"
    "ds_read_b64_tr_b16 a[204:205], v233 offset:8\n\t"
    "v_mfma_f32_16x16x32_bf16 v[64:67], a[208:211], a[80:83], v[64:67]\n\t"
    "ds_read_b64_tr_b16 a[206:207], v233 offset:1032\n\t"
    "v_mfma_f32_16x16x32_bf16 v[72:75], a[208:211], a[84:87], v[72:75]\n\t"
    "ds_write_b32 v228, v129 offset:4608\n\t"
    "v_mfma_f32_16x16x32_bf16 v[80:83], a[208:211], a[88:91], v[80:83]\n\t"
    "buffer_load_dwordx4 a[28:31], v240, s[64:67], s34 offen offset:3072 sc1\n\t"
    "v_mfma_f32_16x16x32_bf16 v[88:91], a[208:211], a[92:95], v[88:91]\n\t"
    "s_waitcnt lgkmcnt(14)\n\t"
    "ds_read_b64_tr_b16 a[208:209], v234 offset:0\n\t"
    "v_mfma_f32_16x16x32_bf16 v[92:95], a[212:215], a[92:95], v[92:95]\n\t"
    "ds_read_b64_tr_b16 a[210:211], v234 offset:1024\n\t"
    "v_mfma_f32_16x16x32_bf16 v[84:87], a[212:215], a[88:91], v[84:87]\n\t"
    "ds_write_b32 v228, v130 offset:9216\n\t"
    "v_mfma_f32_16x16x32_bf16 v[76:79], a[212:215], a[84:87], v[76:79]\n\t"
    "v_mfma_f32_16x16x32_bf16 v[68:71], a[212:215], a[80:83], v[68:71]\n\t"
    "s_waitcnt lgkmcnt(15)\n\t"
    "ds_read_b64_tr_b16 a[212:213], v234 offset:8\n\t"
    "v_mfma_f32_16x16x32_bf16 v[96:99], a[216:219], a[80:83], v[96:99]\n\t"
    "ds_read_b64_tr_b16 a[214:215], v234 offset:1032\n\t"
    "v_mfma_f32_16x16x32_bf16 v[104:107], a[216:219], a[84:87], v[104:107]\n\t"
    "s_add_u32 m0, s31, 41120\n\t"
    "ds_write_b32 v228, v131 offset:13824\n\t"
    "v_mfma_f32_16x16x32_bf16 v[112:115], a[216:219], a[88:91], v[112:115]\n\t"
    "s_mov_b32 s35, 4358144\n\t"
    "buffer_load_dwordx4 v241, s[68:71], s35 offen sc1 lds\n\t"
    "v_mfma_f32_16x16x32_bf16 v[120:123], a[216:219], a[92:95], v[120:123]\n\t"
    "s_waitcnt lgkmcnt(15)\n\t"
    "ds_read_b64_tr_b16 a[216:217], v235 offset:0\n\t"
    "v_mfma_f32_16x16x32_bf16 v[124:127], a[220:223], a[92:95], v[124:127]\n\t"
    "ds_read_b64_tr_b16 a[218:219], v235 offset:1024\n\t"
    "v_mfma_f32_16x16x32_bf16 v[116:119], a[220:223], a[88:91], v[116:119]\n\t"
    "v_mfma_f32_16x16x32_bf16 v[108:111], a[220:223], a[84:87], v[108:111]\n\t"
    "buffer_load_dwordx4 v242, s[68:71], s35 offen offset:1024 sc1 lds\n\t"
    "v_mfma_f32_16x16x32_bf16 v[100:103], a[220:223], a[80:83], v[100:103]\n\t"
    "s_waitcnt vmcnt(21) lgkmcnt(15)\n\t"
    "ds_read_b64_tr_b16 a[220:221], v235 offset:8\n\t"
    "v_mfma_f32_16x16x32_bf16 v[0:3], a[192:195], a[96:99], v[0:3]\n\t"
    "s_barrier\n\t"
    "v_mfma_f32_16x16x32_bf16 v[8:11], a[192:195], a[100:103], v[8:11]\n\t"
    "s_add_u32 m0, s31, 49344\n\t"
    "ds_read_b64_tr_b16 a[222:223], v235 offset:1032\n\t"
    "v_mfma_f32_16x16x32_bf16 v[16:19], a[192:195], a[104:107], v[16:19]\n\t"
    "s_mov_b32 s35, 4587520\n\t"
    "buffer_load_dwordx4 v241, s[68:71], s35 offen sc1 lds\n\t"
    "v_mfma_f32_16x16x32_bf16 v[24:27], a[192:195], a[108:111], v[24:27]\n\t"
    "s_waitcnt lgkmcnt(15)\n\t"
    "ds_read_b64_tr_b16 a[192:193], v232 offset:8224\n\t"
    "v_mfma_f32_16x16x32_bf16 v[28:31], a[196:199], a[108:111], v[28:31]\n\t"
    "ds_read_b64_tr_b16 a[194:195], v232 offset:9248\n\t"
    "v_mfma_f32_16x16x32_bf16 v[20:23], a[196:199], a[104:107], v[20:23]\n\t"
    "global_load_dwordx4 v[144:147], v227, s[42:43] sc1\n\t"
    "v_mfma_f32_16x16x32_bf16 v[12:15], a[196:199], a[100:103], v[12:15]\n\t"
    "s_add_u32 s42, s42, 0x200000\n\t"
    "s_addc_u32 s43, s43, 0\n\t"
    "v_mfma_f32_16x16x32_bf16 v[4:7], a[196:199], a[96:99], v[4:7]\n\t"
    "s_waitcnt lgkmcnt(15)\n\t"
    "ds_read_b64_tr_b16 a[196:197], v232 offset:8232\n\t"
    "v_mfma_f32_16x16x32_bf16 v[32:35], a[200:203], a[96:99], v[32:35]\n\t"
    "ds_read_b64_tr_b16 a[198:199], v232 offset:9256\n\t"
    "v_mfma_f32_16x16x32_bf16 v[40:43], a[200:203], a[100:103], v[40:43]\n\t"
    "v_mfma_f32_16x16x32_bf16 v[48:51], a[200:203], a[104:107], v[48:51]\n\t"
    "buffer_load_dwordx4 v242, s[68:71], s35 offen offset:1024 sc1 lds\n\t"
    "v_mfma_f32_16x16x32_bf16 v[56:59], a[200:203], a[108:111], v[56:59]\n\t"
    "s_waitcnt lgkmcnt(15)\n\t"
    "ds_read_b64_tr_b16 a[200:201], v233 offset:8224\n\t"
    "v_mfma_f32_16x16x32_bf16 v[60:63], a[204:207], a[108:111], v[60:63]\n\t"
    "ds_read_b64_tr_b16 a[202:203], v233 offset:9248\n\t"
    "v_mfma_f32_16x16x32_bf16 v[52:55], a[204:207], a[104:107], v[52:55]\n\t"
    "s_waitcnt vmcnt(23)\n\t"
    "ds_write_b32 v228, v132 offset:32\n\t"
    "v_mfma_f32_16x16x32_bf16 v[44:47], a[204:207], a[100:103], v[44:47]\n\t"
    "s_mov_b32 s34, 147456\n\t"
    "buffer_load_dwordx4 a[32:35], v240, s[64:67], s34 offen offset:0 sc1\n\t"
    "v_mfma_f32_16x16x32_bf16 v[36:39], a[204:207], a[96:99], v[36:39]\n\t"
    "s_waitcnt lgkmcnt(15)\n\t"
    "ds_read_b64_tr_b16 a[204:205], v233 offset:8232\n\t"
    "v_mfma_f32_16x16x32_bf16 v[64:67], a[208:211], a[96:99], v[64:67]\n\t"
    "ds_read_b64_tr_b16 a[206:207], v233 offset:9256\n\t"
    "v_mfma_f32_16x16x32_bf16 v[72:75], a[208:211], a[100:103], v[72:75]\n\t"
    "ds_write_b32 v228, v133 offset:4640\n\t"
    "v_mfma_f32_16x16x32_bf16 v[80:83], a[208:211], a[104:107], v[80:83]\n\t"
    "buffer_load_dwordx4 a[36:39], v240, s[64:67], s34 offen offset:1024 sc1\n\t"
    "v_mfma_f32_16x16x32_bf16 v[88:91], a[208:211], a[108:111], v[88:91]\n\t"
    "s_waitcnt lgkmcnt(15)\n\t"
    "ds_read_b64_tr_b16 a[208:209], v234 offset:8224\n\t"
    "v_mfma_f32_16x16x32_bf16 v[92:95], a[212:215], a[108:111], v[92:95]\n\t"
    "ds_read_b64_tr_b16 a[210:211], v234 offset:9248\n\t"
    "v_mfma_f32_16x16x32_bf16 v[84:87], a[212:215], a[104:107], v[84:87]\n\t"
    "ds_write_b32 v228, v134 offset:9248\n\t"
    "v_mfma_f32_16x16x32_bf16 v[76:79], a[212:215], a[100:103], v[76:79]\n\t"
    "v_mfma_f32_16x16x32_bf16 v[68:71], a[212:215], a[96:99], v[68:71]\n\t"
    "s_waitcnt lgkmcnt(15)\n\t"
    "ds_read_b64_tr_b16 a[212:213], v234 offset:8232\n\t"
    "v_mfma_f32_16x16x32_bf16 v[96:99], a[216:219], a[96:99], v[96:99]\n\t"
    "ds_read_b64_tr_b16 a[214:215], v234 offset:9256\n\t"
    "v_mfma_f32_16x16x32_bf16 v[104:107], a[216:219], a[100:103], v[104:107]\n\t"
    "ds_write_b32 v228, v135 offset:13856\n\t"
    "v_mfma_f32_16x16x32_bf16 v[112:115], a[216:219], a[104:107], v[112:115]\n\t"
    "buffer_load_dwordx4 a[40:43], v240, s[64:67], s34 offen offset:2048 sc1\n\t"
    "v_mfma_f32_16x16x32_bf16 v[120:123], a[216:219], a[108:111], v[120:123]\n\t"
    "s_waitcnt lgkmcnt(15)\n\t"
    "ds_read_b64_tr_b16 a[216:217], v235 offset:8224\n\t"
    "v_mfma_f32_16x16x32_bf16 v[124:127], a[220:223], a[108:111], v[124:127]\n\t"
    "ds_read_b64_tr_b16 a[218:219], v235 offset:9248\n\t"
    "v_mfma_f32_16x16x32_bf16 v[116:119], a[220:223], a[104:107], v[116:119]\n\t"
    "v_mfma_f32_16x16x32_bf16 v[108:111], a[220:223], a[100:103], v[108:111]\n\t"
    "buffer_load_dwordx4 a[44:47], v240, s[64:67], s34 offen offset:3072 sc1\n\t"
    "v_mfma_f32_16x16x32_bf16 v[100:103], a[220:223], a[96:99], v[100:103]\n\t"
    "s_waitcnt vmcnt(23) lgkmcnt(15)\n\t"
    "ds_read_b64_tr_b16 a[220:221], v235 offset:8232\n\t"
    "v_mfma_f32_16x16x32_bf16 v[0:3], a[192:195], a[112:115], v[0:3]\n\t"
    "ds_read_b64_tr_b16 a[222:223], v235 offset:9256\n\t"
    "v_mfma_f32_16x16x32_bf16 v[8:11], a[192:195], a[116:119], v[8:11]\n\t"
    "global_load_dwordx4 v[148:151], v227, s[42:43] sc1\n\t"
    "v_mfma_f32_16x16x32_bf16 v[16:19], a[192:195], a[120:123], v[16:19]\n\t"
    "s_mov_b32 s34, 151552\n\t"
    "buffer_load_dwordx4 a[48:51], v240, s[64:67], s34 offen offset:0 sc1\n\t"
    "v_mfma_f32_16x16x32_bf16 v[24:27], a[192:195], a[124:127], v[24:27]\n\t"
    "s_waitcnt lgkmcnt(15)\n\t"
    "ds_read_b64_tr_b16 a[192:193], v232 offset:16448\n\t"
    "v_mfma_f32_16x16x32_bf16 v[28:31], a[196:199], a[124:127], v[28:31]\n\t"
    "ds_read_b64_tr_b16 a[194:195], v232 offset:17472\n\t"
    "v_mfma_f32_16x16x32_bf16 v[20:23], a[196:199], a[120:123], v[20:23]\n\t"
    "s_add_u32 s42, s42, 0x200000\n\t"
    "s_addc_u32 s43, s43, 0\n\t"
    "v_mfma_f32_16x16x32_bf16 v[12:15], a[196:199], a[116:119], v[12:15]\n\t"
    "v_mfma_f32_16x16x32_bf16 v[4:7], a[196:199], a[112:115], v[4:7]\n\t"
    "s_waitcnt lgkmcnt(15)\n\t"
    "ds_read_b64_tr_b16 a[196:197], v232 offset:16456\n\t"
    "v_mfma_f32_16x16x32_bf16 v[32:35], a[200:203], a[112:115], v[32:35]\n\t"
    "ds_read_b64_tr_b16 a[198:199], v232 offset:17480\n\t"
    "v_mfma_f32_16x16x32_bf16 v[40:43], a[200:203], a[116:119], v[40:43]\n\t"
    "v_mfma_f32_16x16x32_bf16 v[48:51], a[200:203], a[120:123], v[48:51]\n\t"
    "buffer_load_dwordx4 a[52:55], v240, s[64:67], s34 offen offset:1024 sc1\n\t"
    "v_mfma_f32_16x16x32_bf16 v[56:59], a[200:203], a[124:127], v[56:59]\n\t"
    "s_waitcnt lgkmcnt(15)\n\t"
    "ds_read_b64_tr_b16 a[200:201], v233 offset:16448\n\t"
    "v_mfma_f32_16x16x32_bf16 v[60:63], a[204:207], a[124:127], v[60:63]\n\t"
    "ds_read_b64_tr_b16 a[202:203], v233 offset:17472\n\t"
    "v_mfma_f32_16x16x32_bf16 v[52:55], a[204:207], a[120:123], v[52:55]\n\t"
    "s_waitcnt vmcnt(22)\n\t"
    "ds_write_b32 v228, v136 offset:64\n\t"
    "v_mfma_f32_16x16x32_bf16 v[44:47], a[204:207], a[116:119], v[44:47]\n\t"
    "buffer_load_dwordx4 a[56:59], v240, s[64:67], s34 offen offset:2048 sc1\n\t"
    "v_mfma_f32_16x16x32_bf16 v[36:39], a[204:207], a[112:115], v[36:39]\n\t"
    "s_waitcnt lgkmcnt(15)\n\t"
    "ds_read_b64_tr_b16 a[204:205], v233 offset:16456\n\t"
    "v_mfma_f32_16x16x32_bf16 v[64:67], a[208:211], a[112:115], v[64:67]\n\t"
    "ds_read_b64_tr_b16 a[206:207], v233 offset:17480\n\t"
    "v_mfma_f32_16x16x32_bf16 v[72:75], a[208:211], a[116:119], v[72:75]\n\t"
    "ds_write_b32 v228, v137 offset:4672\n\t"
    "v_mfma_f32_16x16x32_bf16 v[80:83], a[208:211], a[120:123], v[80:83]\n\t"
    "buffer_load_dwordx4 a[60:63], v240, s[64:67], s34 offen offset:3072 sc1\n\t"
    "v_mfma_f32_16x16x32_bf16 v[88:91], a[208:211], a[124:127], v[88:91]\n\t"
    "s_waitcnt lgkmcnt(15)\n\t"
    "ds_read_b64_tr_b16 a[208:209], v234 offset:16448\n\t"
    "v_mfma_f32_16x16x32_bf16 v[92:95], a[212:215], a[124:127], v[92:95]\n\t"
    "ds_read_b64_tr_b16 a[210:211], v234 offset:17472\n\t"
    "v_mfma_f32_16x16x32_bf16 v[84:87], a[212:215], a[120:123], v[84:87]\n\t"
    "ds_write_b32 v228, v138 offset:9280\n\t"
    "v_mfma_f32_16x16x32_bf16 v[76:79], a[212:215], a[116:119], v[76:79]\n\t"
    "v_mfma_f32_16x16x32_bf16 v[68:71], a[212:215], a[112:115], v[68:71]\n\t"
    "s_waitcnt lgkmcnt(15)\n\t"
    "ds_read_b64_tr_b16 a[212:213], v234 offset:16456\n\t"
    "v_mfma_f32_16x16x32_bf16 v[96:99], a[216:219], a[112:115], v[96:99]\n\t"
    "ds_read_b64_tr_b16 a[214:215], v234 offset:17480\n\t"
    "v_mfma_f32_16x16x32_bf16 v[104:107], a[216:219], a[116:119], v[104:107]\n\t"
    "s_add_u32 m0, s31, 57568\n\t"
    "ds_write_b32 v228, v139 offset:13888\n\t"
    "v_mfma_f32_16x16x32_bf16 v[112:115], a[216:219], a[120:123], v[112:115]\n\t"
    "s_mov_b32 s35, 4816896\n\t"
    "buffer_load_dwordx4 v241, s[68:71], s35 offen sc1 lds\n\t"
    "v_mfma_f32_16x16x32_bf16 v[120:123], a[216:219], a[124:127], v[120:123]\n\t"
    "s_waitcnt lgkmcnt(15)\n\t"
    "ds_read_b64_tr_b16 a[216:217], v235 offset:16448\n\t"
    "v_mfma_f32_16x16x32_bf16 v[124:127], a[220:223], a[124:127], v[124:127]\n\t"
    "ds_read_b64_tr_b16 a[218:219], v235 offset:17472\n\t"
    "v_mfma_f32_16x16x32_bf16 v[116:119], a[220:223], a[120:123], v[116:119]\n\t"
    "v_mfma_f32_16x16x32_bf16 v[108:111], a[220:223], a[116:119], v[108:111]\n\t"
    "buffer_load_dwordx4 v242, s[68:71], s35 offen offset:1024 sc1 lds\n\t"
    "v_mfma_f32_16x16x32_bf16 v[100:103], a[220:223], a[112:115], v[100:103]\n\t"
    "s_waitcnt vmcnt(21) lgkmcnt(15)\n\t"
    "ds_read_b64_tr_b16 a[220:221], v235 offset:16456\n\t"
    "v_mfma_f32_16x16x32_bf16 v[0:3], a[192:195], a[0:3], v[0:3]\n\t"
    "s_barrier\n\t"
    "v_mfma_f32_16x16x32_bf16 v[8:11], a[192:195], a[4:7], v[8:11]\n\t"
    "s_add_u32 m0, s31, 65792\n\t"
    "ds_read_b64_tr_b16 a[222:223], v235 offset:17480\n\t"
    "v_mfma_f32_16x16x32_bf16 v[16:19], a[192:195], a[8:11], v[16:19]\n\t"
    "s_mov_b32 s35, 5046272\n\t"
    "buffer_load_dwordx4 v241, s[68:71], s35 offen sc1 lds\n\t"
    "v_mfma_f32_16x16x32_bf16 v[24:27], a[192:195], a[12:15], v[24:27]\n\t"
    "s_waitcnt lgkmcnt(15)\n\t"
    "ds_read_b64_tr_b16 a[192:193], v232 offset:24672\n\t"
    "v_mfma_f32_16x16x32_bf16 v[28:31], a[196:199], a[12:15], v[28:31]\n\t"
    "ds_read_b64_tr_b16 a[194:195], v232 offset:25696\n\t"
    "v_mfma_f32_16x16x32_bf16 v[20:23], a[196:199], a[8:11], v[20:23]\n\t"
    "global_load_dwordx4 v[152:155], v227, s[42:43] sc1\n\t"
    "v_mfma_f32_16x16x32_bf16 v[12:15], a[196:199], a[4:7], v[12:15]\n\t"
    "s_add_u32 s42, s42, 0x200000\n\t"
    "s_addc_u32 s43, s43, 0\n\t"
    "v_mfma_f32_16x16x32_bf16 v[4:7], a[196:199], a[0:3], v[4:7]\n\t"
    "s_waitcnt lgkmcnt(15)\n\t"
    "ds_read_b64_tr_b16 a[196:197], v232 offset:24680\n\t"
    "v_mfma_f32_16x16x32_bf16 v[32:35], a[200:203], a[0:3], v[32:35]\n\t"
    "ds_read_b64_tr_b16 a[198:199], v232 offset:25704\n\t"
    "v_mfma_f32_16x16x32_bf16 v[40:43], a[200:203], a[4:7], v[40:43]\n\t"
    "v_mfma_f32_16x16x32_bf16 v[48:51], a[200:203], a[8:11], v[48:51]\n\t"
    "buffer_load_dwordx4 v242, s[68:71], s35 offen offset:1024 sc1 lds\n\t"
    "v_mfma_f32_16x16x32_bf16 v[56:59], a[200:203], a[12:15], v[56:59]\n\t"
    "s_waitcnt lgkmcnt(15)\n\t"
    "ds_read_b64_tr_b16 a[200:201], v233 offset:24672\n\t"
    "v_mfma_f32_16x16x32_bf16 v[60:63], a[204:207], a[12:15], v[60:63]\n\t"
    "ds_read_b64_tr_b16 a[202:203], v233 offset:25696\n\t"
    "v_mfma_f32_16x16x32_bf16 v[52:55], a[204:207], a[8:11], v[52:55]\n\t"
    "s_waitcnt vmcnt(23)\n\t"
    "ds_write_b32 v228, v140 offset:96\n\t"
    "v_mfma_f32_16x16x32_bf16 v[44:47], a[204:207], a[4:7], v[44:47]\n\t"
    "s_mov_b32 s34, 163840\n\t"
    "buffer_load_dwordx4 a[64:67], v240, s[64:67], s34 offen offset:0 sc1\n\t"
    "v_mfma_f32_16x16x32_bf16 v[36:39], a[204:207], a[0:3], v[36:39]\n\t"
    "s_waitcnt lgkmcnt(15)\n\t"
    "ds_read_b64_tr_b16 a[204:205], v233 offset:24680\n\t"
    "v_mfma_f32_16x16x32_bf16 v[64:67], a[208:211], a[0:3], v[64:67]\n\t"
    "ds_read_b64_tr_b16 a[206:207], v233 offset:25704\n\t"
    "v_mfma_f32_16x16x32_bf16 v[72:75], a[208:211], a[4:7], v[72:75]\n\t"
    "ds_write_b32 v228, v141 offset:4704\n\t"
    "v_mfma_f32_16x16x32_bf16 v[80:83], a[208:211], a[8:11], v[80:83]\n\t"
    "buffer_load_dwordx4 a[68:71], v240, s[64:67], s34 offen offset:1024 sc1\n\t"
    "v_mfma_f32_16x16x32_bf16 v[88:91], a[208:211], a[12:15], v[88:91]\n\t"
    "s_waitcnt lgkmcnt(15)\n\t"
    "ds_read_b64_tr_b16 a[208:209], v234 offset:24672\n\t"
    "v_mfma_f32_16x16x32_bf16 v[92:95], a[212:215], a[12:15], v[92:95]\n\t"
    "ds_read_b64_tr_b16 a[210:211], v234 offset:25696\n\t"
    "v_mfma_f32_16x16x32_bf16 v[84:87], a[212:215], a[8:11], v[84:87]\n\t"
    "ds_write_b32 v228, v142 offset:9312\n\t"
    "v_mfma_f32_16x16x32_bf16 v[76:79], a[212:215], a[4:7], v[76:79]\n\t"
    "v_mfma_f32_16x16x32_bf16 v[68:71], a[212:215], a[0:3], v[68:71]\n\t"
    "s_waitcnt lgkmcnt(15)\n\t"
    "ds_read_b64_tr_b16 a[212:213], v234 offset:24680\n\t"
    "v_mfma_f32_16x16x32_bf16 v[96:99], a[216:219], a[0:3], v[96:99]\n\t"
    "ds_read_b64_tr_b16 a[214:215], v234 offset:25704\n\t"
    "v_mfma_f32_16x16x32_bf16 v[104:107], a[216:219], a[4:7], v[104:107]\n\t"
    "ds_write_b32 v228, v143 offset:13920\n\t"
    "v_mfma_f32_16x16x32_bf16 v[112:115], a[216:219], a[8:11], v[112:115]\n\t"
    "buffer_load_dwordx4 a[72:75], v240, s[64:67], s34 offen offset:2048 sc1\n\t"
    "v_mfma_f32_16x16x32_bf16 v[120:123], a[216:219], a[12:15], v[120:123]\n\t"
    "s_waitcnt lgkmcnt(15)\n\t"
    "ds_read_b64_tr_b16 a[216:217], v235 offset:24672\n\t"
    "v_mfma_f32_16x16x32_bf16 v[124:127], a[220:223], a[12:15], v[124:127]\n\t"
    "ds_read_b64_tr_b16 a[218:219], v235 offset:25696\n\t"
    "v_mfma_f32_16x16x32_bf16 v[116:119], a[220:223], a[8:11], v[116:119]\n\t"
    "v_mfma_f32_16x16x32_bf16 v[108:111], a[220:223], a[4:7], v[108:111]\n\t"
    "buffer_load_dwordx4 a[76:79], v240, s[64:67], s34 offen offset:3072 sc1\n\t"
    "v_mfma_f32_16x16x32_bf16 v[100:103], a[220:223], a[0:3], v[100:103]\n\t"
    "s_waitcnt vmcnt(23) lgkmcnt(15)\n\t"
    "ds_read_b64_tr_b16 a[220:221], v235 offset:24680\n\t"
    "v_mfma_f32_16x16x32_bf16 v[0:3], a[192:195], a[16:19], v[0:3]\n\t"
    "ds_read_b64_tr_b16 a[222:223], v235 offset:25704\n\t"
    "v_mfma_f32_16x16x32_bf16 v[8:11], a[192:195], a[20:23], v[8:11]\n\t"
    "global_load_dwordx4 v[156:159], v227, s[42:43] sc1\n\t"
    "v_mfma_f32_16x16x32_bf16 v[16:19], a[192:195], a[24:27], v[16:19]\n\t"
    "s_mov_b32 s34, 167936\n\t"
    "buffer_load_dwordx4 a[80:83], v240, s[64:67], s34 offen offset:0 sc1\n\t"
    "v_mfma_f32_16x16x32_bf16 v[24:27], a[192:195], a[28:31], v[24:27]\n\t"
    "s_waitcnt lgkmcnt(15)\n\t"
    "ds_read_b64_tr_b16 a[192:193], v232 offset:32896\n\t"
    "v_mfma_f32_16x16x32_bf16 v[28:31], a[196:199], a[28:31], v[28:31]\n\t"
    "ds_read_b64_tr_b16 a[194:195], v232 offset:33920\n\t"
    "v_mfma_f32_16x16x32_bf16 v[20:23], a[196:199], a[24:27], v[20:23]\n\t"
    "v_mfma_f32_16x16x32_bf16 v[12:15], a[196:199], a[20:23], v[12:15]\n\t"
    "s_waitcnt vmcnt(21)\n\t"
    "ds_write_b32 v228, v144 offset:576\n\t"
    "v_mfma_f32_16x16x32_bf16 v[4:7], a[196:199], a[16:19], v[4:7]\n\t"
    "s_waitcnt lgkmcnt(15)\n\t"
    "ds_read_b64_tr_b16 a[196:197], v232 offset:32904\n\t"
    "v_mfma_f32_16x16x32_bf16 v[32:35], a[200:203], a[16:19], v[32:35]\n\t"
    "ds_read_b64_tr_b16 a[198:199], v232 offset:33928\n\t"
    "v_mfma_f32_16x16x32_bf16 v[40:43], a[200:203], a[20:23], v[40:43]\n\t"
    "v_mfma_f32_16x16x32_bf16 v[48:51], a[200:203], a[24:27], v[48:51]\n\t"
    "buffer_load_dwordx4 a[84:87], v240, s[64:67], s34 offen offset:1024 sc1\n\t"
    "v_mfma_f32_16x16x32_bf16 v[56:59], a[200:203], a[28:31], v[56:59]\n\t"
    "s_waitcnt lgkmcnt(15)\n\t"
    "ds_read_b64_tr_b16 a[200:201], v233 offset:32896\n\t"
    "v_mfma_f32_16x16x32_bf16 v[60:63], a[204:207], a[28:31], v[60:63]\n\t"
    "ds_read_b64_tr_b16 a[202:203], v233 offset:33920\n\t"
    "v_mfma_f32_16x16x32_bf16 v[52:55], a[204:207], a[24:27], v[52:55]\n\t"
    "ds_write_b32 v228, v145 offset:5184\n\t"
    "v_mfma_f32_16x16x32_bf16 v[44:47], a[204:207], a[20:23], v[44:47]\n\t"
    "buffer_load_dwordx4 a[88:91], v240, s[64:67], s34 offen offset:2048 sc1\n\t"
    "v_mfma_f32_16x16x32_bf16 v[36:39], a[204:207], a[16:19], v[36:39]\n\t"
    "s_waitcnt lgkmcnt(15)\n\t"
    "ds_read_b64_tr_b16 a[204:205], v233 offset:32904\n\t"
    "v_mfma_f32_16x16x32_bf16 v[64:67], a[208:211], a[16:19], v[64:67]\n\t"
    "ds_read_b64_tr_b16 a[206:207], v233 offset:33928\n\t"
    "v_mfma_f32_16x16x32_bf16 v[72:75], a[208:211], a[20:23], v[72:75]\n\t"
    "ds_write_b32 v228, v146 offset:9792\n\t"
    "v_mfma_f32_16x16x32_bf16 v[80:83], a[208:211], a[24:27], v[80:83]\n\t"
    "buffer_load_dwordx4 a[92:95], v240, s[64:67], s34 offen offset:3072 sc1\n\t"
    "v_mfma_f32_16x16x32_bf16 v[88:91], a[208:211], a[28:31], v[88:91]\n\t"
    "s_waitcnt lgkmcnt(15)\n\t"
    "ds_read_b64_tr_b16 a[208:209], v234 offset:32896\n\t"
    "v_mfma_f32_16x16x32_bf16 v[92:95], a[212:215], a[28:31], v[92:95]\n\t"
    "ds_read_b64_tr_b16 a[210:211], v234 offset:33920\n\t"
    "v_mfma_f32_16x16x32_bf16 v[84:87], a[212:215], a[24:27], v[84:87]\n\t"
    "v_mfma_f32_16x16x32_bf16 v[76:79], a[212:215], a[20:23], v[76:79]\n\t"
    "v_mfma_f32_16x16x32_bf16 v[68:71], a[212:215], a[16:19], v[68:71]\n\t"
    "s_waitcnt lgkmcnt(15)\n\t"
    "ds_read_b64_tr_b16 a[212:213], v234 offset:32904\n\t"
    "v_mfma_f32_16x16x32_bf16 v[96:99], a[216:219], a[16:19], v[96:99]\n\t"
    "ds_read_b64_tr_b16 a[214:215], v234 offset:33928\n\t"
    "v_mfma_f32_16x16x32_bf16 v[104:107], a[216:219], a[20:23], v[104:107]\n\t"
    "s_add_u32 m0, s31, 74016\n\t"
    "ds_write_b32 v228, v147 offset:14400\n\t"
    "v_mfma_f32_16x16x32_bf16 v[112:115], a[216:219], a[24:27], v[112:115]\n\t"
    "s_mov_b32 s35, 5275648\n\t"
    "buffer_load_dwordx4 v241, s[68:71], s35 offen sc1 lds\n\t"
    "v_mfma_f32_16x16x32_bf16 v[120:123], a[216:219], a[28:31], v[120:123]\n\t"
    "s_waitcnt lgkmcnt(15)\n\t"
    "ds_read_b64_tr_b16 a[216:217], v235 offset:32896\n\t"
    "v_mfma_f32_16x16x32_bf16 v[124:127], a[220:223], a[28:31], v[124:127]\n\t"
    "ds_read_b64_tr_b16 a[218:219], v235 offset:33920\n\t"
    "v_mfma_f32_16x16x32_bf16 v[116:119], a[220:223], a[24:27], v[116:119]\n\t"
    "v_mfma_f32_16x16x32_bf16 v[108:111], a[220:223], a[20:23], v[108:111]\n\t"
    "buffer_load_dwordx4 v242, s[68:71], s35 offen offset:1024 sc1 lds\n\t"
    "v_mfma_f32_16x16x32_bf16 v[100:103], a[220:223], a[16:19], v[100:103]\n\t"
    "s_waitcnt vmcnt(21) lgkmcnt(15)\n\t"
    "ds_read_b64_tr_b16 a[220:221], v235 offset:32904\n\t"
    "v_mfma_f32_16x16x32_bf16 v[0:3], a[192:195], a[32:35], v[0:3]\n\t"
    "s_barrier\n\t"
    "v_mfma_f32_16x16x32_bf16 v[8:11], a[192:195], a[36:39], v[8:11]\n\t"
    "s_add_u32 m0, s31, 82240\n\t"
    "ds_read_b64_tr_b16 a[222:223], v235 offset:33928\n\t"
    "v_mfma_f32_16x16x32_bf16 v[16:19], a[192:195], a[40:43], v[16:19]\n\t"
    "s_mov_b32 s35, 5505024\n\t"
    "buffer_load_dwordx4 v241, s[68:71], s35 offen sc1 lds\n\t"
    "v_mfma_f32_16x16x32_bf16 v[24:27], a[192:195], a[44:47], v[24:27]\n\t"
    "s_waitcnt lgkmcnt(15)\n\t"
    "ds_read_b64_tr_b16 a[192:193], v232 offset:41120\n\t"
    "v_mfma_f32_16x16x32_bf16 v[28:31], a[196:199], a[44:47], v[28:31]\n\t"
    "ds_read_b64_tr_b16 a[194:195], v232 offset:42144\n\t"
    "v_mfma_f32_16x16x32_bf16 v[20:23], a[196:199], a[40:43], v[20:23]\n\t"
    "s_waitcnt vmcnt(21)\n\t"
    "ds_write_b32 v228, v148 offset:608\n\t"
    "v_mfma_f32_16x16x32_bf16 v[12:15], a[196:199], a[36:39], v[12:15]\n\t"
    "buffer_load_dwordx4 v242, s[68:71], s35 offen offset:1024 sc1 lds\n\t"
    "v_mfma_f32_16x16x32_bf16 v[4:7], a[196:199], a[32:35], v[4:7]\n\t"
    "s_waitcnt lgkmcnt(15)\n\t"
    "ds_read_b64_tr_b16 a[196:197], v232 offset:41128\n\t"
    "v_mfma_f32_16x16x32_bf16 v[32:35], a[200:203], a[32:35], v[32:35]\n\t"
    "ds_read_b64_tr_b16 a[198:199], v232 offset:42152\n\t"
    "v_mfma_f32_16x16x32_bf16 v[40:43], a[200:203], a[36:39], v[40:43]\n\t"
    "ds_write_b32 v228, v149 offset:5216\n\t"
    "v_mfma_f32_16x16x32_bf16 v[48:51], a[200:203], a[40:43], v[48:51]\n\t"
    "s_mov_b32 s34, 180224\n\t"
    "buffer_load_dwordx4 a[96:99], v240, s[64:67], s34 offen offset:0 sc1\n\t"
    "v_mfma_f32_16x16x32_bf16 v[56:59], a[200:203], a[44:47], v[56:59]\n\t"
    "s_waitcnt lgkmcnt(15)\n\t"
    "ds_read_b64_tr_b16 a[200:201], v233 offset:41120\n\t"
    "v_mfma_f32_16x16x32_bf16 v[60:63], a[204:207], a[44:47], v[60:63]\n\t"
    "ds_read_b64_tr_b16 a[202:203], v233 offset:42144\n\t"
    "v_mfma_f32_16x16x32_bf16 v[52:55], a[204:207], a[40:43], v[52:55]\n\t"
    "ds_write_b32 v228, v150 offset:9824\n\t"
    "v_mfma_f32_16x16x32_bf16 v[44:47], a[204:207], a[36:39], v[44:47]\n\t"
    "ds_write_b32 v228, v151 offset:14432\n\t"
    "v_mfma_f32_16x16x32_bf16 v[36:39], a[204:207], a[32:35], v[36:39]\n\t"
    "s_waitcnt lgkmcnt(15)\n\t"
    "ds_read_b64_tr_b16 a[204:205], v233 offset:41128\n\t"
    "v_mfma_f32_16x16x32_bf16 v[64:67], a[208:211], a[32:35], v[64:67]\n\t"
    "ds_read_b64_tr_b16 a[206:207], v233 offset:42152\n\t"
    "v_and_b32 v224, 31, v245\n\t"
    "v_mfma_f32_16x16x32_bf16 v[72:75], a[208:211], a[36:39], v[72:75]\n\t"
    "v_lshrrev_b32 v225, 5, v245\n\t"
    "v_mul_u32_u24 v224, 144, v224\n\t"
    "v_mfma_f32_16x16x32_bf16 v[80:83], a[208:211], a[40:43], v[80:83]\n\t"
    "buffer_load_dwordx4 a[100:103], v240, s[64:67], s34 offen offset:1024 sc1\n\t"
    "v_lshl_add_u32 v225, v225, 4, v224\n\t"
    "v_mfma_f32_16x16x32_bf16 v[88:91], a[208:211], a[44:47], v[88:91]\n\t"
    "ds_read_b64_tr_b16 a[208:209], v234 offset:41120\n\t"
    "v_mfma_f32_16x16x32_bf16 v[92:95], a[212:215], a[44:47], v[92:95]\n\t"
    "s_mul_i32 s38, s22, 4608\n\t"
    "ds_read_b64_tr_b16 a[210:211], v234 offset:42144\n\t"
    "v_mfma_f32_16x16x32_bf16 v[84:87], a[212:215], a[40:43], v[84:87]\n\t"
    "s_add_u32 s38, s38, 115200\n\t"
    "v_add_u32 v224, s38, v225\n\t"
    "v_mfma_f32_16x16x32_bf16 v[76:79], a[212:215], a[36:39], v[76:79]\n\t"
    "buffer_load_dwordx4 a[104:107], v240, s[64:67], s34 offen offset:2048 sc1\n\t"
    "v_mfma_f32_16x16x32_bf16 v[68:71], a[212:215], a[32:35], v[68:71]\n\t"
    "s_waitcnt lgkmcnt(15)\n\t"
    "ds_read_b64_tr_b16 a[212:213], v234 offset:41128\n\t"
    "v_mfma_f32_16x16x32_bf16 v[96:99], a[216:219], a[32:35], v[96:99]\n\t"
    "s_and_b32 s39, s22, 1\n\t"
    "ds_read_b64_tr_b16 a[214:215], v234 offset:42152\n\t"
    "v_mfma_f32_16x16x32_bf16 v[104:107], a[216:219], a[36:39], v[104:107]\n\t"
    "s_mul_i32 s39, s39, 9216\n\t"
    "v_mfma_f32_16x16x32_bf16 v[112:115], a[216:219], a[40:43], v[112:115]\n\t"
    "buffer_load_dwordx4 a[108:111], v240, s[64:67], s34 offen offset:3072 sc1\n\t"
    "v_mfma_f32_16x16x32_bf16 v[120:123], a[216:219], a[44:47], v[120:123]\n\t"
    "Lpad4_%=:\n\t"
    "s_cbranch_execz LretA_%=\n\t"
    "s_waitcnt lgkmcnt(15)\n\t"
    "ds_read_b64_tr_b16 a[216:217], v235 offset:41120\n\t"
    "v_mfma_f32_16x16x32_bf16 v[124:127], a[220:223], a[44:47], v[124:127]\n\t"
    "s_add_u32 s39, s39, 133632\n\t"
    "ds_read_b64_tr_b16 a[218:219], v235 offset:42144\n\t"
    "v_mfma_f32_16x16x32_bf16 v[116:119], a[220:223], a[40:43], v[116:119]\n\t"
    "v_mfma_f32_16x16x32_bf16 v[108:111], a[220:223], a[36:39], v[108:111]\n\t"
    "s_mov_b32 s34, 184320\n\t"
    "buffer_load_dwordx4 a[112:115], v240, s[64:67], s34 offen offset:0 sc1\n\t"
    "v_mfma_f32_16x16x32_bf16 v[100:103], a[220:223], a[32:35], v[100:103]\n\t"
    "s_waitcnt vmcnt(23) lgkmcnt(15)\n\t"
    "ds_read_b64_tr_b16 a[220:221], v235 offset:41128\n\t"
    "v_mfma_f32_16x16x32_bf16 v[0:3], a[192:195], a[48:51], v[0:3]\n\t"
    "ds_read_b64_tr_b16 a[222:223], v235 offset:42152\n\t"
    "v_mfma_f32_16x16x32_bf16 v[8:11], a[192:195], a[52:55], v[8:11]\n\t"
    "s_waitcnt vmcnt(19)\n\t"
    "ds_write_b32 v228, v152 offset:640\n\t"
    "v_mfma_f32_16x16x32_bf16 v[16:19], a[192:195], a[56:59], v[16:19]\n\t"
    "buffer_load_dwordx4 a[116:119], v240, s[64:67], s34 offen offset:1024 sc1\n\t"
    "v_mfma_f32_16x16x32_bf16 v[24:27], a[192:195], a[60:63], v[24:27]\n\t"
    "s_waitcnt lgkmcnt(15)\n\t"
    "ds_read_b64_tr_b16 a[192:193], v232 offset:49344\n\t"
    "v_mfma_f32_16x16x32_bf16 v[28:31], a[196:199], a[60:63], v[28:31]\n\t"
    "ds_read_b64_tr_b16 a[194:195], v232 offset:50368\n\t"
    "v_mfma_f32_16x16x32_bf16 v[20:23], a[196:199], a[56:59], v[20:23]\n\t"
    "ds_write_b32 v228, v153 offset:5248\n\t"
    "v_mfma_f32_16x16x32_bf16 v[12:15], a[196:199], a[52:55], v[12:15]\n\t"
    "buffer_load_dwordx4 a[120:123], v240, s[64:67], s34 offen offset:2048 sc1\n\t"
    "v_mfma_f32_16x16x32_bf16 v[4:7], a[196:199], a[48:51], v[4:7]\n\t"
    "s_waitcnt lgkmcnt(15)\n\t"
    "ds_read_b64_tr_b16 a[196:197], v232 offset:49352\n\t"
    "v_mfma_f32_16x16x32_bf16 v[32:35], a[200:203], a[48:51], v[32:35]\n\t"
    "ds_read_b64_tr_b16 a[198:199], v232 offset:50376\n\t"
    "v_mfma_f32_16x16x32_bf16 v[40:43], a[200:203], a[52:55], v[40:43]\n\t"
    "ds_write_b32 v228, v154 offset:9856\n\t"
    "v_mfma_f32_16x16x32_bf16 v[48:51], a[200:203], a[56:59], v[48:51]\n\t"
    "buffer_load_dwordx4 a[124:127], v240, s[64:67], s34 offen offset:3072 sc1\n\t"
    "v_mfma_f32_16x16x32_bf16 v[56:59], a[200:203], a[60:63], v[56:59]\n\t"
    "s_waitcnt lgkmcnt(15)\n\t"
    "ds_read_b64_tr_b16 a[200:201], v233 offset:49344\n\t"
    "v_mfma_f32_16x16x32_bf16 v[60:63], a[204:207], a[60:63], v[60:63]\n\t"
    "ds_read_b64_tr_b16 a[202:203], v233 offset:50368\n\t"
    "v_mfma_f32_16x16x32_bf16 v[52:55], a[204:207], a[56:59], v[52:55]\n\t"
    "ds_write_b32 v228, v155 offset:14464\n\t"
    "v_mfma_f32_16x16x32_bf16 v[44:47], a[204:207], a[52:55], v[44:47]\n\t"
    "s_waitcnt vmcnt(16)\n\t"
    "ds_write_b32 v228, v156 offset:672\n\t"
    "v_mfma_f32_16x16x32_bf16 v[36:39], a[204:207], a[48:51], v[36:39]\n\t"
    "s_waitcnt lgkmcnt(15)\n\t"
    "ds_read_b64_tr_b16 a[204:205], v233 offset:49352\n\t"
    "v_mfma_f32_16x16x32_bf16 v[64:67], a[208:211], a[48:51], v[64:67]\n\t"
    "ds_read_b64_tr_b16 a[206:207], v233 offset:50376\n\t"
    "v_mfma_f32_16x16x32_bf16 v[72:75], a[208:211], a[52:55], v[72:75]\n\t"
    "s_add_u32 m0, s31, 90464\n\t"
    "ds_write_b32 v228, v157 offset:5280\n\t"
    "v_mfma_f32_16x16x32_bf16 v[80:83], a[208:211], a[56:59], v[80:83]\n\t"
    "s_mov_b32 s35, 5734400\n\t"
    "buffer_load_dwordx4 v241, s[68:71], s35 offen sc1 lds\n\t"
    "v_mfma_f32_16x16x32_bf16 v[88:91], a[208:211], a[60:63], v[88:91]\n\t"
    "ds_read_b64_tr_b16 a[208:209], v234 offset:49344\n\t"
    "v_mfma_f32_16x16x32_bf16 v[92:95], a[212:215], a[60:63], v[92:95]\n\t"
    "ds_read_b64_tr_b16 a[210:211], v234 offset:50368\n\t"
    "v_mfma_f32_16x16x32_bf16 v[84:87], a[212:215], a[56:59], v[84:87]\n\t"
    "ds_write_b32 v228, v158 offset:9888\n\t"
    "v_mfma_f32_16x16x32_bf16 v[76:79], a[212:215], a[52:55], v[76:79]\n\t"
    "buffer_load_dwordx4 v242, s[68:71], s35 offen offset:1024 sc1 lds\n\t"
    "v_mfma_f32_16x16x32_bf16 v[68:71], a[212:215], a[48:51], v[68:71]\n\t"
    "s_waitcnt lgkmcnt(15)\n\t"
    "ds_read_b64_tr_b16 a[212:213], v234 offset:49352\n\t"
    "v_mfma_f32_16x16x32_bf16 v[96:99], a[216:219], a[48:51], v[96:99]\n\t"
    "ds_read_b64_tr_b16 a[214:215], v234 offset:50376\n\t"
    "ds_write_b32 v228, v159 offset:14496\n\t"
    "v_mfma_f32_16x16x32_bf16 v[104:107], a[216:219], a[52:55], v[104:107]\n\t"
    "s_add_u32 m0, s31, 98688\n\t"
    "v_mfma_f32_16x16x32_bf16 v[112:115], a[216:219], a[56:59], v[112:115]\n\t"
    "s_mov_b32 s35, 5963776\n\t"
    "buffer_load_dwordx4 v241, s[68:71], s35 offen sc1 lds\n\t"
    "v_mfma_f32_16x16x32_bf16 v[120:123], a[216:219], a[60:63], v[120:123]\n\t"
    "ds_read_b64_tr_b16 a[216:217], v235 offset:49344\n\t"
    "v_mfma_f32_16x16x32_bf16 v[124:127], a[220:223], a[60:63], v[124:127]\n\t"
    "ds_read_b64_tr_b16 a[218:219], v235 offset:50368\n\t"
    "v_mfma_f32_16x16x32_bf16 v[116:119], a[220:223], a[56:59], v[116:119]\n\t"
    "v_mfma_f32_16x16x32_bf16 v[108:111], a[220:223], a[52:55], v[108:111]\n\t"
    "buffer_load_dwordx4 v242, s[68:71], s35 offen offset:1024 sc1 lds\n\t"
    "v_mfma_f32_16x16x32_bf16 v[100:103], a[220:223], a[48:51], v[100:103]\n\t"
    "s_waitcnt lgkmcnt(2)\n\t"
    "ds_read_b64_tr_b16 a[220:221], v235 offset:49352\n\t"
    "v_mfma_f32_16x16x32_bf16 v[0:3], a[192:195], a[64:67], v[0:3]\n\t"
    "s_barrier\n\t"
    "v_mfma_f32_16x16x32_bf16 v[8:11], a[192:195], a[68:71], v[8:11]\n\t"
    "ds_read_b64_tr_b16 a[222:223], v235 offset:50376\n\t"
    "v_mov_b32 v228, 152336\n\t"
    "v_mfma_f32_16x16x32_bf16 v[16:19], a[192:195], a[72:75], v[16:19]\n\t"
    "s_mov_b32 s34, 196608\n\t"
    "buffer_load_dwordx4 a[0:3], v240, s[64:67], s34 offen offset:0 sc1\n\t"
    "v_mfma_f32_16x16x32_bf16 v[24:27], a[192:195], a[76:79], v[24:27]\n\t"
    "ds_read_b64_tr_b16 a[192:193], v236 offset:0\n\t"
    "v_mfma_f32_16x16x32_bf16 v[28:31], a[196:199], a[76:79], v[28:31]\n\t"
    "ds_read_b64_tr_b16 a[194:195], v236 offset:1024\n\t"
    "v_mfma_f32_16x16x32_bf16 v[20:23], a[196:199], a[72:75], v[20:23]\n\t"
    "ds_read_b128 v[228:231], v228\n\t"
    "v_add_u32 v225, s39, v225\n\t"
    "v_mfma_f32_16x16x32_bf16 v[12:15], a[196:199], a[68:71], v[12:15]\n\t"
    "buffer_load_dwordx4 a[4:7], v240, s[64:67], s34 offen offset:1024 sc1\n\t"
    "v_mfma_f32_16x16x32_bf16 v[4:7], a[196:199], a[64:67], v[4:7]\n\t"
    "ds_read_b64_tr_b16 a[196:197], v236 offset:8\n\t"
    "v_mfma_f32_16x16x32_bf16 v[32:35], a[200:203], a[64:67], v[32:35]\n\t"
    "ds_read_b64_tr_b16 a[198:199], v236 offset:1032\n\t"
    "v_mfma_f32_16x16x32_bf16 v[40:43], a[200:203], a[68:71], v[40:43]\n\t"
    "ds_read_b128 v[128:131], v225 offset:0\n\t"
    "v_mfma_f32_16x16x32_bf16 v[48:51], a[200:203], a[72:75], v[48:51]\n\t"
    "buffer_load_dwordx4 a[8:11], v240, s[64:67], s34 offen offset:2048 sc1\n\t"
    "v_mfma_f32_16x16x32_bf16 v[56:59], a[200:203], a[76:79], v[56:59]\n\t"
    "ds_read_b64_tr_b16 a[200:201], v237 offset:0\n\t"
    "v_mfma_f32_16x16x32_bf16 v[60:63], a[204:207], a[76:79], v[60:63]\n\t"
    "ds_read_b64_tr_b16 a[202:203], v237 offset:1024\n\t"
    "v_mfma_f32_16x16x32_bf16 v[52:55], a[204:207], a[72:75], v[52:55]\n\t"
    "ds_read_b128 v[144:147], v225 offset:4608\n\t"
    "v_mfma_f32_16x16x32_bf16 v[44:47], a[204:207], a[68:71], v[44:47]\n\t"
    "ds_read_b128 v[132:135], v225 offset:32\n\t"
    "v_mfma_f32_16x16x32_bf16 v[36:39], a[204:207], a[64:67], v[36:39]\n\t"
    "ds_read_b64_tr_b16 a[204:205], v237 offset:8\n\t"
    "v_mfma_f32_16x16x32_bf16 v[64:67], a[208:211], a[64:67], v[64:67]\n\t"
    "ds_read_b64_tr_b16 a[206:207], v237 offset:1032\n\t"
    "v_mfma_f32_16x16x32_bf16 v[72:75], a[208:211], a[68:71], v[72:75]\n\t"
    "ds_read_b128 v[148:151], v225 offset:4640\n\t"
    "v_mfma_f32_16x16x32_bf16 v[80:83], a[208:211], a[72:75], v[80:83]\n\t"
    "buffer_load_dwordx4 a[12:15], v240, s[64:67], s34 offen offset:3072 sc1\n\t"
    "v_mfma_f32_16x16x32_bf16 v[88:91], a[208:211], a[76:79], v[88:91]\n\t"
    "ds_read_b64_tr_b16 a[208:209], v238 offset:0\n\t"
    "v_mfma_f32_16x16x32_bf16 v[92:95], a[212:215], a[76:79], v[92:95]\n\t"
    "ds_read_b64_tr_b16 a[210:211], v238 offset:1024\n\t"
    "v_mfma_f32_16x16x32_bf16 v[84:87], a[212:215], a[72:75], v[84:87]\n\t"
    "ds_read_b128 v[136:139], v225 offset:64\n\t"
    "v_mfma_f32_16x16x32_bf16 v[76:79], a[212:215], a[68:71], v[76:79]\n\t"
    "s_mov_b32 s34, 200704\n\t"
    "buffer_load_dwordx4 a[16:19], v240, s[64:67], s34 offen offset:0 sc1\n\t"
    "v_mfma_f32_16x16x32_bf16 v[68:71], a[212:215], a[64:67], v[68:71]\n\t"
    "s_waitcnt lgkmcnt(15)\n\t"
    "ds_read_b64_tr_b16 a[212:213], v238 offset:8\n\t"
    "v_mfma_f32_16x16x32_bf16 v[96:99], a[216:219], a[64:67], v[96:99]\n\t"
    "ds_read_b64_tr_b16 a[214:215], v238 offset:1032\n\t"
    "v_mfma_f32_16x16x32_bf16 v[104:107], a[216:219], a[68:71], v[104:107]\n\t"
    "ds_read_b128 v[152:155], v225 offset:4672\n\t"
    "v_mfma_f32_16x16x32_bf16 v[112:115], a[216:219], a[72:75], v[112:115]\n\t"
    "buffer_load_dwordx4 a[20:23], v240, s[64:67], s34 offen offset:1024 sc1\n\t"
    "v_mfma_f32_16x16x32_bf16 v[120:123], a[216:219], a[76:79], v[120:123]\n\t"
    "ds_read_b64_tr_b16 a[216:217], v239 offset:0\n\t"
    "v_mfma_f32_16x16x32_bf16 v[124:127], a[220:223], a[76:79], v[124:127]\n\t"
    "ds_read_b64_tr_b16 a[218:219], v239 offset:1024\n\t"
    "v_mfma_f32_16x16x32_bf16 v[116:119], a[220:223], a[72:75], v[116:119]\n\t"
    "ds_read_b128 v[140:143], v225 offset:96\n\t"
    "v_mfma_f32_16x16x32_bf16 v[108:111], a[220:223], a[68:71], v[108:111]\n\t"
    "buffer_load_dwordx4 a[24:27], v240, s[64:67], s34 offen offset:2048 sc1\n\t"
    "ds_read_b128 v[156:159], v225 offset:4704\n\t"
    "s_waitcnt lgkmcnt(15)\n\t"
    "v_add3_u32 v227, v228, v229, v230\n\t"
    "v_add_u32 v227, v227, v231\n\t"
    "s_nop 0\n\t"
    "v_readfirstlane_b32 s6, v227\n\t"
    "s_nop 1\n\t"
    "s_and_b32 s6, s6, 0xff\n\t"
    "s_cmp_gt_u32 s6, 14\n\t"
    "s_cselect_b32 s6, 0x2000, 0\n\t"
    "s_xor_b32 s30, s30, s6\n\t"
    "s_lshl_b32 s6, s6, 8\n\t"
    "s_add_u32 s4, s4, s6\n\t"
    "s_addc_u32 s5, s5, 0\n\t"
    "v_max3_u32 v228, v228, v229, v230\n\t"
    "v_max_u32 v228, v228, v231\n\t"
    "s_nop 0\n\t"
    "v_readfirstlane_b32 s62, v228\n\t"
    "s_nop 1\n\t"
    "s_lshr_b32 s62, s62, 8\n\t"
    "s_max_u32 s62, s62, 29\n\t"
    "s_mov_b32 s60, 0x04000400\n\t"
    "s_sub_u32 s72, s62, 29\n\t"
    "s_mul_i32 s72, s72, 0x800080\n\t"
    "s_mov_b32 s73, 0x7fff7fff\n\t"
    "s_sub_u32 s62, s62, 1\n\t"
    "s_lshl_b32 s62, s62, 23\n\t"
    "s_mov_b32 s63, s62\n\t"
    "v_mfma_f32_16x16x32_bf16 v[100:103], a[220:223], a[64:67], v[100:103]\n\t"
    "s_waitcnt vmcnt(23)\n\t"
    "ds_read_b64_tr_b16 a[220:221], v239 offset:8\n\t"
    "v_mfma_f32_16x16x32_bf16 v[0:3], a[192:195], a[80:83], v[0:3]\n\t"
    "ds_read_b64_tr_b16 a[222:223], v239 offset:1032\n\t"
    "v_mfma_f32_16x16x32_bf16 v[8:11], a[192:195], a[84:87], v[8:11]\n\t"
    "ds_read_b128 v[208:211], v224 offset:0\n\t"
    "v_and_b32 v226, s73, v128\n\t"
    "v_mfma_f32_16x16x32_bf16 v[16:19], a[192:195], a[88:91], v[16:19]\n\t"
    "buffer_load_dwordx4 a[28:31], v240, s[64:67], s34 offen offset:3072 sc1\n\t"
    "v_pk_sub_u16 v226, v226, s72 clamp\n\t"
    "v_mfma_f32_16x16x32_bf16 v[24:27], a[192:195], a[92:95], v[24:27]\n\t"
    "ds_read_b64_tr_b16 a[192:193], v236 offset:8224\n\t"
    "v_pk_lshlrev_b16 v226, 3, v226 op_sel_hi:[0,1]\n\t"
    "v_mfma_f32_16x16x32_bf16 v[28:31], a[196:199], a[92:95], v[28:31]\n\t"
    "ds_read_b64_tr_b16 a[194:195], v236 offset:9248\n\t"
    "v_bfi_b32 v226, s73, v226, v128\n\t"
    "v_mfma_f32_16x16x32_bf16 v[20:23], a[196:199], a[88:91], v[20:23]\n\t"
    "s_add_u32 m0, s31, 106912\n\t"
    "s_waitcnt lgkmcnt(15)\n\t"
    "v_and_b32 v227, s73, v144\n\t"
    "v_pk_sub_u16 v227, v227, s72 clamp\n\t"
    "v_mfma_f32_16x16x32_bf16 v[12:15], a[196:199], a[84:87], v[12:15]\n\t"
    "s_mov_b32 s35, 6193152\n\t"
    "buffer_load_dwordx4 v241, s[68:71], s35 offen sc1 lds\n\t"
    "v_pk_lshlrev_b16 v227, 3, v227 op_sel_hi:[0,1]\n\t"
    "v_mfma_f32_16x16x32_bf16 v[4:7], a[196:199], a[80:83], v[4:7]\n\t"
    "ds_read_b64_tr_b16 a[196:197], v236 offset:8232\n\t"
    "v_bfi_b32 v227, s73, v227, v144\n\t"
    "v_mfma_f32_16x16x32_bf16 v[32:35], a[200:203], a[80:83], v[32:35]\n\t"
    "ds_read_b64_tr_b16 a[198:199], v236 offset:9256\n\t"
    "v_pk_mul_f16 v226, v226, s60\n\t"
    "v_mfma_f32_16x16x32_bf16 v[40:43], a[200:203], a[84:87], v[40:43]\n\t"
    "v_pk_fma_f16 v128, v227, s60, v226\n\t"
    "v_pk_fma_f16 v144, v227, s60, v226 neg_lo:[1,0,0] neg_hi:[1,0,0]\n\t"
    "v_mfma_f32_16x16x32_bf16 v[48:51], a[200:203], a[88:91], v[48:51]\n\t"
    "buffer_load_dwordx4 v242, s[68:71], s35 offen offset:1024 sc1 lds\n\t"
    "v_and_b32 v228, s73, v129\n\t"
    "v_mfma_f32_16x16x32_bf16 v[56:59], a[200:203], a[92:95], v[56:59]\n\t"
    "ds_read_b64_tr_b16 a[200:201], v237 offset:8224\n\t"
    "v_pk_sub_u16 v228, v228, s72 clamp\n\t"
    "v_mfma_f32_16x16x32_bf16 v[60:63], a[204:207], a[92:95], v[60:63]\n\t"
    "ds_read_b64_tr_b16 a[202:203], v237 offset:9248\n\t"
    "v_pk_lshlrev_b16 v228, 3, v228 op_sel_hi:[0,1]\n\t"
    "v_mfma_f32_16x16x32_bf16 v[52:55], a[204:207], a[88:91], v[52:55]\n\t"
    "v_bfi_b32 v228, s73, v228, v129\n\t"
    "v_and_b32 v229, s73, v145\n\t"
    "v_mfma_f32_16x16x32_bf16 v[44:47], a[204:207], a[84:87], v[44:47]\n\t"
    "v_pk_sub_u16 v229, v229, s72 clamp\n\t"
    "v_mfma_f32_16x16x32_bf16 v[36:39], a[204:207], a[80:83], v[36:39]\n\t"
    "s_waitcnt lgkmcnt(15)\n\t"
    "ds_read_b64_tr_b16 a[204:205], v237 offset:8232\n\t"
    "v_pk_lshlrev_b16 v229, 3, v229 op_sel_hi:[0,1]\n\t"
    "v_mfma_f32_16x16x32_bf16 v[64:67], a[208:211], a[80:83], v[64:67]\n\t"
    "ds_read_b64_tr_b16 a[206:207], v237 offset:9256\n\t"
    "v_bfi_b32 v229, s73, v229, v145\n\t"
    "v_mfma_f32_16x16x32_bf16 v[72:75], a[208:211], a[84:87], v[72:75]\n\t"
    "s_add_u32 m0, s31, 0\n\t"
    "v_pk_mul_f16 v228, v228, s60\n\t"
    "v_pk_fma_f16 v129, v229, s60, v228\n\t"
    "v_mfma_f32_16x16x32_bf16 v[80:83], a[208:211], a[88:91], v[80:83]\n\t"
    "s_mov_b32 s35, 6422528\n\t"
    "buffer_load_dwordx4 v241, s[68:71], s35 offen sc1 lds\n\t"
    "v_pk_fma_f16 v145, v229, s60, v228 neg_lo:[1,0,0] neg_hi:[1,0,0]\n\t"
    "v_mfma_f32_16x16x32_bf16 v[88:91], a[208:211], a[92:95], v[88:91]\n\t"
    "s_waitcnt lgkmcnt(15)\n\t"
    "ds_read_b64_tr_b16 a[208:209], v238 offset:8224\n\t"
    "v_and_b32 v226, s73, v130\n\t"
    "v_mfma_f32_16x16x32_bf16 v[92:95], a[212:215], a[92:95], v[92:95]\n\t"
    "ds_read_b64_tr_b16 a[210:211], v238 offset:9248\n\t"
    "v_pk_sub_u16 v226, v226, s72 clamp\n\t"
    "v_mfma_f32_16x16x32_bf16 v[84:87], a[212:215], a[88:91], v[84:87]\n\t"
    "v_pk_lshlrev_b16 v226, 3, v226 op_sel_hi:[0,1]\n\t"
    "v_bfi_b32 v226, s73, v226, v130\n\t"
    "v_mfma_f32_16x16x32_bf16 v[76:79], a[212:215], a[84:87], v[76:79]\n\t"
    "buffer_load_dwordx4 v242, s[68:71], s35 offen offset:1024 sc1 lds\n\t"
    "v_and_b32 v227, s73, v146\n\t"
    "v_mfma_f32_16x16x32_bf16 v[68:71], a[212:215], a[80:83], v[68:71]\n\t"
    "s_waitcnt lgkmcnt(15)\n\t"
    "ds_read_b64_tr_b16 a[212:213], v238 offset:8232\n\t"
    "v_pk_sub_u16 v227, v227, s72 clamp\n\t"
    "v_mfma_f32_16x16x32_bf16 v[96:99], a[216:219], a[80:83], v[96:99]\n\t"
    "ds_read_b64_tr_b16 a[214:215], v238 offset:9256\n\t"
    "v_pk_lshlrev_b16 v227, 3, v227 op_sel_hi:[0,1]\n\t"
    "v_mfma_f32_16x16x32_bf16 v[104:107], a[216:219], a[84:87], v[104:107]\n\t"
    "s_add_u32 m0, s31, 8224\n\t"
    "v_bfi_b32 v227, s73, v227, v146\n\t"
    "v_pk_mul_f16 v226, v226, s60\n\t"
    "v_mfma_f32_16x16x32_bf16 v[112:115], a[216:219], a[88:91], v[112:115]\n\t"
    "s_mov_b32 s35, 6651904\n\t"
    "buffer_load_dwordx4 v241, s[68:71], s35 offen sc1 lds\n\t"
    "v_pk_fma_f16 v130, v227, s60, v226\n\t"
    "v_mfma_f32_16x16x32_bf16 v[120:123], a[216:219], a[92:95], v[120:123]\n\t"
    "s_waitcnt lgkmcnt(13)\n\t"
    "ds_read_b64_tr_b16 a[216:217], v239 offset:8224\n\t"
    "v_pk_fma_f16 v146, v227, s60, v226 neg_lo:[1,0,0] neg_hi:[1,0,0]\n\t"
    "v_mfma_f32_16x16x32_bf16 v[124:127], a[220:223], a[92:95], v[124:127]\n\t"
    "ds_read_b64_tr_b16 a[218:219], v239 offset:9248\n\t"
    "v_and_b32 v228, s73, v131\n\t"
    "v_mfma_f32_16x16x32_bf16 v[116:119], a[220:223], a[88:91], v[116:119]\n\t"
    "v_pk_sub_u16 v228, v228, s72 clamp\n\t"
    "v_mfma_f32_16x16x32_bf16 v[108:111], a[220:223], a[84:87], v[108:111]\n\t"
    "buffer_load_dwordx4 v242, s[68:71], s35 offen offset:1024 sc1 lds\n\t"
    "v_mfma_f32_16x16x32_bf16 v[100:103], a[220:223], a[80:83], v[100:103]\n\t"
    "s_waitcnt vmcnt(22) lgkmcnt(12)\n\t"
    "ds_read_b64_tr_b16 a[220:221], v239 offset:8232\n\t"
    "v_mfma_f32_16x16x32_bf16 v[0:3], a[192:195], a[96:99], v[0:3]\n\t"
    "s_barrier\n\t"
    "v_mfma_f32_16x16x32_bf16 v[8:11], a[192:195], a[100:103], v[8:11]\n\t"
    "s_add_u32 m0, s31, 16448\n\t"
    "ds_read_b64_tr_b16 a[222:223], v239 offset:9256\n\t"
    "v_mfma_f32_16x16x32_bf16 v[16:19], a[192:195], a[104:107], v[16:19]\n\t"
    "s_mov_b32 s35, 6881280\n\t"
    "buffer_load_dwordx4 v241, s[68:71], s35 offen sc1 lds\n\t"
    "v_mfma_f32_16x16x32_bf16 v[24:27], a[192:195], a[108:111], v[24:27]\n\t"
    "s_waitcnt lgkmcnt(12)\n\t"
    "ds_read_b64_tr_b16 a[192:193], v236 offset:16448\n\t"
    "v_mfma_f32_16x16x32_bf16 v[28:31], a[196:199], a[108:111], v[28:31]\n\t"
    "ds_read_b64_tr_b16 a[194:195], v236 offset:17472\n\t"
    "v_mfma_f32_16x16x32_bf16 v[20:23], a[196:199], a[104:107], v[20:23]\n\t"
    "ds_read_b128 v[212:215], v224 offset:32\n\t"
    "v_pk_lshlrev_b16 v228, 3, v228 op_sel_hi:[0,1]\n\t"
    "v_mfma_f32_16x16x32_bf16 v[12:15], a[196:199], a[100:103], v[12:15]\n\t"
    "buffer_load_dwordx4 v242, s[68:71], s35 offen offset:1024 sc1 lds\n\t"
    "v_bfi_b32 v228, s73, v228, v131\n\t"
    "v_mfma_f32_16x16x32_bf16 v[4:7], a[196:199], a[96:99], v[4:7]\n\t"
    "s_waitcnt lgkmcnt(13)\n\t"
    "ds_read_b64_tr_b16 a[196:197], v236 offset:16456\n\t"
    "v_and_b32 v229, s73, v147\n\t"
    "v_mfma_f32_16x16x32_bf16 v[32:35], a[200:203], a[96:99], v[32:35]\n\t"
    "ds_read_b64_tr_b16 a[198:199], v236 offset:17480\n\t"
    "v_pk_sub_u16 v229, v229, s72 clamp\n\t"
    "v_mfma_f32_16x16x32_bf16 v[40:43], a[200:203], a[100:103], v[40:43]\n\t"
    "s_add_u32 m0, s31, 24672\n\t"
    "v_pk_lshlrev_b16 v229, 3, v229 op_sel_hi:[0,1]\n\t"
    "v_bfi_b32 v229, s73, v229, v147\n\t"
    "v_mfma_f32_16x16x32_bf16 v[48:51], a[200:203], a[104:107], v[48:51]\n\t"
    "s_mov_b32 s35, 7110656\n\t"
    "buffer_load_dwordx4 v241, s[68:71], s35 offen sc1 lds\n\t"
    "v_pk_mul_f16 v228, v228, s60\n\t"
    "v_mfma_f32_16x16x32_bf16 v[56:59], a[200:203], a[108:111], v[56:59]\n\t"
    "s_waitcnt lgkmcnt(13)\n\t"
    "ds_read_b64_tr_b16 a[200:201], v237 offset:16448\n\t"
    "v_pk_fma_f16 v131, v229, s60, v228\n\t"
    "v_mfma_f32_16x16x32_bf16 v[60:63], a[204:207], a[108:111], v[60:63]\n\t"
    "ds_read_b64_tr_b16 a[202:203], v237 offset:17472\n\t"
    "v_pk_fma_f16 v147, v229, s60, v228 neg_lo:[1,0,0] neg_hi:[1,0,0]\n\t"
    "v_mfma_f32_16x16x32_bf16 v[52:55], a[204:207], a[104:107], v[52:55]\n\t"
    "v_and_b32 v226, s73, v132\n\t"
    "v_pk_sub_u16 v226, v226, s72 clamp\n\t"
    "v_mfma_f32_16x16x32_bf16 v[44:47], a[204:207], a[100:103], v[44:47]\n\t"
    "v_pk_lshlrev_b16 v226, 3, v226 op_sel_hi:[0,1]\n\t"
    "v_bfi_b32 v226, s73, v226, v132\n\t"
    "v_mfma_f32_16x16x32_bf16 v[36:39], a[204:207], a[96:99], v[36:39]\n\t"
    "s_waitcnt lgkmcnt(13)\n\t"
    "ds_read_b64_tr_b16 a[204:205], v237 offset:16456\n\t"
    "v_and_b32 v227, s73, v148\n\t"
    "v_mfma_f32_16x16x32_bf16 v[64:67], a[208:211], a[96:99], v[64:67]\n\t"
    "ds_read_b64_tr_b16 a[206:207], v237 offset:17480\n\t"
    "v_pk_sub_u16 v227, v227, s72 clamp\n\t"
    "v_mfma_f32_16x16x32_bf16 v[72:75], a[208:211], a[100:103], v[72:75]\n\t"
    "v_pk_lshlrev_b16 v227, 3, v227 op_sel_hi:[0,1]\n\t"
    "v_bfi_b32 v227, s73, v227, v148\n\t"
    "v_mfma_f32_16x16x32_bf16 v[80:83], a[208:211], a[104:107], v[80:83]\n\t"
    "buffer_load_dwordx4 v242, s[68:71], s35 offen offset:1024 sc1 lds\n\t"
    "v_pk_mul_f16 v226, v226, s60\n\t"
    "v_mfma_f32_16x16x32_bf16 v[88:91], a[208:211], a[108:111], v[88:91]\n\t"
    "s_waitcnt lgkmcnt(13)\n\t"
    "ds_read_b64_tr_b16 a[208:209], v238 offset:16448\n\t"
    "v_pk_fma_f16 v132, v227, s60, v226\n\t"
    "v_mfma_f32_16x16x32_bf16 v[92:95], a[212:215], a[108:111], v[92:95]\n\t"
    "ds_read_b64_tr_b16 a[210:211], v238 offset:17472\n\t"
    "v_pk_fma_f16 v148, v227, s60, v226 neg_lo:[1,0,0] neg_hi:[1,0,0]\n\t"
    "v_mfma_f32_16x16x32_bf16 v[84:87], a[212:215], a[104:107], v[84:87]\n\t"
    "v_and_b32 v228, s73, v133\n\t"
    "v_pk_sub_u16 v228, v228, s72 clamp\n\t"
    "v_mfma_f32_16x16x32_bf16 v[76:79], a[212:215], a[100:103], v[76:79]\n\t"
    "s_mov_b32 s34, 212992\n\t"
    "buffer_load_dwordx4 a[32:35], v240, s[64:67], s34 offen offset:0 sc1\n\t"
    "v_pk_lshlrev_b16 v228, 3, v228 op_sel_hi:[0,1]\n\t"
    "v_mfma_f32_16x16x32_bf16 v[68:71], a[212:215], a[96:99], v[68:71]\n\t"
    "s_waitcnt lgkmcnt(13)\n\t"
    "ds_read_b64_tr_b16 a[212:213], v238 offset:16456\n\t"
    "v_bfi_b32 v228, s73, v228, v133\n\t"
    "v_mfma_f32_16x16x32_bf16 v[96:99], a[216:219], a[96:99], v[96:99]\n\t"
    "ds_read_b64_tr_b16 a[214:215], v238 offset:17480\n\t"
    "v_and_b32 v229, s73, v149\n\t"
    "v_mfma_f32_16x16x32_bf16 v[104:107], a[216:219], a[100:103], v[104:107]\n\t"
    "v_pk_sub_u16 v229, v229, s72 clamp\n\t"
    "v_pk_lshlrev_b16 v229, 3, v229 op_sel_hi:[0,1]\n\t"
    "v_mfma_f32_16x16x32_bf16 v[112:115], a[216:219], a[104:107], v[112:115]\n\t"
    "buffer_load_dwordx4 a[36:39], v240, s[64:67], s34 offen offset:1024 sc1\n\t"
    "v_bfi_b32 v229, s73, v229, v149\n\t"
    "v_mfma_f32_16x16x32_bf16 v[120:123], a[216:219], a[108:111], v[120:123]\n\t"
    "s_waitcnt lgkmcnt(13)\n\t"
    "ds_read_b64_tr_b16 a[216:217], v239 offset:16448\n\t"
    "v_pk_mul_f16 v228, v228, s60\n\t"
    "v_mfma_f32_16x16x32_bf16 v[124:127], a[220:223], a[108:111], v[124:127]\n\t"
    "ds_read_b64_tr_b16 a[218:219], v239 offset:17472\n\t"
    "v_pk_fma_f16 v133, v229, s60, v228\n\t"
    "v_mfma_f32_16x16x32_bf16 v[116:119], a[220:223], a[104:107], v[116:119]\n\t"
    "v_pk_fma_f16 v149, v229, s60, v228 neg_lo:[1,0,0] neg_hi:[1,0,0]\n\t"
    "v_and_b32 v226, s73, v134\n\t"
    "v_mfma_f32_16x16x32_bf16 v[108:111], a[220:223], a[100:103], v[108:111]\n\t"
    "buffer_load_dwordx4 a[40:43], v240, s[64:67], s34 offen offset:2048 sc1\n\t"
    "v_pk_sub_u16 v226, v226, s72 clamp\n\t"
    "v_pk_lshlrev_b16 v226, 3, v226 op_sel_hi:[0,1]\n\t"
    "v_bfi_b32 v226, s73, v226, v134\n\t"
    "s_lshl_b32 s38, s29, 6\n\t"
    "s_add_u32 s38, s38, 0x1c000\n\t"
    "s_add_u32 s52, s20, s38\n\t"
    "s_addc_u32 s53, s21, 0\n\t"
    "s_and_b32 s41, s27, 15\n\t"
    "v_mfma_f32_16x16x32_bf16 v[100:103], a[220:223], a[96:99], v[100:103]\n\t"
    "s_waitcnt vmcnt(25) lgkmcnt(13)\n\t"
    "ds_read_b64_tr_b16 a[220:221], v239 offset:16456\n\t"
    "v_mfma_f32_16x16x32_bf16 v[0:3], a[192:195], a[112:115], v[0:3]\n\t"
    "ds_read_b64_tr_b16 a[222:223], v239 offset:17480\n\t"
    "v_mfma_f32_16x16x32_bf16 v[8:11], a[192:195], a[116:119], v[8:11]\n\t"
    "ds_read_b128 v[216:219], v224 offset:64\n\t"
    "v_and_b32 v227, s73, v150\n\t"
    "v_mfma_f32_16x16x32_bf16 v[16:19], a[192:195], a[120:123], v[16:19]\n\t"
    "buffer_load_dwordx4 a[44:47], v240, s[64:67], s34 offen offset:3072 sc1\n\t"
    "v_pk_sub_u16 v227, v227, s72 clamp\n\t"
    "v_mfma_f32_16x16x32_bf16 v[24:27], a[192:195], a[124:127], v[24:27]\n\t"
    "s_waitcnt lgkmcnt(13)\n\t"
    "ds_read_b64_tr_b16 a[192:193], v236 offset:24672\n\t"
    "v_pk_lshlrev_b16 v227, 3, v227 op_sel_hi:[0,1]\n\t"
    "v_mfma_f32_16x16x32_bf16 v[28:31], a[196:199], a[124:127], v[28:31]\n\t"
    "ds_read_b64_tr_b16 a[194:195], v236 offset:25696\n\t"
    "v_bfi_b32 v227, s73, v227, v150\n\t"
    "v_mfma_f32_16x16x32_bf16 v[20:23], a[196:199], a[120:123], v[20:23]\n\t"
    "v_pk_mul_f16 v226, v226, s60\n\t"
    "v_pk_fma_f16 v134, v227, s60, v226\n\t"
    "v_mfma_f32_16x16x32_bf16 v[12:15], a[196:199], a[116:119], v[12:15]\n\t"
    "s_mov_b32 s34, 217088\n\t"
    "buffer_load_dwordx4 a[48:51], v240, s[64:67], s34 offen offset:0 sc1\n\t"
    "v_pk_fma_f16 v150, v227, s60, v226 neg_lo:[1,0,0] neg_hi:[1,0,0]\n\t"
    "v_mfma_f32_16x16x32_bf16 v[4:7], a[196:199], a[112:115], v[4:7]\n\t"
    "s_waitcnt lgkmcnt(13)\n\t"
    "ds_read_b64_tr_b16 a[196:197], v236 offset:24680\n\t"
    "v_and_b32 v228, s73, v135\n\t"
    "v_mfma_f32_16x16x32_bf16 v[32:35], a[200:203], a[112:115], v[32:35]\n\t"
    "ds_read_b64_tr_b16 a[198:199], v236 offset:25704\n\t"
    "v_pk_sub_u16 v228, v228, s72 clamp\n\t"
    "v_mfma_f32_16x16x32_bf16 v[40:43], a[200:203], a[116:119], v[40:43]\n\t"
    "v_pk_lshlrev_b16 v228, 3, v228 op_sel_hi:[0,1]\n\t"
    "v_bfi_b32 v228, s73, v228, v135\n\t"
    "v_mfma_f32_16x16x32_bf16 v[48:51], a[200:203], a[120:123], v[48:51]\n\t"
    "buffer_load_dwordx4 a[52:55], v240, s[64:67], s34 offen offset:1024 sc1\n\t"
    "v_and_b32 v229, s73, v151\n\t"
    "v_mfma_f32_16x16x32_bf16 v[56:59], a[200:203], a[124:127], v[56:59]\n\t"
    "s_waitcnt lgkmcnt(13)\n\t"
    "ds_read_b64_tr_b16 a[200:201], v237 offset:24672\n\t"
    "v_pk_sub_u16 v229, v229, s72 clamp\n\t"
    "v_mfma_f32_16x16x32_bf16 v[60:63], a[204:207], a[124:127], v[60:63]\n\t"
    "ds_read_b64_tr_b16 a[202:203], v237 offset:25696\n\t"
    "v_pk_lshlrev_b16 v229, 3, v229 op_sel_hi:[0,1]\n\t"
    "v_mfma_f32_16x16x32_bf16 v[52:55], a[204:207], a[120:123], v[52:55]\n\t"
    "v_bfi_b32 v229, s73, v229, v151\n\t"
    "v_pk_mul_f16 v228, v228, s60\n\t"
    "v_mfma_f32_16x16x32_bf16 v[44:47], a[204:207], a[116:119], v[44:47]\n\t"
    "v_pk_fma_f16 v135, v229, s60, v228\n\t"
    "v_pk_fma_f16 v151, v229, s60, v228 neg_lo:[1,0,0] neg_hi:[1,0,0]\n\t"
    "v_mfma_f32_16x16x32_bf16 v[36:39], a[204:207], a[112:115], v[36:39]\n\t"
    "s_waitcnt lgkmcnt(13)\n\t"
    "ds_read_b64_tr_b16 a[204:205], v237 offset:24680\n\t"
    "v_and_b32 v226, s73, v136\n\t"
    "v_mfma_f32_16x16x32_bf16 v[64:67], a[208:211], a[112:115], v[64:67]\n\t"
    "ds_read_b64_tr_b16 a[206:207], v237 offset:25704\n\t"
    "v_pk_sub_u16 v226, v226, s72 clamp\n\t"
    "v_mfma_f32_16x16x32_bf16 v[72:75], a[208:211], a[116:119], v[72:75]\n\t"
    "v_pk_lshlrev_b16 v226, 3, v226 op_sel_hi:[0,1]\n\t"
    "v_bfi_b32 v226, s73, v226, v136\n\t"
    "v_mfma_f32_16x16x32_bf16 v[80:83], a[208:211], a[120:123], v[80:83]\n\t"
    "buffer_load_dwordx4 a[56:59], v240, s[64:67], s34 offen offset:2048 sc1\n\t"
    "v_and_b32 v227, s73, v152\n\t"
    "v_mfma_f32_16x16x32_bf16 v[88:91], a[208:211], a[124:127], v[88:91]\n\t"
    "s_waitcnt lgkmcnt(13)\n\t"
    "ds_read_b64_tr_b16 a[208:209], v238 offset:24672\n\t"
    "v_pk_sub_u16 v227, v227, s72 clamp\n\t"
    "v_mfma_f32_16x16x32_bf16 v[92:95], a[212:215], a[124:127], v[92:95]\n\t"
    "ds_read_b64_tr_b16 a[210:211], v238 offset:25696\n\t"
    "v_pk_lshlrev_b16 v227, 3, v227 op_sel_hi:[0,1]\n\t"
    "v_mfma_f32_16x16x32_bf16 v[84:87], a[212:215], a[120:123], v[84:87]\n\t"
    "v_bfi_b32 v227, s73, v227, v152\n\t"
    "v_pk_mul_f16 v226, v226, s60\n\t"
    "v_mfma_f32_16x16x32_bf16 v[76:79], a[212:215], a[116:119], v[76:79]\n\t"
    "buffer_load_dwordx4 a[60:63], v240, s[64:67], s34 offen offset:3072 sc1\n\t"
    "v_pk_fma_f16 v136, v227, s60, v226\n\t"
    "v_mfma_f32_16x16x32_bf16 v[68:71], a[212:215], a[112:115], v[68:71]\n\t"
    "s_waitcnt lgkmcnt(13)\n\t"
    "ds_read_b64_tr_b16 a[212:213], v238 offset:24680\n\t"
    "v_pk_fma_f16 v152, v227, s60, v226 neg_lo:[1,0,0] neg_hi:[1,0,0]\n\t"
    "v_mfma_f32_16x16x32_bf16 v[96:99], a[216:219], a[112:115], v[96:99]\n\t"
    "ds_read_b64_tr_b16 a[214:215], v238 offset:25704\n\t"
    "v_and_b32 v228, s73, v137\n\t"
    "v_mfma_f32_16x16x32_bf16 v[104:107], a[216:219], a[116:119], v[104:107]\n\t"
    "v_pk_sub_u16 v228, v228, s72 clamp\n\t"
    "v_pk_lshlrev_b16 v228, 3, v228 op_sel_hi:[0,1]\n\t"
    "v_mfma_f32_16x16x32_bf16 v[112:115], a[216:219], a[120:123], v[112:115]\n\t"
    "s_mov_b32 s34, 229376\n\t"
    "buffer_load_dwordx4 a[64:67], v240, s[64:67], s34 offen offset:0 sc1\n\t"
    "v_bfi_b32 v228, s73, v228, v137\n\t"
    "v_mfma_f32_16x16x32_bf16 v[120:123], a[216:219], a[124:127], v[120:123]\n\t"
    "s_waitcnt lgkmcnt(13)\n\t"
    "ds_read_b64_tr_b16 a[216:217], v239 offset:24672\n\t"
    "v_and_b32 v229, s73, v153\n\t"
    "v_mfma_f32_16x16x32_bf16 v[124:127], a[220:223], a[124:127], v[124:127]\n\t"
    "ds_read_b64_tr_b16 a[218:219], v239 offset:25696\n\t"
    "v_pk_sub_u16 v229, v229, s72 clamp\n\t"
    "v_mfma_f32_16x16x32_bf16 v[116:119], a[220:223], a[120:123], v[116:119]\n\t"
    "s_lshl_b32 s41, s41, 2\n\t"
    "s_add_u32 s41, s41, s25\n\t"
    "s_lshl_b32 s41, s41, 18\n\t"
    "v_mfma_f32_16x16x32_bf16 v[108:111], a[220:223], a[116:119], v[108:111]\n\t"
    "buffer_load_dwordx4 a[68:71], v240, s[64:67], s34 offen offset:1024 sc1\n\t"
    "v_mfma_f32_16x16x32_bf16 v[100:103], a[220:223], a[112:115], v[100:103]\n\t"
    "s_waitcnt vmcnt(24) lgkmcnt(12)\n\t"
    "ds_read_b64_tr_b16 a[220:221], v239 offset:24680\n\t"
    "v_mfma_f32_16x16x32_bf16 v[0:3], a[192:195], a[0:3], v[0:3]\n\t"
    "s_barrier\n\t"
    "v_mfma_f32_16x16x32_bf16 v[8:11], a[192:195], a[4:7], v[8:11]\n\t"
    "ds_read_b64_tr_b16 a[222:223], v239 offset:25704\n\t"
    "v_mfma_f32_16x16x32_bf16 v[16:19], a[192:195], a[8:11], v[16:19]\n\t"
    "buffer_load_dwordx4 a[72:75], v240, s[64:67], s34 offen offset:2048 sc1\n\t"
    "v_mfma_f32_16x16x32_bf16 v[24:27], a[192:195], a[12:15], v[24:27]\n\t"
    "s_waitcnt lgkmcnt(12)\n\t"
    "ds_read_b64_tr_b16 a[192:193], v236 offset:32896\n\t"
    "v_mfma_f32_16x16x32_bf16 v[28:31], a[196:199], a[12:15], v[28:31]\n\t"
    "ds_read_b64_tr_b16 a[194:195], v236 offset:33920\n\t"
    "v_mfma_f32_16x16x32_bf16 v[20:23], a[196:199], a[8:11], v[20:23]\n\t"
    "ds_read_b128 v[220:223], v224 offset:96\n\t"
    "v_pk_lshlrev_b16 v229, 3, v229 op_sel_hi:[0,1]\n\t"
    "v_mfma_f32_16x16x32_bf16 v[12:15], a[196:199], a[4:7], v[12:15]\n\t"
    "buffer_load_dwordx4 a[76:79], v240, s[64:67], s34 offen offset:3072 sc1\n\t"
    "v_bfi_b32 v229, s73, v229, v153\n\t"
    "v_mfma_f32_16x16x32_bf16 v[4:7], a[196:199], a[0:3], v[4:7]\n\t"
    "s_waitcnt lgkmcnt(13)\n\t"
    "ds_read_b64_tr_b16 a[196:197], v236 offset:32904\n\t"
    "v_pk_mul_f16 v228, v228, s60\n\t"
    "v_mfma_f32_16x16x32_bf16 v[32:35], a[200:203], a[0:3], v[32:35]\n\t"
    "ds_read_b64_tr_b16 a[198:199], v236 offset:33928\n\t"
    "v_pk_fma_f16 v137, v229, s60, v228\n\t"
    "v_mfma_f32_16x16x32_bf16 v[40:43], a[200:203], a[4:7], v[40:43]\n\t"
    "v_pk_fma_f16 v153, v229, s60, v228 neg_lo:[1,0,0] neg_hi:[1,0,0]\n\t"
    "v_and_b32 v226, s73, v138\n\t"
    "v_mfma_f32_16x16x32_bf16 v[48:51], a[200:203], a[8:11], v[48:51]\n\t"
    "s_mov_b32 s34, 233472\n\t"
    "buffer_load_dwordx4 a[80:83], v240, s[64:67], s34 offen offset:0 sc1\n\t"
    "v_pk_sub_u16 v226, v226, s72 clamp\n\t"
    "v_mfma_f32_16x16x32_bf16 v[56:59], a[200:203], a[12:15], v[56:59]\n\t"
    "s_waitcnt lgkmcnt(13)\n\t"
    "ds_read_b64_tr_b16 a[200:201], v237 offset:32896\n\t"
    "v_pk_lshlrev_b16 v226, 3, v226 op_sel_hi:[0,1]\n\t"
    "v_mfma_f32_16x16x32_bf16 v[60:63], a[204:207], a[12:15], v[60:63]\n\t"
    "ds_read_b64_tr_b16 a[202:203], v237 offset:33920\n\t"
    "v_bfi_b32 v226, s73, v226, v138\n\t"
    "v_mfma_f32_16x16x32_bf16 v[52:55], a[204:207], a[8:11], v[52:55]\n\t"
    "v_and_b32 v227, s73, v154\n\t"
    "v_pk_sub_u16 v227, v227, s72 clamp\n\t"
    "v_mfma_f32_16x16x32_bf16 v[44:47], a[204:207], a[4:7], v[44:47]\n\t"
    "v_pk_lshlrev_b16 v227, 3, v227 op_sel_hi:[0,1]\n\t"
    "v_bfi_b32 v227, s73, v227, v154\n\t"
    "v_mfma_f32_16x16x32_bf16 v[36:39], a[204:207], a[0:3], v[36:39]\n\t"
    "s_waitcnt lgkmcnt(13)\n\t"
    "ds_read_b64_tr_b16 a[204:205], v237 offset:32904\n\t"
    "v_pk_mul_f16 v226, v226, s60\n\t"
    "v_mfma_f32_16x16x32_bf16 v[64:67], a[208:211], a[0:3], v[64:67]\n\t"
    "ds_read_b64_tr_b16 a[206:207], v237 offset:33928\n\t"
    "v_pk_fma_f16 v138, v227, s60, v226\n\t"
    "v_mfma_f32_16x16x32_bf16 v[72:75], a[208:211], a[4:7], v[72:75]\n\t"
    "v_pk_fma_f16 v154, v227, s60, v226 neg_lo:[1,0,0] neg_hi:[1,0,0]\n\t"
    "v_and_b32 v228, s73, v139\n\t"
    "v_mfma_f32_16x16x32_bf16 v[80:83], a[208:211], a[8:11], v[80:83]\n\t"
    "Lpad5_%=:\n\t"
    "s_cbranch_execz Lpad9_%=\n\t"
    "buffer_load_dwordx4 a[84:87], v240, s[64:67], s34 offen offset:1024 sc1\n\t"
    "v_pk_sub_u16 v228, v228, s72 clamp\n\t"
    "v_mfma_f32_16x16x32_bf16 v[88:91], a[208:211], a[12:15], v[88:91]\n\t"
    "s_waitcnt lgkmcnt(13)\n\t"
    "ds_read_b64_tr_b16 a[208:209], v238 offset:32896\n\t"
    "v_pk_lshlrev_b16 v228, 3, v228 op_sel_hi:[0,1]\n\t"
    "v_mfma_f32_16x16x32_bf16 v[92:95], a[212:215], a[12:15], v[92:95]\n\t"
    "ds_read_b64_tr_b16 a[210:211], v238 offset:33920\n\t"
    "v_bfi_b32 v228, s73, v228, v139\n\t"
    "v_mfma_f32_16x16x32_bf16 v[84:87], a[212:215], a[8:11], v[84:87]\n\t"
    "v_and_b32 v229, s73, v155\n\t"
    "v_pk_sub_u16 v229, v229, s72 clamp\n\t"
    "v_mfma_f32_16x16x32_bf16 v[76:79], a[212:215], a[4:7], v[76:79]\n\t"
    "buffer_load_dwordx4 a[88:91], v240, s[64:67], s34 offen offset:2048 sc1\n\t"
    "v_pk_lshlrev_b16 v229, 3, v229 op_sel_hi:[0,1]\n\t"
    "v_mfma_f32_16x16x32_bf16 v[68:71], a[212:215], a[0:3], v[68:71]\n\t"
    "s_waitcnt lgkmcnt(13)\n\t"
    "ds_read_b64_tr_b16 a[212:213], v238 offset:32904\n\t"
    "v_bfi_b32 v229, s73, v229, v155\n\t"
    "v_mfma_f32_16x16x32_bf16 v[96:99], a[216:219], a[0:3], v[96:99]\n\t"
    "ds_read_b64_tr_b16 a[214:215], v238 offset:33928\n\t"
    "v_pk_mul_f16 v228, v228, s60\n\t"
    "v_mfma_f32_16x16x32_bf16 v[104:107], a[216:219], a[4:7], v[104:107]\n\t"
    "v_pk_fma_f16 v139, v229, s60, v228\n\t"
    "v_pk_fma_f16 v155, v229, s60, v228 neg_lo:[1,0,0] neg_hi:[1,0,0]\n\t"
    "v_mfma_f32_16x16x32_bf16 v[112:115], a[216:219], a[8:11], v[112:115]\n\t"
    "buffer_load_dwordx4 a[92:95], v240, s[64:67], s34 offen offset:3072 sc1\n\t"
    "v_and_b32 v226, s73, v140\n\t"
    "v_mfma_f32_16x16x32_bf16 v[120:123], a[216:219], a[12:15], v[120:123]\n\t"
    "s_waitcnt lgkmcnt(13)\n\t"
    "ds_read_b64_tr_b16 a[216:217], v239 offset:32896\n\t"
    "v_pk_sub_u16 v226, v226, s72 clamp\n\t"
    "v_mfma_f32_16x16x32_bf16 v[124:127], a[220:223], a[12:15], v[124:127]\n\t"
    "ds_read_b64_tr_b16 a[218:219], v239 offset:33920\n\t"
    "v_pk_lshlrev_b16 v226, 3, v226 op_sel_hi:[0,1]\n\t"
    "v_mfma_f32_16x16x32_bf16 v[116:119], a[220:223], a[8:11], v[116:119]\n\t"
    "v_bfi_b32 v226, s73, v226, v140\n\t"
    "v_and_b32 v227, s73, v156\n\t"
    "v_mfma_f32_16x16x32_bf16 v[108:111], a[220:223], a[4:7], v[108:111]\n\t"
    "s_mov_b32 s34, 245760\n\t"
    "buffer_load_dwordx4 a[96:99], v240, s[64:67], s34 offen offset:0 sc1\n\t"
    "v_pk_sub_u16 v227, v227, s72 clamp\n\t"
    "v_pk_lshlrev_b16 v227, 3, v227 op_sel_hi:[0,1]\n\t"
    "v_bfi_b32 v227, s73, v227, v156\n\t"
    "s_lshr_b32 s38, s41, 8\n\t"
    "s_xor_b32 s38, s38, s30\n\t"
    "s_and_b32 s38, s38, 0x2000\n\t"
    "v_mfma_f32_16x16x32_bf16 v[100:103], a[220:223], a[0:3], v[100:103]\n\t"
    "s_waitcnt vmcnt(27) lgkmcnt(13)\n\t"
    "ds_read_b64_tr_b16 a[220:221], v239 offset:32904\n\t"
    "v_mfma_f32_16x16x32_bf16 v[0:3], a[192:195], a[16:19], v[0:3]\n\t"
    "ds_read_b64_tr_b16 a[222:223], v239 offset:33928\n\t"
    "v_mfma_f32_16x16x32_bf16 v[8:11], a[192:195], a[20:23], v[8:11]\n\t"
    "v_pk_mul_f16 v226, v226, s60\n\t"
    "v_pk_fma_f16 v140, v227, s60, v226\n\t"
    "v_mfma_f32_16x16x32_bf16 v[16:19], a[192:195], a[24:27], v[16:19]\n\t"
    "buffer_load_dwordx4 a[100:103], v240, s[64:67], s34 offen offset:1024 sc1\n\t"
    "v_pk_fma_f16 v156, v227, s60, v226 neg_lo:[1,0,0] neg_hi:[1,0,0]\n\t"
    "v_mfma_f32_16x16x32_bf16 v[24:27], a[192:195], a[28:31], v[24:27]\n\t"
    "s_waitcnt lgkmcnt(12)\n\t"
    "ds_read_b64_tr_b16 a[192:193], v236 offset:41120\n\t"
    "v_and_b32 v228, s73, v141\n\t"
    "v_mfma_f32_16x16x32_bf16 v[28:31], a[196:199], a[28:31], v[28:31]\n\t"
    "ds_read_b64_tr_b16 a[194:195], v236 offset:42144\n\t"
    "v_pk_sub_u16 v228, v228, s72 clamp\n\t"
    "v_mfma_f32_16x16x32_bf16 v[20:23], a[196:199], a[24:27], v[20:23]\n\t"
    "v_pk_lshlrev_b16 v228, 3, v228 op_sel_hi:[0,1]\n\t"
    "v_bfi_b32 v228, s73, v228, v141\n\t"
    "v_mfma_f32_16x16x32_bf16 v[12:15], a[196:199], a[20:23], v[12:15]\n\t"
    "buffer_load_dwordx4 a[104:107], v240, s[64:67], s34 offen offset:2048 sc1\n\t"
    "v_and_b32 v229, s73, v157\n\t"
    "v_mfma_f32_16x16x32_bf16 v[4:7], a[196:199], a[16:19], v[4:7]\n\t"
    "s_waitcnt lgkmcnt(12)\n\t"
    "ds_read_b64_tr_b16 a[196:197], v236 offset:41128\n\t"
    "v_pk_sub_u16 v229, v229, s72 clamp\n\t"
    "v_mfma_f32_16x16x32_bf16 v[32:35], a[200:203], a[16:19], v[32:35]\n\t"
    "ds_read_b64_tr_b16 a[198:199], v236 offset:42152\n\t"
    "v_pk_lshlrev_b16 v229, 3, v229 op_sel_hi:[0,1]\n\t"
    "v_mfma_f32_16x16x32_bf16 v[40:43], a[200:203], a[20:23], v[40:43]\n\t"
    "v_bfi_b32 v229, s73, v229, v157\n\t"
    "v_pk_mul_f16 v228, v228, s60\n\t"
    "v_mfma_f32_16x16x32_bf16 v[48:51], a[200:203], a[24:27], v[48:51]\n\t"
    "buffer_load_dwordx4 a[108:111], v240, s[64:67], s34 offen offset:3072 sc1\n\t"
    "v_pk_fma_f16 v141, v229, s60, v228\n\t"
    "v_mfma_f32_16x16x32_bf16 v[56:59], a[200:203], a[28:31], v[56:59]\n\t"
    "s_waitcnt lgkmcnt(12)\n\t"
    "ds_read_b64_tr_b16 a[200:201], v237 offset:41120\n\t"
    "v_pk_fma_f16 v157, v229, s60, v228 neg_lo:[1,0,0] neg_hi:[1,0,0]\n\t"
    "v_mfma_f32_16x16x32_bf16 v[60:63], a[204:207], a[28:31], v[60:63]\n\t"
    "ds_read_b64_tr_b16 a[202:203], v237 offset:42144\n\t"
    "v_and_b32 v226, s73, v142\n\t"
    "v_mfma_f32_16x16x32_bf16 v[52:55], a[204:207], a[24:27], v[52:55]\n\t"
    "v_pk_sub_u16 v226, v226, s72 clamp\n\t"
    "v_pk_lshlrev_b16 v226, 3, v226 op_sel_hi:[0,1]\n\t"
    "v_mfma_f32_16x16x32_bf16 v[44:47], a[204:207], a[20:23], v[44:47]\n\t"
    "v_bfi_b32 v226, s73, v226, v142\n\t"
    "v_and_b32 v227, s73, v158\n\t"
    "v_mfma_f32_16x16x32_bf16 v[36:39], a[204:207], a[16:19], v[36:39]\n\t"
    "s_waitcnt lgkmcnt(12)\n\t"
    "ds_read_b64_tr_b16 a[204:205], v237 offset:41128\n\t"
    "v_pk_sub_u16 v227, v227, s72 clamp\n\t"
    "v_mfma_f32_16x16x32_bf16 v[64:67], a[208:211], a[16:19], v[64:67]\n\t"
    "ds_read_b64_tr_b16 a[206:207], v237 offset:42152\n\t"
    "v_pk_lshlrev_b16 v227, 3, v227 op_sel_hi:[0,1]\n\t"
    "v_mfma_f32_16x16x32_bf16 v[72:75], a[208:211], a[20:23], v[72:75]\n\t"
    "v_bfi_b32 v227, s73, v227, v158\n\t"
    "v_pk_mul_f16 v226, v226, s60\n\t"
    "v_mfma_f32_16x16x32_bf16 v[80:83], a[208:211], a[24:27], v[80:83]\n\t"
    "s_mov_b32 s34, 249856\n\t"
    "buffer_load_dwordx4 a[112:115], v240, s[64:67], s34 offen offset:0 sc1\n\t"
    "v_pk_fma_f16 v142, v227, s60, v226\n\t"
    "v_mfma_f32_16x16x32_bf16 v[88:91], a[208:211], a[28:31], v[88:91]\n\t"
    "s_waitcnt lgkmcnt(12)\n\t"
    "ds_read_b64_tr_b16 a[208:209], v238 offset:41120\n\t"
    "v_pk_fma_f16 v158, v227, s60, v226 neg_lo:[1,0,0] neg_hi:[1,0,0]\n\t"
    "v_mfma_f32_16x16x32_bf16 v[92:95], a[212:215], a[28:31], v[92:95]\n\t"
    "ds_read_b64_tr_b16 a[210:211], v238 offset:42144\n\t"
    "v_and_b32 v228, s73, v143\n\t"
    "v_mfma_f32_16x16x32_bf16 v[84:87], a[212:215], a[24:27], v[84:87]\n\t"
    "v_pk_sub_u16 v228, v228, s72 clamp\n\t"
    "v_pk_lshlrev_b16 v228, 3, v228 op_sel_hi:[0,1]\n\t"
    "v_mfma_f32_16x16x32_bf16 v[76:79], a[212:215], a[20:23], v[76:79]\n\t"
    "buffer_load_dwordx4 a[116:119], v240, s[64:67], s34 offen offset:1024 sc1\n\t"
    "v_bfi_b32 v228, s73, v228, v143\n\t"
    "v_mfma_f32_16x16x32_bf16 v[68:71], a[212:215], a[16:19], v[68:71]\n\t"
    "s_waitcnt lgkmcnt(12)\n\t"
    "ds_read_b64_tr_b16 a[212:213], v238 offset:41128\n\t"
    "v_and_b32 v229, s73, v159\n\t"
    "v_mfma_f32_16x16x32_bf16 v[96:99], a[216:219], a[16:19], v[96:99]\n\t"
    "ds_read_b64_tr_b16 a[214:215], v238 offset:42152\n\t"
    "v_pk_sub_u16 v229, v229, s72 clamp\n\t"
    "v_mfma_f32_16x16x32_bf16 v[104:107], a[216:219], a[20:23], v[104:107]\n\t"
    "v_pk_lshlrev_b16 v229, 3, v229 op_sel_hi:[0,1]\n\t"
    "v_bfi_b32 v229, s73, v229, v159\n\t"
    "v_mfma_f32_16x16x32_bf16 v[112:115], a[216:219], a[24:27], v[112:115]\n\t"
    "buffer_load_dwordx4 a[120:123], v240, s[64:67], s34 offen offset:2048 sc1\n\t"
    "v_pk_mul_f16 v228, v228, s60\n\t"
    "v_mfma_f32_16x16x32_bf16 v[120:123], a[216:219], a[28:31], v[120:123]\n\t"
    "s_waitcnt lgkmcnt(12)\n\t"
    "ds_read_b64_tr_b16 a[216:217], v239 offset:41120\n\t"
    "v_pk_fma_f16 v143, v229, s60, v228\n\t"
    "v_mfma_f32_16x16x32_bf16 v[124:127], a[220:223], a[28:31], v[124:127]\n\t"
    "s_or_b32 s41, s41, s38\n\t"
    "ds_read_b64_tr_b16 a[218:219], v239 offset:42144\n\t"
    "s_add_u32 s41, s41, 20971520\n\t"
    "v_mfma_f32_16x16x32_bf16 v[116:119], a[220:223], a[24:27], v[116:119]\n\t"
    "s_lshl_b32 s38, s26, 15\n\t"
    "v_mfma_f32_16x16x32_bf16 v[108:111], a[220:223], a[20:23], v[108:111]\n\t"
    "buffer_load_dwordx4 a[124:127], v240, s[64:67], s34 offen offset:3072 sc1\n\t"
    "v_mfma_f32_16x16x32_bf16 v[100:103], a[220:223], a[16:19], v[100:103]\n\t"
    "s_waitcnt vmcnt(20) lgkmcnt(12)\n\t"
    "ds_read_b64_tr_b16 a[220:221], v239 offset:41128\n\t"
    "v_mfma_f32_16x16x32_bf16 v[0:3], a[192:195], a[32:35], v[0:3]\n\t"
    "s_barrier\n\t"
    "v_readlane_b32 s24, v246, 32\n\t"
    "v_mfma_f32_16x16x32_bf16 v[8:11], a[192:195], a[36:39], v[8:11]\n\t"
    "s_add_u32 m0, s31, 32896\n\t"
    "ds_read_b64_tr_b16 a[222:223], v239 offset:42152\n\t"
    "v_pk_fma_f16 v159, v229, s60, v228 neg_lo:[1,0,0] neg_hi:[1,0,0]\n\t"
    "v_mfma_f32_16x16x32_bf16 v[16:19], a[192:195], a[40:43], v[16:19]\n\t"
    "s_lshl_b32 s35, s24, 4\n\t"
    "buffer_load_dwordx4 v243, s[96:99], s35 offen sc1 lds\n\t"
    "v_pk_fma_f16 v128, v144, v176, v128 op_sel_hi:[1,0,1]\n\t"
    "v_mfma_f32_16x16x32_bf16 v[24:27], a[192:195], a[44:47], v[24:27]\n\t"
    "s_waitcnt lgkmcnt(12)\n\t"
    "ds_read_b64_tr_b16 a[192:193], v236 offset:49344\n\t"
    "v_pk_mul_f16 v144, v144, v176 op_sel:[0,1] op_sel_hi:[1,1]\n\t"
    "v_mfma_f32_16x16x32_bf16 v[28:31], a[196:199], a[44:47], v[28:31]\n\t"
    "ds_read_b64_tr_b16 a[194:195], v236 offset:50368\n\t"
    "v_pk_fma_f16 v129, v145, v177, v129 op_sel_hi:[1,0,1]\n\t"
    "v_mfma_f32_16x16x32_bf16 v[20:23], a[196:199], a[40:43], v[20:23]\n\t"
    "v_pk_mul_f16 v145, v145, v177 op_sel:[0,1] op_sel_hi:[1,1]\n\t"
    "v_pk_fma_f16 v130, v146, v178, v130 op_sel_hi:[1,0,1]\n\t"
    "v_mfma_f32_16x16x32_bf16 v[12:15], a[196:199], a[36:39], v[12:15]\n\t"
    "buffer_load_dwordx4 v244, s[96:99], s35 offen offset:1024 sc1 lds\n\t"
    "v_pk_mul_f16 v146, v146, v178 op_sel:[0,1] op_sel_hi:[1,1]\n\t"
    "v_mfma_f32_16x16x32_bf16 v[4:7], a[196:199], a[32:35], v[4:7]\n\t"
    "s_waitcnt lgkmcnt(12)\n\t"
    "ds_read_b64_tr_b16 a[196:197], v236 offset:49352\n\t"
    "v_pk_fma_f16 v131, v147, v179, v131 op_sel_hi:[1,0,1]\n\t"
    "v_mfma_f32_16x16x32_bf16 v[32:35], a[200:203], a[32:35], v[32:35]\n\t"
    "ds_read_b64_tr_b16 a[198:199], v236 offset:50376\n\t"
    "v_readlane_b32 s24, v246, 33\n\t"
    "v_mfma_f32_16x16x32_bf16 v[40:43], a[200:203], a[36:39], v[40:43]\n\t"
    "s_add_u32 m0, s31, 41120\n\t"
    "v_pk_mul_f16 v147, v147, v179 op_sel:[0,1] op_sel_hi:[1,1]\n\t"
    "v_pk_fma_f16 v132, v148, v180, v132 op_sel_hi:[1,0,1]\n\t"
    "v_mfma_f32_16x16x32_bf16 v[48:51], a[200:203], a[40:43], v[48:51]\n\t"
    "s_lshl_b32 s35, s24, 4\n\t"
    "buffer_load_dwordx4 v243, s[96:99], s35 offen sc1 lds\n\t"
    "v_pk_mul_f16 v148, v148, v180 op_sel:[0,1] op_sel_hi:[1,1]\n\t"
    "v_mfma_f32_16x16x32_bf16 v[56:59], a[200:203], a[44:47], v[56:59]\n\t"
    "s_waitcnt lgkmcnt(12)\n\t"
    "ds_read_b64_tr_b16 a[200:201], v237 offset:49344\n\t"
    "v_pk_fma_f16 v133, v149, v181, v133 op_sel_hi:[1,0,1]\n\t"
    "v_mfma_f32_16x16x32_bf16 v[60:63], a[204:207], a[44:47], v[60:63]\n\t"
    "ds_read_b64_tr_b16 a[202:203], v237 offset:50368\n\t"
    "v_pk_mul_f16 v149, v149, v181 op_sel:[0,1] op_sel_hi:[1,1]\n\t"
    "v_mfma_f32_16x16x32_bf16 v[52:55], a[204:207], a[40:43], v[52:55]\n\t"
    "v_pk_fma_f16 v134, v150, v182, v134 op_sel_hi:[1,0,1]\n\t"
    "v_pk_mul_f16 v150, v150, v182 op_sel:[0,1] op_sel_hi:[1,1]\n\t"
    "v_mfma_f32_16x16x32_bf16 v[44:47], a[204:207], a[36:39], v[44:47]\n\t"
    "v_pk_fma_f16 v135, v151, v183, v135 op_sel_hi:[1,0,1]\n\t"
    "v_pk_mul_f16 v151, v151, v183 op_sel:[0,1] op_sel_hi:[1,1]\n\t"
    "v_mfma_f32_16x16x32_bf16 v[36:39], a[204:207], a[32:35], v[36:39]\n\t"
    "s_waitcnt lgkmcnt(12)\n\t"
    "ds_read_b64_tr_b16 a[204:205], v237 offset:49352\n\t"
    "v_pk_fma_f16 v136, v152, v184, v136 op_sel_hi:[1,0,1]\n\t"
    "v_mfma_f32_16x16x32_bf16 v[64:67], a[208:211], a[32:35], v[64:67]\n\t"
    "ds_read_b64_tr_b16 a[206:207], v237 offset:50376\n\t"
    "v_pk_mul_f16 v152, v152, v184 op_sel:[0,1] op_sel_hi:[1,1]\n\t"
    "v_mfma_f32_16x16x32_bf16 v[72:75], a[208:211], a[36:39], v[72:75]\n\t"
    "v_pk_fma_f16 v137, v153, v185, v137 op_sel_hi:[1,0,1]\n\t"
    "v_pk_mul_f16 v153, v153, v185 op_sel:[0,1] op_sel_hi:[1,1]\n\t"
    "v_mfma_f32_16x16x32_bf16 v[80:83], a[208:211], a[40:43], v[80:83]\n\t"
    "buffer_load_dwordx4 v244, s[96:99], s35 offen offset:1024 sc1 lds\n\t"
    "v_pk_fma_f16 v138, v154, v186, v138 op_sel_hi:[1,0,1]\n\t"
    "v_mfma_f32_16x16x32_bf16 v[88:91], a[208:211], a[44:47], v[88:91]\n\t"
    "s_waitcnt lgkmcnt(12)\n\t"
    "ds_read_b64_tr_b16 a[208:209], v238 offset:49344\n\t"
    "v_pk_mul_f16 v154, v154, v186 op_sel:[0,1] op_sel_hi:[1,1]\n\t"
    "v_mfma_f32_16x16x32_bf16 v[92:95], a[212:215], a[44:47], v[92:95]\n\t"
    "ds_read_b64_tr_b16 a[210:211], v238 offset:50368\n\t"
    "v_readlane_b32 s24, v246, 34\n\t"
    "v_mfma_f32_16x16x32_bf16 v[84:87], a[212:215], a[40:43], v[84:87]\n\t"
    "s_add_u32 m0, s31, 49344\n\t"
    "v_pk_fma_f16 v139, v155, v187, v139 op_sel_hi:[1,0,1]\n\t"
    "v_pk_mul_f16 v155, v155, v187 op_sel:[0,1] op_sel_hi:[1,1]\n\t"
    "v_mfma_f32_16x16x32_bf16 v[76:79], a[212:215], a[36:39], v[76:79]\n\t"
    "s_lshl_b32 s35, s24, 4\n\t"
    "buffer_load_dwordx4 v243, s[96:99], s35 offen sc1 lds\n\t"
    "v_pk_fma_f16 v140, v156, v188, v140 op_sel_hi:[1,0,1]\n\t"
    "v_mfma_f32_16x16x32_bf16 v[68:71], a[212:215], a[32:35], v[68:71]\n\t"
    "s_waitcnt lgkmcnt(12)\n\t"
    "ds_read_b64_tr_b16 a[212:213], v238 offset:49352\n\t"
    "v_pk_mul_f16 v156, v156, v188 op_sel:[0,1] op_sel_hi:[1,1]\n\t"
    "v_mfma_f32_16x16x32_bf16 v[96:99], a[216:219], a[32:35], v[96:99]\n\t"
    "ds_read_b64_tr_b16 a[214:215], v238 offset:50376\n\t"
    "v_pk_fma_f16 v141, v157, v189, v141 op_sel_hi:[1,0,1]\n\t"
    "v_mfma_f32_16x16x32_bf16 v[104:107], a[216:219], a[36:39], v[104:107]\n\t"
    "v_pk_mul_f16 v157, v157, v189 op_sel:[0,1] op_sel_hi:[1,1]\n\t"
    "v_pk_fma_f16 v142, v158, v190, v142 op_sel_hi:[1,0,1]\n\t"
    "v_mfma_f32_16x16x32_bf16 v[112:115], a[216:219], a[40:43], v[112:115]\n\t"
    "buffer_load_dwordx4 v244, s[96:99], s35 offen offset:1024 sc1 lds\n\t"
    "v_pk_mul_f16 v158, v158, v190 op_sel:[0,1] op_sel_hi:[1,1]\n\t"
    "v_mfma_f32_16x16x32_bf16 v[120:123], a[216:219], a[44:47], v[120:123]\n\t"
    "s_waitcnt lgkmcnt(12)\n\t"
    "ds_read_b64_tr_b16 a[216:217], v239 offset:49344\n\t"
    "v_pk_fma_f16 v143, v159, v191, v143 op_sel_hi:[1,0,1]\n\t"
    "v_mfma_f32_16x16x32_bf16 v[124:127], a[220:223], a[44:47], v[124:127]\n\t"
    "ds_read_b64_tr_b16 a[218:219], v239 offset:50368\n\t"
    "v_readlane_b32 s24, v246, 35\n\t"
    "v_mfma_f32_16x16x32_bf16 v[116:119], a[220:223], a[40:43], v[116:119]\n\t"
    "s_add_u32 m0, s31, 57568\n\t"
    "v_pk_mul_f16 v159, v159, v191 op_sel:[0,1] op_sel_hi:[1,1]\n\t"
    "s_add_u32 s38, s38, s41\n\t"
    "v_mfma_f32_16x16x32_bf16 v[108:111], a[220:223], a[36:39], v[108:111]\n\t"
    "s_lshl_b32 s35, s24, 4\n\t"
    "buffer_load_dwordx4 v243, s[96:99], s35 offen sc1 lds\n\t"
    "s_add_u32 s38, s38, 0x1000\n\t"
    "s_add_u32 s46, s20, s38\n\t"
    "s_addc_u32 s47, s21, 0\n\t"
    "v_mfma_f32_16x16x32_bf16 v[100:103], a[220:223], a[32:35], v[100:103]\n\t"
    "s_waitcnt vmcnt(23) lgkmcnt(12)\n\t"
    "ds_read_b64_tr_b16 a[220:221], v239 offset:49352\n\t"
    "v_mfma_f32_16x16x32_bf16 v[0:3], a[192:195], a[48:51], v[0:3]\n\t"
    "ds_read_b64_tr_b16 a[222:223], v239 offset:50376\n\t"
    "v_mfma_f32_16x16x32_bf16 v[8:11], a[192:195], a[52:55], v[8:11]\n\t"
    "s_lshl_b32 s38, s22, 12\n\t"
    "ds_read_b64_tr_b16 a[128:129], v232 offset:0\n\t"
    "v_mfma_f32_16x16x32_bf16 v[16:19], a[192:195], a[56:59], v[16:19]\n\t"
    "s_add_u32 s39, s38, 133632\n\t"
    "v_mfma_f32_16x16x32_bf16 v[24:27], a[192:195], a[60:63], v[24:27]\n\t"
    "s_waitcnt lgkmcnt(13)\n\t"
    "ds_read_b64_tr_b16 a[130:131], v232 offset:1024\n\t"
    "v_mfma_f32_16x16x32_bf16 v[28:31], a[196:199], a[60:63], v[28:31]\n\t"
    "ds_read_b64_tr_b16 a[132:133], v232 offset:8\n\t"
    "v_add_u32 v241, s39, v240\n\t"
    "v_mfma_f32_16x16x32_bf16 v[20:23], a[196:199], a[56:59], v[20:23]\n\t"
    "s_xor_b32 s39, s38, 0x2000\n\t"
    "ds_read_b64_tr_b16 a[134:135], v232 offset:1032\n\t"
    "v_mfma_f32_16x16x32_bf16 v[12:15], a[196:199], a[52:55], v[12:15]\n\t"
    "s_add_u32 s39, s39, 133632\n\t"
    "v_mfma_f32_16x16x32_bf16 v[4:7], a[196:199], a[48:51], v[4:7]\n\t"
    "s_waitcnt lgkmcnt(14)\n\t"
    "ds_read_b64_tr_b16 a[136:137], v232 offset:8232\n\t"
    "v_mfma_f32_16x16x32_bf16 v[32:35], a[200:203], a[48:51], v[32:35]\n\t"
    "ds_read_b64_tr_b16 a[138:139], v232 offset:9256\n\t"
    "v_add_u32 v242, s39, v240\n\t"
    "v_mfma_f32_16x16x32_bf16 v[40:43], a[200:203], a[52:55], v[40:43]\n\t"
    "s_lshl_b32 s38, s22, 12\n\t"
    "ds_read_b64_tr_b16 a[140:141], v232 offset:8224\n\t"
    "v_mfma_f32_16x16x32_bf16 v[48:51], a[200:203], a[56:59], v[48:51]\n\t"
    "s_xor_b32 s38, s38, s30\n\t"
    "v_mfma_f32_16x16x32_bf16 v[56:59], a[200:203], a[60:63], v[56:59]\n\t"
    "s_waitcnt lgkmcnt(15)\n\t"
    "ds_read_b64_tr_b16 a[142:143], v232 offset:9248\n\t"
    "v_mfma_f32_16x16x32_bf16 v[60:63], a[204:207], a[60:63], v[60:63]\n\t"
    "s_and_b32 s39, s29, 255\n\t"
    "ds_read_b64_tr_b16 a[144:145], v232 offset:16448\n\t"
    "v_mfma_f32_16x16x32_bf16 v[52:55], a[204:207], a[56:59], v[52:55]\n\t"
    "s_lshl_b32 s39, s39, 14\n\t"
    "ds_read_b64_tr_b16 a[146:147], v232 offset:17472\n\t"
    "v_mfma_f32_16x16x32_bf16 v[44:47], a[204:207], a[52:55], v[44:47]\n\t"
    "s_add_u32 s38, s38, s39\n\t"
    "v_mfma_f32_16x16x32_bf16 v[36:39], a[204:207], a[48:51], v[36:39]\n\t"
    "s_waitcnt lgkmcnt(15)\n\t"
    "ds_read_b64_tr_b16 a[148:149], v232 offset:16456\n\t"
    "v_mfma_f32_16x16x32_bf16 v[64:67], a[208:211], a[48:51], v[64:67]\n\t"
    "ds_read_b64_tr_b16 a[150:151], v232 offset:17480\n\t"
    "v_mfma_f32_16x16x32_bf16 v[72:75], a[208:211], a[52:55], v[72:75]\n\t"
    "ds_read_b64_tr_b16 a[152:153], v232 offset:24680\n\t"
    "v_mfma_f32_16x16x32_bf16 v[80:83], a[208:211], a[56:59], v[80:83]\n\t"
    "buffer_load_dwordx4 v244, s[96:99], s35 offen offset:1024 sc1 lds\n\t"
    "v_mfma_f32_16x16x32_bf16 v[88:91], a[208:211], a[60:63], v[88:91]\n\t"
    "s_waitcnt lgkmcnt(15)\n\t"
    "ds_read_b64_tr_b16 a[154:155], v232 offset:25704\n\t"
    "v_mfma_f32_16x16x32_bf16 v[92:95], a[212:215], a[60:63], v[92:95]\n\t"
    "ds_read_b64_tr_b16 a[156:157], v232 offset:24672\n\t"
    "v_mfma_f32_16x16x32_bf16 v[84:87], a[212:215], a[56:59], v[84:87]\n\t"
    "ds_read_b64_tr_b16 a[158:159], v232 offset:25696\n\t"
    "v_mfma_f32_16x16x32_bf16 v[76:79], a[212:215], a[52:55], v[76:79]\n\t"
    "s_add_u32 s38, s38, 0x1000000\n\t"
    "s_add_u32 s50, s20, s38\n\t"
    "s_addc_u32 s51, s21, 0\n\t"
    "v_mfma_f32_16x16x32_bf16 v[68:71], a[212:215], a[48:51], v[68:71]\n\t"
    "v_and_b32 v249, 7, v245\n\t"
    "v_mfma_f32_16x16x32_bf16 v[96:99], a[216:219], a[48:51], v[96:99]\n\t"
    "v_min_u32 v249, 6, v249\n\t"
    "s_and_b32 s38, s2, 0xc7\n\t"
    "v_mfma_f32_16x16x32_bf16 v[104:107], a[216:219], a[52:55], v[104:107]\n\t"
    "s_lshl_b32 s38, s38, 2\n\t"
    "s_add_u32 s38, s38, s22\n\t"
    "v_mfma_f32_16x16x32_bf16 v[112:115], a[216:219], a[56:59], v[112:115]\n\t"
    "s_lshl_b32 s38, s38, 9\n\t"
    "s_add_u32 s38, s38, 262144\n\t"
    "v_mfma_f32_16x16x32_bf16 v[120:123], a[216:219], a[60:63], v[120:123]\n\t"
    "s_waitcnt lgkmcnt(15)\n\t"
    "s_add_u32 s38, s38, s6\n\t"
    "v_mfma_f32_16x16x32_bf16 v[124:127], a[220:223], a[60:63], v[124:127]\n\t"
    "v_lshl_add_u32 v249, v249, 14, s38\n\t"
    "v_mfma_f32_16x16x32_bf16 v[116:119], a[220:223], a[56:59], v[116:119]\n\t"
    "v_mfma_f32_16x16x32_bf16 v[108:111], a[220:223], a[52:55], v[108:111]\n\t"
    "v_mfma_f32_16x16x32_bf16 v[100:103], a[220:223], a[48:51], v[100:103]\n\t"
    "s_waitcnt vmcnt(23) lgkmcnt(14)\n\t"
    "v_mfma_f32_16x16x32_bf16 v[0:3], a[128:131], a[64:67], v[0:3]\n\t"
    "s_waitcnt lgkmcnt(8)\n\t"
    "ds_read_b64_tr_b16 a[160:161], v233 offset:0\n\t"
    "v_mfma_f32_16x16x32_bf16 v[4:7], a[132:135], a[64:67], v[4:7]\n\t"
    "s_add_u32 s64, s20, s41\n\t"
    "s_addc_u32 s65, s21, 0\n\t"
    "ds_read_b64_tr_b16 a[162:163], v233 offset:1024\n\t"
    "s_waitcnt vmcnt(19)\n\t"
    "v_mfma_f32_16x16x32_bf16 v[4:7], a[136:139], a[80:83], v[4:7]\n\t"
    "s_and_b32 s65, s65, 0xffff\n\t"
    "ds_read_b64_tr_b16 a[164:165], v233 offset:8\n\t"
    "v_mfma_f32_16x16x32_bf16 v[0:3], a[140:143], a[80:83], v[0:3]\n\t"
    "s_waitcnt lgkmcnt(9)\n\t"
    "s_mov_b32 s66, 0x36000\n\t"
    "s_waitcnt vmcnt(15)\n\t"
    "v_mfma_f32_16x16x32_bf16 v[0:3], a[144:147], a[96:99], v[0:3]\n\t"
    "s_waitcnt lgkmcnt(7)\n\t"
    "ds_read_b64_tr_b16 a[166:167], v233 offset:1032\n\t"
    "s_mov_b32 s67, 0x20000\n\t"
    "v_mfma_f32_16x16x32_bf16 v[4:7], a[148:151], a[96:99], v[4:7]\n\t"
    "s_waitcnt lgkmcnt(6)\n\t"
    "s_waitcnt vmcnt(11)\n\t"
    "v_mfma_f32_16x16x32_bf16 v[4:7], a[152:155], a[112:115], v[4:7]\n\t"
    "s_waitcnt lgkmcnt(4)\n\t"
    "ds_read_b64_tr_b16 a[168:169], v233 offset:8232\n\t"
    "v_mfma_f32_16x16x32_bf16 v[0:3], a[156:159], a[112:115], v[0:3]\n\t"
    "v_mfma_f32_16x16x32_bf16 v[8:11], a[128:131], a[68:71], v[8:11]\n\t"
    "ds_read_b64_tr_b16 a[170:171], v233 offset:9256\n\t"
    "v_mfma_f32_16x16x32_bf16 v[12:15], a[132:135], a[68:71], v[12:15]\n\t"
    "ds_read_b64_tr_b16 a[172:173], v233 offset:8224\n\t"
    "v_mfma_f32_16x16x32_bf16 v[12:15], a[136:139], a[84:87], v[12:15]\n\t"
    "ds_read_b64_tr_b16 a[174:175], v233 offset:9248\n\t"
    "v_mfma_f32_16x16x32_bf16 v[8:11], a[140:143], a[84:87], v[8:11]\n\t"
    "s_nop 0\n\t"
    "v_mul_f32_e64 v0, v0, |v0|\n\t"
    "v_mul_f32_e64 v1, v1, |v1|\n\t"
    "v_mul_f32_e64 v2, v2, |v2|\n\t"
    "v_mfma_f32_16x16x32_bf16 v[8:11], a[144:147], a[100:103], v[8:11]\n\t"
    "ds_read_b64_tr_b16 a[176:177], v233 offset:16448\n\t"
    "v_mul_f32_e64 v3, v3, |v3|\n\t"
    "v_mfma_f32_16x16x32_bf16 v[12:15], a[148:151], a[100:103], v[12:15]\n\t"
    "v_mul_f32_e64 v4, v4, |v4|\n\t"
    "v_mul_f32_e64 v5, v5, |v5|\n\t"
    "v_mul_f32_e64 v6, v6, |v6|\n\t"
    "s_waitcnt vmcnt(10)\n\t"
    "v_mfma_f32_16x16x32_bf16 v[12:15], a[152:155], a[116:119], v[12:15]\n\t"
    "ds_read_b64_tr_b16 a[178:179], v233 offset:17472\n\t"
    "v_mul_f32_e64 v7, v7, |v7|\n\t"
    "v_cvt_pk_bf16_f32 v224, v0, v1\n\t"
    "v_mfma_f32_16x16x32_bf16 v[8:11], a[156:159], a[116:119], v[8:11]\n\t"
    "v_cvt_pk_bf16_f32 v225, v2, v3\n\t"
    "v_cvt_pk_bf16_f32 v226, v4, v5\n\t"
    "v_cvt_pk_bf16_f32 v227, v6, v7\n\t"
    "v_mfma_f32_16x16x32_bf16 v[16:19], a[128:131], a[72:75], v[16:19]\n\t"
    "ds_read_b64_tr_b16 a[180:181], v233 offset:16456\n\t"
    "v_pk_max_i16 v224, v224, 0\n\t"
    "v_mfma_f32_16x16x32_bf16 v[20:23], a[132:135], a[72:75], v[20:23]\n\t"
    "ds_read_b64_tr_b16 a[182:183], v233 offset:17480\n\t"
    "v_pk_max_i16 v225, v225, 0\n\t"
    "v_pk_max_i16 v226, v226, 0\n\t"
    "v_mfma_f32_16x16x32_bf16 v[20:23], a[136:139], a[88:91], v[20:23]\n\t"
    "ds_read_b64_tr_b16 a[184:185], v233 offset:24680\n\t"
    "v_pk_max_i16 v227, v227, 0\n\t"
    "v_accvgpr_write_b32 a0, v224\n\t"
    "v_mfma_f32_16x16x32_bf16 v[16:19], a[140:143], a[88:91], v[16:19]\n\t"
    "global_store_dwordx4 v240, v[224:227], s[46:47] offset:-4096 sc1\n\t"
    "v_accvgpr_write_b32 a1, v225\n\t"
    "v_accvgpr_write_b32 a2, v226\n\t"
    "v_mfma_f32_16x16x32_bf16 v[16:19], a[144:147], a[104:107], v[16:19]\n\t"
    "ds_read_b64_tr_b16 a[186:187], v233 offset:25704\n\t"
    "v_accvgpr_write_b32 a3, v227\n\t"
    "v_mfma_f32_16x16x32_bf16 v[20:23], a[148:151], a[104:107], v[20:23]\n\t"
    "v_mul_f32_e64 v8, v8, |v8|\n\t"
    "v_mul_f32_e64 v9, v9, |v9|\n\t"
    "v_mul_f32_e64 v10, v10, |v10|\n\t"
    "s_waitcnt vmcnt(10)\n\t"
    "v_mfma_f32_16x16x32_bf16 v[20:23], a[152:155], a[120:123], v[20:23]\n\t"
    "ds_read_b64_tr_b16 a[188:189], v233 offset:24672\n\t"
    "v_mul_f32_e64 v11, v11, |v11|\n\t"
    "v_mul_f32_e64 v12, v12, |v12|\n\t"
    "v_mfma_f32_16x16x32_bf16 v[16:19], a[156:159], a[120:123], v[16:19]\n\t"
    "v_mul_f32_e64 v13, v13, |v13|\n\t"
    "v_mul_f32_e64 v14, v14, |v14|\n\t"
    "v_mul_f32_e64 v15, v15, |v15|\n\t"
    "v_mfma_f32_16x16x32_bf16 v[24:27], a[128:131], a[76:79], v[24:27]\n\t"
    "ds_read_b64_tr_b16 a[190:191], v233 offset:25696\n\t"
    "v_cvt_pk_bf16_f32 v228, v8, v9\n\t"
    "v_mfma_f32_16x16x32_bf16 v[28:31], a[132:135], a[76:79], v[28:31]\n\t"
    "v_cvt_pk_bf16_f32 v229, v10, v11\n\t"
    "v_cvt_pk_bf16_f32 v230, v12, v13\n\t"
    "v_mfma_f32_16x16x32_bf16 v[28:31], a[136:139], a[92:95], v[28:31]\n\t"
    "v_cvt_pk_bf16_f32 v231, v14, v15\n\t"
    "v_pk_max_i16 v228, v228, 0\n\t"
    "v_mfma_f32_16x16x32_bf16 v[24:27], a[140:143], a[92:95], v[24:27]\n\t"
    "v_pk_max_i16 v229, v229, 0\n\t"
    "v_pk_max_i16 v230, v230, 0\n\t"
    "v_pk_max_i16 v231, v231, 0\n\t"
    "v_mfma_f32_16x16x32_bf16 v[24:27], a[144:147], a[108:111], v[24:27]\n\t"
    "global_store_dwordx4 v240, v[228:231], s[46:47] offset:-3072 sc1\n\t"
    "v_accvgpr_write_b32 a4, v228\n\t"
    "v_mfma_f32_16x16x32_bf16 v[28:31], a[148:151], a[108:111], v[28:31]\n\t"
    "v_accvgpr_write_b32 a5, v229\n\t"
    "v_accvgpr_write_b32 a6, v230\n\t"
    "s_waitcnt vmcnt(10)\n\t"
    "v_mfma_f32_16x16x32_bf16 v[28:31], a[152:155], a[124:127], v[28:31]\n\t"
    "v_accvgpr_write_b32 a7, v231\n\t"
    "v_mul_f32_e64 v16, v16, |v16|\n\t"
    "v_mfma_f32_16x16x32_bf16 v[24:27], a[156:159], a[124:127], v[24:27]\n\t"
    "s_waitcnt lgkmcnt(8)\n\t"
    "v_mul_f32_e64 v17, v17, |v17|\n\t"
    "v_mul_f32_e64 v18, v18, |v18|\n\t"
    "v_mul_f32_e64 v19, v19, |v19|\n\t"
    "v_mfma_f32_16x16x32_bf16 v[32:35], a[160:163], a[64:67], v[32:35]\n\t"
    "ds_read_b64_tr_b16 a[192:193], v234 offset:0\n\t"
    "v_mul_f32_e64 v20, v20, |v20|\n\t"
    "v_mfma_f32_16x16x32_bf16 v[36:39], a[164:167], a[64:67], v[36:39]\n\t"
    "ds_read_b64_tr_b16 a[194:195], v234 offset:1024\n\t"
    "v_mul_f32_e64 v21, v21, |v21|\n\t"
    "v_mul_f32_e64 v22, v22, |v22|\n\t"
    "v_mfma_f32_16x16x32_bf16 v[36:39], a[168:171], a[80:83], v[36:39]\n\t"
    "ds_read_b64_tr_b16 a[196:197], v234 offset:8\n\t"
    "v_mul_f32_e64 v23, v23, |v23|\n\t"
    "v_cvt_pk_bf16_f32 v224, v16, v17\n\t"
    "v_mfma_f32_16x16x32_bf16 v[32:35], a[172:175], a[80:83], v[32:35]\n\t"
    "s_waitcnt lgkmcnt(9)\n\t"
    "v_cvt_pk_bf16_f32 v225, v18, v19\n\t"
    "v_cvt_pk_bf16_f32 v226, v20, v21\n\t"
    "v_cvt_pk_bf16_f32 v227, v22, v23\n\t"
    "v_mfma_f32_16x16x32_bf16 v[32:35], a[176:179], a[96:99], v[32:35]\n\t"
    "s_waitcnt lgkmcnt(7)\n\t"
    "ds_read_b64_tr_b16 a[198:199], v234 offset:1032\n\t"
    "v_pk_max_i16 v224, v224, 0\n\t"
    "v_mfma_f32_16x16x32_bf16 v[36:39], a[180:183], a[96:99], v[36:39]\n\t"
    "s_waitcnt lgkmcnt(6)\n\t"
    "v_pk_max_i16 v225, v225, 0\n\t"
    "v_pk_max_i16 v226, v226, 0\n\t"
    "v_pk_max_i16 v227, v227, 0\n\t"
    "v_mfma_f32_16x16x32_bf16 v[36:39], a[184:187], a[112:115], v[36:39]\n\t"
    "s_waitcnt lgkmcnt(4)\n\t"
    "ds_read_b64_tr_b16 a[200:201], v234 offset:8232\n\t"
    "global_store_dwordx4 v240, v[224:227], s[46:47] offset:-2048 sc1\n\t"
    "v_accvgpr_write_b32 a8, v224\n\t"
    "v_mfma_f32_16x16x32_bf16 v[32:35], a[188:191], a[112:115], v[32:35]\n\t"
    "v_accvgpr_write_b32 a9, v225\n\t"
    "v_accvgpr_write_b32 a10, v226\n\t"
    "v_accvgpr_write_b32 a11, v227\n\t"
    "v_mfma_f32_16x16x32_bf16 v[40:43], a[160:163], a[68:71], v[40:43]\n\t"
    "ds_read_b64_tr_b16 a[202:203], v234 offset:9256\n\t"
    "v_mul_f32_e64 v24, v24, |v24|\n\t"
    "v_mfma_f32_16x16x32_bf16 v[44:47], a[164:167], a[68:71], v[44:47]\n\t"
    "ds_read_b64_tr_b16 a[204:205], v234 offset:8224\n\t"
    "v_mul_f32_e64 v25, v25, |v25|\n\t"
    "v_mul_f32_e64 v26, v26, |v26|\n\t"
    "v_mfma_f32_16x16x32_bf16 v[44:47], a[168:171], a[84:87], v[44:47]\n\t"
    "ds_read_b64_tr_b16 a[206:207], v234 offset:9248\n\t"
    "v_mul_f32_e64 v27, v27, |v27|\n\t"
    "v_mul_f32_e64 v28, v28, |v28|\n\t"
    "v_mfma_f32_16x16x32_bf16 v[40:43], a[172:175], a[84:87], v[40:43]\n\t"
    "v_mul_f32_e64 v29, v29, |v29|\n\t"
    "v_mul_f32_e64 v30, v30, |v30|\n\t"
    "v_mul_f32_e64 v31, v31, |v31|\n\t"
    "v_mfma_f32_16x16x32_bf16 v[40:43], a[176:179], a[100:103], v[40:43]\n\t"
    "ds_read_b64_tr_b16 a[208:209], v234 offset:16448\n\t"
    "v_cvt_pk_bf16_f32 v228, v24, v25\n\t"
    "v_mfma_f32_16x16x32_bf16 v[44:47], a[180:183], a[100:103], v[44:47]\n\t"
    "v_cvt_pk_bf16_f32 v229, v26, v27\n\t"
    "v_cvt_pk_bf16_f32 v230, v28, v29\n\t"
    "v_cvt_pk_bf16_f32 v231, v30, v31\n\t"
    "v_mfma_f32_16x16x32_bf16 v[44:47], a[184:187], a[116:119], v[44:47]\n\t"
    "ds_read_b64_tr_b16 a[210:211], v234 offset:17472\n\t"
    "v_pk_max_i16 v228, v228, 0\n\t"
    "v_pk_max_i16 v229, v229, 0\n\t"
    "v_mfma_f32_16x16x32_bf16 v[40:43], a[188:191], a[116:119], v[40:43]\n\t"
    "v_pk_max_i16 v230, v230, 0\n\t"
    "v_pk_max_i16 v231, v231, 0\n\t"
    "v_accvgpr_write_b32 a12, v228\n\t"
    "v_mfma_f32_16x16x32_bf16 v[48:51], a[160:163], a[72:75], v[48:51]\n\t"
    "ds_read_b64_tr_b16 a[212:213], v234 offset:16456\n\t"
    "global_store_dwordx4 v240, v[228:231], s[46:47] offset:-1024 sc1\n\t"
    "v_mfma_f32_16x16x32_bf16 v[52:55], a[164:167], a[72:75], v[52:55]\n\t"
    "ds_read_b64_tr_b16 a[214:215], v234 offset:17480\n\t"
    "v_accvgpr_write_b32 a13, v229\n\t"
    "v_accvgpr_write_b32 a14, v230\n\t"
    "v_mfma_f32_16x16x32_bf16 v[52:55], a[168:171], a[88:91], v[52:55]\n\t"
    "ds_read_b64_tr_b16 a[216:217], v234 offset:24680\n\t"
    "v_accvgpr_write_b32 a15, v231\n\t"
    "v_mul_f32_e64 v32, v32, |v32|\n\t"
    "v_mfma_f32_16x16x32_bf16 v[48:51], a[172:175], a[88:91], v[48:51]\n\t"
    "v_mul_f32_e64 v33, v33, |v33|\n\t"
    "v_mul_f32_e64 v34, v34, |v34|\n\t"
    "v_mul_f32_e64 v35, v35, |v35|\n\t"
    "v_mfma_f32_16x16x32_bf16 v[48:51], a[176:179], a[104:107], v[48:51]\n\t"
    "ds_read_b64_tr_b16 a[218:219], v234 offset:25704\n\t"
    "v_mul_f32_e64 v36, v36, |v36|\n\t"
    "v_mfma_f32_16x16x32_bf16 v[52:55], a[180:183], a[104:107], v[52:55]\n\t"
    "v_mul_f32_e64 v37, v37, |v37|\n\t"
    "v_mul_f32_e64 v38, v38, |v38|\n\t"
    "v_mul_f32_e64 v39, v39, |v39|\n\t"
    "v_mfma_f32_16x16x32_bf16 v[52:55], a[184:187], a[120:123], v[52:55]\n\t"
    "ds_read_b64_tr_b16 a[220:221], v234 offset:24672\n\t"
    "v_cvt_pk_bf16_f32 v224, v32, v33\n\t"
    "v_cvt_pk_bf16_f32 v225, v34, v35\n\t"
    "v_mfma_f32_16x16x32_bf16 v[48:51], a[188:191], a[120:123], v[48:51]\n\t"
    "v_cvt_pk_bf16_f32 v226, v36, v37\n\t"
    "v_cvt_pk_bf16_f32 v227, v38, v39\n\t"
    "v_pk_max_i16 v224, v224, 0\n\t"
    "v_mfma_f32_16x16x32_bf16 v[56:59], a[160:163], a[76:79], v[56:59]\n\t"
    "Lpad6_%=:\n\t"
    "s_cbranch_execz Lpad10_%=\n\t"
    "ds_read_b64_tr_b16 a[222:223], v234 offset:25696\n\t"
    "v_pk_max_i16 v225, v225, 0\n\t"
    "v_mfma_f32_16x16x32_bf16 v[60:63], a[164:167], a[76:79], v[60:63]\n\t"
    "v_pk_max_i16 v226, v226, 0\n\t"
    "v_pk_max_i16 v227, v227, 0\n\t"
    "v_mfma_f32_16x16x32_bf16 v[60:63], a[168:171], a[92:95], v[60:63]\n\t"
    "global_store_dwordx4 v240, v[224:227], s[46:47] offset:0 sc1\n\t"
    "v_accvgpr_write_b32 a16, v224\n\t"
    "v_mfma_f32_16x16x32_bf16 v[56:59], a[172:175], a[92:95], v[56:59]\n\t"
    "v_accvgpr_write_b32 a17, v225\n\t"
    "v_accvgpr_write_b32 a18, v226\n\t"
    "v_accvgpr_write_b32 a19, v227\n\t"
    "v_mfma_f32_16x16x32_bf16 v[56:59], a[176:179], a[108:111], v[56:59]\n\t"
    "v_mul_f32_e64 v40, v40, |v40|\n\t"
    "v_mul_f32_e64 v41, v41, |v41|\n\t"
    "v_mfma_f32_16x16x32_bf16 v[60:63], a[180:183], a[108:111], v[60:63]\n\t"
    "v_mul_f32_e64 v42, v42, |v42|\n\t"
    "v_mul_f32_e64 v43, v43, |v43|\n\t"
    "v_mfma_f32_16x16x32_bf16 v[60:63], a[184:187], a[124:127], v[60:63]\n\t"
    "v_mul_f32_e64 v44, v44, |v44|\n\t"
    "v_mul_f32_e64 v45, v45, |v45|\n\t"
    "v_mfma_f32_16x16x32_bf16 v[56:59], a[188:191], a[124:127], v[56:59]\n\t"
    "s_waitcnt lgkmcnt(8)\n\t"
    "v_mul_f32_e64 v46, v46, |v46|\n\t"
    "v_mul_f32_e64 v47, v47, |v47|\n\t"
    "v_cvt_pk_bf16_f32 v228, v40, v41\n\t"
    "v_mfma_f32_16x16x32_bf16 v[64:67], a[192:195], a[64:67], v[64:67]\n\t"
    "ds_read_b64_tr_b16 a[128:129], v235 offset:0\n\t"
    "v_cvt_pk_bf16_f32 v229, v42, v43\n\t"
    "v_mfma_f32_16x16x32_bf16 v[68:71], a[196:199], a[64:67], v[68:71]\n\t"
    "ds_read_b64_tr_b16 a[130:131], v235 offset:1024\n\t"
    "v_cvt_pk_bf16_f32 v230, v44, v45\n\t"
    "v_cvt_pk_bf16_f32 v231, v46, v47\n\t"
    "v_mfma_f32_16x16x32_bf16 v[68:71], a[200:203], a[80:83], v[68:71]\n\t"
    "ds_read_b64_tr_b16 a[132:133], v235 offset:8\n\t"
    "v_pk_max_i16 v228, v228, 0\n\t"
    "v_pk_max_i16 v229, v229, 0\n\t"
    "v_mfma_f32_16x16x32_bf16 v[64:67], a[204:207], a[80:83], v[64:67]\n\t"
    "s_waitcnt lgkmcnt(9)\n\t"
    "v_pk_max_i16 v230, v230, 0\n\t"
    "v_pk_max_i16 v231, v231, 0\n\t"
    "v_accvgpr_write_b32 a20, v228\n\t"
    "v_mfma_f32_16x16x32_bf16 v[64:67], a[208:211], a[96:99], v[64:67]\n\t"
    "s_waitcnt lgkmcnt(7)\n\t"
    "ds_read_b64_tr_b16 a[134:135], v235 offset:1032\n\t"
    "global_store_dwordx4 v240, v[228:231], s[46:47] offset:1024 sc1\n\t"
    "v_mfma_f32_16x16x32_bf16 v[68:71], a[212:215], a[96:99], v[68:71]\n\t"
    "s_waitcnt lgkmcnt(6)\n\t"
    "v_accvgpr_write_b32 a21, v229\n\t"
    "v_accvgpr_write_b32 a22, v230\n\t"
    "v_accvgpr_write_b32 a23, v231\n\t"
    "v_mfma_f32_16x16x32_bf16 v[68:71], a[216:219], a[112:115], v[68:71]\n\t"
    "s_waitcnt lgkmcnt(4)\n\t"
    "ds_read_b64_tr_b16 a[136:137], v235 offset:8232\n\t"
    "v_mul_f32_e64 v48, v48, |v48|\n\t"
    "v_mul_f32_e64 v49, v49, |v49|\n\t"
    "v_mfma_f32_16x16x32_bf16 v[64:67], a[220:223], a[112:115], v[64:67]\n\t"
    "v_mul_f32_e64 v50, v50, |v50|\n\t"
    "v_mul_f32_e64 v51, v51, |v51|\n\t"
    "v_mul_f32_e64 v52, v52, |v52|\n\t"
    "v_mfma_f32_16x16x32_bf16 v[72:75], a[192:195], a[68:71], v[72:75]\n\t"
    "ds_read_b64_tr_b16 a[138:139], v235 offset:9256\n\t"
    "v_mul_f32_e64 v53, v53, |v53|\n\t"
    "v_mfma_f32_16x16x32_bf16 v[76:79], a[196:199], a[68:71], v[76:79]\n\t"
    "ds_read_b64_tr_b16 a[140:141], v235 offset:8224\n\t"
    "v_mul_f32_e64 v54, v54, |v54|\n\t"
    "v_mul_f32_e64 v55, v55, |v55|\n\t"
    "v_mfma_f32_16x16x32_bf16 v[76:79], a[200:203], a[84:87], v[76:79]\n\t"
    "ds_read_b64_tr_b16 a[142:143], v235 offset:9248\n\t"
    "v_cvt_pk_bf16_f32 v224, v48, v49\n\t"
    "v_cvt_pk_bf16_f32 v225, v50, v51\n\t"
    "v_mfma_f32_16x16x32_bf16 v[72:75], a[204:207], a[84:87], v[72:75]\n\t"
    "v_cvt_pk_bf16_f32 v226, v52, v53\n\t"
    "v_cvt_pk_bf16_f32 v227, v54, v55\n\t"
    "v_pk_max_i16 v224, v224, 0\n\t"
    "v_mfma_f32_16x16x32_bf16 v[72:75], a[208:211], a[100:103], v[72:75]\n\t"
    "ds_read_b64_tr_b16 a[144:145], v235 offset:16448\n\t"
    "v_pk_max_i16 v225, v225, 0\n\t"
    "v_mfma_f32_16x16x32_bf16 v[76:79], a[212:215], a[100:103], v[76:79]\n\t"
    "v_pk_max_i16 v226, v226, 0\n\t"
    "v_pk_max_i16 v227, v227, 0\n\t"
    "v_accvgpr_write_b32 a24, v224\n\t"
    "v_mfma_f32_16x16x32_bf16 v[76:79], a[216:219], a[116:119], v[76:79]\n\t"
    "ds_read_b64_tr_b16 a[146:147], v235 offset:17472\n\t"
    "global_store_dwordx4 v240, v[224:227], s[46:47] offset:2048 sc1\n\t"
    "v_accvgpr_write_b32 a25, v225\n\t"
    "v_mfma_f32_16x16x32_bf16 v[72:75], a[220:223], a[116:119], v[72:75]\n\t"
    "v_accvgpr_write_b32 a26, v226\n\t"
    "v_accvgpr_write_b32 a27, v227\n\t"
    "v_mul_f32_e64 v56, v56, |v56|\n\t"
    "v_mfma_f32_16x16x32_bf16 v[80:83], a[192:195], a[72:75], v[80:83]\n\t"
    "ds_read_b64_tr_b16 a[148:149], v235 offset:16456\n\t"
    "v_mul_f32_e64 v57, v57, |v57|\n\t"
    "v_mfma_f32_16x16x32_bf16 v[84:87], a[196:199], a[72:75], v[84:87]\n\t"
    "ds_read_b64_tr_b16 a[150:151], v235 offset:17480\n\t"
    "v_mul_f32_e64 v58, v58, |v58|\n\t"
    "v_mul_f32_e64 v59, v59, |v59|\n\t"
    "v_mfma_f32_16x16x32_bf16 v[84:87], a[200:203], a[88:91], v[84:87]\n\t"
    "ds_read_b64_tr_b16 a[152:153], v235 offset:24680\n\t"
    "v_mul_f32_e64 v60, v60, |v60|\n\t"
    "v_mul_f32_e64 v61, v61, |v61|\n\t"
    "v_mfma_f32_16x16x32_bf16 v[80:83], a[204:207], a[88:91], v[80:83]\n\t"
    "v_mul_f32_e64 v62, v62, |v62|\n\t"
    "v_mul_f32_e64 v63, v63, |v63|\n\t"
    "v_cvt_pk_bf16_f32 v228, v56, v57\n\t"
    "v_mfma_f32_16x16x32_bf16 v[80:83], a[208:211], a[104:107], v[80:83]\n\t"
    "ds_read_b64_tr_b16 a[154:155], v235 offset:25704\n\t"
    "v_cvt_pk_bf16_f32 v229, v58, v59\n\t"
    "v_mfma_f32_16x16x32_bf16 v[84:87], a[212:215], a[104:107], v[84:87]\n\t"
    "v_cvt_pk_bf16_f32 v230, v60, v61\n\t"
    "v_cvt_pk_bf16_f32 v231, v62, v63\n\t"
    "v_pk_max_i16 v228, v228, 0\n\t"
    "v_mfma_f32_16x16x32_bf16 v[84:87], a[216:219], a[120:123], v[84:87]\n\t"
    "ds_read_b64_tr_b16 a[156:157], v235 offset:24672\n\t"
    "v_pk_max_i16 v229, v229, 0\n\t"
    "v_pk_max_i16 v230, v230, 0\n\t"
    "v_mfma_f32_16x16x32_bf16 v[80:83], a[220:223], a[120:123], v[80:83]\n\t"
    "v_pk_max_i16 v231, v231, 0\n\t"
    "v_accvgpr_write_b32 a28, v228\n\t"
    "v_accvgpr_write_b32 a29, v229\n\t"
    "v_mfma_f32_16x16x32_bf16 v[88:91], a[192:195], a[76:79], v[88:91]\n\t"
    "ds_read_b64_tr_b16 a[158:159], v235 offset:25696\n\t"
    "global_store_dwordx4 v240, v[228:231], s[46:47] offset:3072 sc1\n\t"
    "v_mfma_f32_16x16x32_bf16 v[92:95], a[196:199], a[76:79], v[92:95]\n\t"
    "s_waitcnt vmcnt(4)\n\t"
    "global_store_dwordx2 v248, v[254:255], s[4:5] offset:0 sc1\n\t"
    "v_accvgpr_write_b32 a30, v230\n\t"
    "v_mfma_f32_16x16x32_bf16 v[92:95], a[200:203], a[92:95], v[92:95]\n\t"
    "v_accvgpr_write_b32 a31, v231\n\t"
    "s_add_u32 s46, s46, 0x4000\n\t"
    "s_addc_u32 s47, s47, 0\n\t"
    "v_mfma_f32_16x16x32_bf16 v[88:91], a[204:207], a[92:95], v[88:91]\n\t"
    "v_mul_f32_e64 v64, v64, |v64|\n\t"
    "v_mul_f32_e64 v65, v65, |v65|\n\t"
    "v_mfma_f32_16x16x32_bf16 v[88:91], a[208:211], a[108:111], v[88:91]\n\t"
    "v_mul_f32_e64 v66, v66, |v66|\n\t"
    "v_mul_f32_e64 v67, v67, |v67|\n\t"
    "v_mfma_f32_16x16x32_bf16 v[92:95], a[212:215], a[108:111], v[92:95]\n\t"
    "v_mul_f32_e64 v68, v68, |v68|\n\t"
    "v_mul_f32_e64 v69, v69, |v69|\n\t"
    "v_mul_f32_e64 v70, v70, |v70|\n\t"
    "v_mfma_f32_16x16x32_bf16 v[92:95], a[216:219], a[124:127], v[92:95]\n\t"
    "v_mul_f32_e64 v71, v71, |v71|\n\t"
    "v_cvt_pk_bf16_f32 v176, v64, v65\n\t"
    "v_mfma_f32_16x16x32_bf16 v[88:91], a[220:223], a[124:127], v[88:91]\n\t"
    "s_waitcnt lgkmcnt(0)\n\t"
    "v_cvt_pk_bf16_f32 v177, v66, v67\n\t"
    "v_cvt_pk_bf16_f32 v178, v68, v69\n\t"
    "v_cvt_pk_bf16_f32 v179, v70, v71\n\t"
    "v_mfma_f32_16x16x32_bf16 v[96:99], a[128:131], a[64:67], v[96:99]\n\t"
    "v_pk_max_i16 v176, v176, 0\n\t"
    "v_pk_max_i16 v177, v177, 0\n\t"
    "v_mfma_f32_16x16x32_bf16 v[100:103], a[132:135], a[64:67], v[100:103]\n\t"
    "v_pk_max_i16 v178, v178, 0\n\t"
    "v_pk_max_i16 v179, v179, 0\n\t"
    "v_mfma_f32_16x16x32_bf16 v[100:103], a[136:139], a[80:83], v[100:103]\n\t"
    "global_store_dwordx4 v240, v[176:179], s[46:47] offset:-4096 sc1\n\t"
    "v_mul_f32_e64 v72, v72, |v72|\n\t"
    "v_mfma_f32_16x16x32_bf16 v[96:99], a[140:143], a[80:83], v[96:99]\n\t"
    "v_mul_f32_e64 v73, v73, |v73|\n\t"
    "v_mul_f32_e64 v74, v74, |v74|\n\t"
    "v_mfma_f32_16x16x32_bf16 v[96:99], a[144:147], a[96:99], v[96:99]\n\t"
    "v_mul_f32_e64 v75, v75, |v75|\n\t"
    "v_mul_f32_e64 v76, v76, |v76|\n\t"
    "v_mfma_f32_16x16x32_bf16 v[100:103], a[148:151], a[96:99], v[100:103]\n\t"
    "v_mul_f32_e64 v77, v77, |v77|\n\t"
    "v_mul_f32_e64 v78, v78, |v78|\n\t"
    "v_mfma_f32_16x16x32_bf16 v[100:103], a[152:155], a[112:115], v[100:103]\n\t"
    "v_mul_f32_e64 v79, v79, |v79|\n\t"
    "v_cvt_pk_bf16_f32 v180, v72, v73\n\t"
    "v_mfma_f32_16x16x32_bf16 v[96:99], a[156:159], a[112:115], v[96:99]\n\t"
    "v_cvt_pk_bf16_f32 v181, v74, v75\n\t"
    "v_cvt_pk_bf16_f32 v182, v76, v77\n\t"
    "v_cvt_pk_bf16_f32 v183, v78, v79\n\t"
    "v_mfma_f32_16x16x32_bf16 v[104:107], a[128:131], a[68:71], v[104:107]\n\t"
    "v_pk_max_i16 v180, v180, 0\n\t"
    "v_pk_max_i16 v181, v181, 0\n\t"
    "v_mfma_f32_16x16x32_bf16 v[108:111], a[132:135], a[68:71], v[108:111]\n\t"
    "v_pk_max_i16 v182, v182, 0\n\t"
    "v_pk_max_i16 v183, v183, 0\n\t"
    "v_mfma_f32_16x16x32_bf16 v[108:111], a[136:139], a[84:87], v[108:111]\n\t"
    "global_store_dwordx4 v240, v[180:183], s[46:47] offset:-3072 sc1\n\t"
    "v_mul_f32_e64 v80, v80, |v80|\n\t"
    "v_mfma_f32_16x16x32_bf16 v[104:107], a[140:143], a[84:87], v[104:107]\n\t"
    "v_mul_f32_e64 v81, v81, |v81|\n\t"
    "v_mul_f32_e64 v82, v82, |v82|\n\t"
    "v_mfma_f32_16x16x32_bf16 v[104:107], a[144:147], a[100:103], v[104:107]\n\t"
    "v_mul_f32_e64 v83, v83, |v83|\n\t"
    "v_mul_f32_e64 v84, v84, |v84|\n\t"
    "v_mfma_f32_16x16x32_bf16 v[108:111], a[148:151], a[100:103], v[108:111]\n\t"
    "v_mul_f32_e64 v85, v85, |v85|\n\t"
    "v_mul_f32_e64 v86, v86, |v86|\n\t"
    "v_mfma_f32_16x16x32_bf16 v[108:111], a[152:155], a[116:119], v[108:111]\n\t"
    "v_mul_f32_e64 v87, v87, |v87|\n\t"
    "v_cvt_pk_bf16_f32 v184, v80, v81\n\t"
    "v_mfma_f32_16x16x32_bf16 v[104:107], a[156:159], a[116:119], v[104:107]\n\t"
    "v_cvt_pk_bf16_f32 v185, v82, v83\n\t"
    "v_cvt_pk_bf16_f32 v186, v84, v85\n\t"
    "v_cvt_pk_bf16_f32 v187, v86, v87\n\t"
    "v_mfma_f32_16x16x32_bf16 v[112:115], a[128:131], a[72:75], v[112:115]\n\t"
    "v_pk_max_i16 v184, v184, 0\n\t"
    "v_pk_max_i16 v185, v185, 0\n\t"
    "v_mfma_f32_16x16x32_bf16 v[116:119], a[132:135], a[72:75], v[116:119]\n\t"
    "v_pk_max_i16 v186, v186, 0\n\t"
    "v_pk_max_i16 v187, v187, 0\n\t"
    "v_mfma_f32_16x16x32_bf16 v[116:119], a[136:139], a[88:91], v[116:119]\n\t"
    "global_store_dwordx4 v240, v[184:187], s[46:47] offset:-2048 sc1\n\t"
    "v_mul_f32_e64 v88, v88, |v88|\n\t"
    "v_mfma_f32_16x16x32_bf16 v[112:115], a[140:143], a[88:91], v[112:115]\n\t"
    "v_mul_f32_e64 v89, v89, |v89|\n\t"
    "v_mul_f32_e64 v90, v90, |v90|\n\t"
    "v_mfma_f32_16x16x32_bf16 v[112:115], a[144:147], a[104:107], v[112:115]\n\t"
    "v_mul_f32_e64 v91, v91, |v91|\n\t"
    "v_mul_f32_e64 v92, v92, |v92|\n\t"
    "v_mfma_f32_16x16x32_bf16 v[116:119], a[148:151], a[104:107], v[116:119]\n\t"
    "v_mul_f32_e64 v93, v93, |v93|\n\t"
    "v_mul_f32_e64 v94, v94, |v94|\n\t"
    "v_mfma_f32_16x16x32_bf16 v[116:119], a[152:155], a[120:123], v[116:119]\n\t"
    "v_mul_f32_e64 v95, v95, |v95|\n\t"
    "v_cvt_pk_bf16_f32 v188, v88, v89\n\t"
    "v_mfma_f32_16x16x32_bf16 v[112:115], a[156:159], a[120:123], v[112:115]\n\t"
    "v_cvt_pk_bf16_f32 v189, v90, v91\n\t"
    "v_cvt_pk_bf16_f32 v190, v92, v93\n\t"
    "v_cvt_pk_bf16_f32 v191, v94, v95\n\t"
    "v_mfma_f32_16x16x32_bf16 v[120:123], a[128:131], a[76:79], v[120:123]\n\t"
    "v_pk_max_i16 v188, v188, 0\n\t"
    "v_pk_max_i16 v189, v189, 0\n\t"
    "v_mfma_f32_16x16x32_bf16 v[124:127], a[132:135], a[76:79], v[124:127]\n\t"
    "v_pk_max_i16 v190, v190, 0\n\t"
    "v_pk_max_i16 v191, v191, 0\n\t"
    "v_mfma_f32_16x16x32_bf16 v[124:127], a[136:139], a[92:95], v[124:127]\n\t"
    "global_store_dwordx4 v240, v[188:191], s[46:47] offset:-1024 sc1\n\t"
    "v_mul_f32_e64 v96, v96, |v96|\n\t"
    "v_mfma_f32_16x16x32_bf16 v[120:123], a[140:143], a[92:95], v[120:123]\n\t"
    "v_mul_f32_e64 v97, v97, |v97|\n\t"
    "v_mul_f32_e64 v98, v98, |v98|\n\t"
    "v_mfma_f32_16x16x32_bf16 v[120:123], a[144:147], a[108:111], v[120:123]\n\t"
    "v_mul_f32_e64 v99, v99, |v99|\n\t"
    "v_mul_f32_e64 v100, v100, |v100|\n\t"
    "v_mfma_f32_16x16x32_bf16 v[124:127], a[148:151], a[108:111], v[124:127]\n\t"
    "v_mul_f32_e64 v101, v101, |v101|\n\t"
    "v_mul_f32_e64 v102, v102, |v102|\n\t"
    "v_mfma_f32_16x16x32_bf16 v[124:127], a[152:155], a[124:127], v[124:127]\n\t"
    "v_mul_f32_e64 v103, v103, |v103|\n\t"
    "v_cvt_pk_bf16_f32 v224, v96, v97\n\t"
    "v_mfma_f32_16x16x32_bf16 v[120:123], a[156:159], a[124:127], v[120:123]\n\t"
    "v_cvt_pk_bf16_f32 v225, v98, v99\n\t"
    "v_cvt_pk_bf16_f32 v226, v100, v101\n\t"
    "v_cvt_pk_bf16_f32 v227, v102, v103\n\t"
    "v_mfma_f32_32x32x16_f16 v[0:15], v[208:211], a[224:227], 0\n\t"
    "v_pk_max_i16 v224, v224, 0\n\t"
    "v_pk_max_i16 v225, v225, 0\n\t"
    "v_pk_max_i16 v226, v226, 0\n\t"
    "v_pk_max_i16 v227, v227, 0\n\t"
    "global_store_dwordx4 v240, v[224:227], s[46:47] offset:0 sc1\n\t"
    "v_accvgpr_write_b32 a48, v224\n\t"
    "v_mfma_f32_32x32x16_f16 v[16:31], v[208:211], a[240:243], 0\n\t"
    "v_accvgpr_write_b32 a49, v225\n\t"
    "v_accvgpr_write_b32 a50, v226\n\t"
    "v_accvgpr_write_b32 a51, v227\n\t"
    "v_mul_f32_e64 v104, v104, |v104|\n\t"
    "v_mul_f32_e64 v105, v105, |v105|\n\t"
    "v_mul_f32_e64 v106, v106, |v106|\n\t"
    "v_mfma_f32_32x32x16_f16 v[0:15], v[212:215], a[228:231], v[0:15]\n\t"
    "v_mul_f32_e64 v107, v107, |v107|\n\t"
    "v_mul_f32_e64 v108, v108, |v108|\n\t"
    "v_mul_f32_e64 v109, v109, |v109|\n\t"
    "v_mul_f32_e64 v110, v110, |v110|\n\t"
    "v_mul_f32_e64 v111, v111, |v111|\n\t"
    "v_cvt_pk_bf16_f32 v228, v104, v105\n\t"
    "v_mfma_f32_32x32x16_f16 v[16:31], v[212:215], a[244:247], v[16:31]\n\t"
    "v_cvt_pk_bf16_f32 v229, v106, v107\n\t"
    "v_cvt_pk_bf16_f32 v230, v108, v109\n\t"
    "v_cvt_pk_bf16_f32 v231, v110, v111\n\t"
    "v_pk_max_i16 v228, v228, 0\n\t"
    "v_pk_max_i16 v229, v229, 0\n\t"
    "v_pk_max_i16 v230, v230, 0\n\t"
    "v_mfma_f32_32x32x16_f16 v[0:15], v[216:219], a[232:235], v[0:15]\n\t"
    "v_pk_max_i16 v231, v231, 0\n\t"
    "global_store_dwordx4 v240, v[228:231], s[46:47] offset:1024 sc1\n\t"
    "v_accvgpr_write_b32 a52, v228\n\t"
    "v_accvgpr_write_b32 a53, v229\n\t"
    "v_accvgpr_write_b32 a54, v230\n\t"
    "v_accvgpr_write_b32 a55, v231\n\t"
    "v_mfma_f32_32x32x16_f16 v[16:31], v[216:219], a[248:251], v[16:31]\n\t"
    "v_mul_f32_e64 v112, v112, |v112|\n\t"
    "v_mul_f32_e64 v113, v113, |v113|\n\t"
    "v_mul_f32_e64 v114, v114, |v114|\n\t"
    "v_mul_f32_e64 v115, v115, |v115|\n\t"
    "v_mul_f32_e64 v116, v116, |v116|\n\t"
    "v_mul_f32_e64 v117, v117, |v117|\n\t"
    "v_mfma_f32_32x32x16_f16 v[0:15], v[220:223], a[236:239], v[0:15]\n\t"
    "v_mul_f32_e64 v118, v118, |v118|\n\t"
    "v_mul_f32_e64 v119, v119, |v119|\n\t"
    "v_cvt_pk_bf16_f32 v224, v112, v113\n\t"
    "v_cvt_pk_bf16_f32 v225, v114, v115\n\t"
    "v_cvt_pk_bf16_f32 v226, v116, v117\n\t"
    "v_cvt_pk_bf16_f32 v227, v118, v119\n\t"
    "v_mfma_f32_32x32x16_f16 v[16:31], v[220:223], a[252:255], v[16:31]\n\t"
    "v_pk_max_i16 v224, v224, 0\n\t"
    "v_pk_max_i16 v225, v225, 0\n\t"
    "v_pk_max_i16 v226, v226, 0\n\t"
    "v_pk_max_i16 v227, v227, 0\n\t"
    "global_store_dwordx4 v240, v[224:227], s[46:47] offset:2048 sc1\n\t"
    "v_accvgpr_write_b32 a56, v224\n\t"
    "v_accvgpr_write_b32 a57, v225\n\t"
    "v_accvgpr_write_b32 a58, v226\n\t"
    "v_accvgpr_write_b32 a59, v227\n\t"
    "v_mul_f32_e64 v120, v120, |v120|\n\t"
    "v_mul_f32_e64 v121, v121, |v121|\n\t"
    "v_mul_f32_e64 v122, v122, |v122|\n\t"
    "v_mul_f32_e64 v123, v123, |v123|\n\t"
    "v_mul_f32_e64 v124, v124, |v124|\n\t"
    "v_mul_f32_e64 v125, v125, |v125|\n\t"
    "v_mul_f32_e64 v126, v126, |v126|\n\t"
    "v_mul_f32_e64 v127, v127, |v127|\n\t"
    "v_cvt_pk_bf16_f32 v228, v120, v121\n\t"
    "v_cvt_pk_f16_f32 v80, v0, v16\n\t"
    "v_cvt_pk_f16_f32 v81, v1, v17\n\t"
    "v_pk_mul_f16 v32, v80, v160 op_sel_hi:[1,0]\n\t"
    "v_pk_mul_f16 v33, v81, v161 op_sel_hi:[1,0]\n\t"
    "v_pk_fma_f16 v32, v80, v160, v32 op_sel:[1,1,0] op_sel_hi:[0,1,1] neg_hi:[1,0,0]\n\t"
    "v_pk_fma_f16 v33, v81, v161, v33 op_sel:[1,1,0] op_sel_hi:[0,1,1] neg_hi:[1,0,0]\n\t"
    "v_cvt_pk_f16_f32 v82, v2, v18\n\t"
    "v_cvt_pk_f16_f32 v83, v3, v19\n\t"
    "v_pk_mul_f16 v34, v82, v162 op_sel_hi:[1,0]\n\t"
    "v_pk_mul_f16 v35, v83, v163 op_sel_hi:[1,0]\n\t"
    "v_pk_fma_f16 v34, v82, v162, v34 op_sel:[1,1,0] op_sel_hi:[0,1,1] neg_hi:[1,0,0]\n\t"
    "v_pk_fma_f16 v35, v83, v163, v35 op_sel:[1,1,0] op_sel_hi:[0,1,1] neg_hi:[1,0,0]\n\t"
    "v_cvt_pk_f16_f32 v80, v4, v20\n\t"
    "v_cvt_pk_f16_f32 v81, v5, v21\n\t"
    "v_mfma_f32_32x32x16_f16 v[48:63], a[224:227], v[32:35], 0\n\t"
    "v_pk_mul_f16 v36, v80, v164 op_sel_hi:[1,0]\n\t"
    "v_pk_mul_f16 v37, v81, v165 op_sel_hi:[1,0]\n\t"
    "v_pk_fma_f16 v36, v80, v164, v36 op_sel:[1,1,0] op_sel_hi:[0,1,1] neg_hi:[1,0,0]\n\t"
    "v_pk_fma_f16 v37, v81, v165, v37 op_sel:[1,1,0] op_sel_hi:[0,1,1] neg_hi:[1,0,0]\n\t"
    "v_cvt_pk_f16_f32 v82, v6, v22\n\t"
    "v_cvt_pk_f16_f32 v83, v7, v23\n\t"
    "v_mfma_f32_32x32x16_f16 v[64:79], a[240:243], v[32:35], 0\n\t"
    "v_pk_mul_f16 v38, v82, v166 op_sel_hi:[1,0]\n\t"
    "v_pk_mul_f16 v39, v83, v167 op_sel_hi:[1,0]\n\t"
    "v_pk_fma_f16 v38, v82, v166, v38 op_sel:[1,1,0] op_sel_hi:[0,1,1] neg_hi:[1,0,0]\n\t"
    "v_pk_fma_f16 v39, v83, v167, v39 op_sel:[1,1,0] op_sel_hi:[0,1,1] neg_hi:[1,0,0]\n\t"
    "v_cvt_pk_f16_f32 v80, v8, v24\n\t"
    "v_cvt_pk_f16_f32 v81, v9, v25\n\t"
    "v_mfma_f32_32x32x16_f16 v[48:63], a[228:231], v[36:39], v[48:63]\n\t"
    "v_pk_mul_f16 v40, v80, v168 op_sel_hi:[1,0]\n\t"
    "v_pk_mul_f16 v41, v81, v169 op_sel_hi:[1,0]\n\t"
    "v_pk_fma_f16 v40, v80, v168, v40 op_sel:[1,1,0] op_sel_hi:[0,1,1] neg_hi:[1,0,0]\n\t"
    "v_pk_fma_f16 v41, v81, v169, v41 op_sel:[1,1,0] op_sel_hi:[0,1,1] neg_hi:[1,0,0]\n\t"
    "v_cvt_pk_f16_f32 v82, v10, v26\n\t"
    "v_cvt_pk_f16_f32 v83, v11, v27\n\t"
    "v_mfma_f32_32x32x16_f16 v[64:79], a[244:247], v[36:39], v[64:79]\n\t"
    "v_pk_mul_f16 v42, v82, v170 op_sel_hi:[1,0]\n\t"
    "v_pk_mul_f16 v43, v83, v171 op_sel_hi:[1,0]\n\t"
    "v_pk_fma_f16 v42, v82, v170, v42 op_sel:[1,1,0] op_sel_hi:[0,1,1] neg_hi:[1,0,0]\n\t"
    "v_pk_fma_f16 v43, v83, v171, v43 op_sel:[1,1,0] op_sel_hi:[0,1,1] neg_hi:[1,0,0]\n\t"
    "v_cvt_pk_f16_f32 v80, v12, v28\n\t"
    "v_cvt_pk_f16_f32 v81, v13, v29\n\t"
    "v_mfma_f32_32x32x16_f16 v[48:63], a[232:235], v[40:43], v[48:63]\n\t"
    "v_pk_mul_f16 v44, v80, v172 op_sel_hi:[1,0]\n\t"
    "v_pk_mul_f16 v45, v81, v173 op_sel_hi:[1,0]\n\t"
    "v_pk_fma_f16 v44, v80, v172, v44 op_sel:[1,1,0] op_sel_hi:[0,1,1] neg_hi:[1,0,0]\n\t"
    "v_pk_fma_f16 v45, v81, v173, v45 op_sel:[1,1,0] op_sel_hi:[0,1,1] neg_hi:[1,0,0]\n\t"
    "v_cvt_pk_f16_f32 v82, v14, v30\n\t"
    "v_cvt_pk_f16_f32 v83, v15, v31\n\t"
    "v_mfma_f32_32x32x16_f16 v[64:79], a[248:251], v[40:43], v[64:79]\n\t"
    "v_pk_mul_f16 v46, v82, v174 op_sel_hi:[1,0]\n\t"
    "v_pk_mul_f16 v47, v83, v175 op_sel_hi:[1,0]\n\t"
    "v_pk_fma_f16 v46, v82, v174, v46 op_sel:[1,1,0] op_sel_hi:[0,1,1] neg_hi:[1,0,0]\n\t"
    "v_pk_fma_f16 v47, v83, v175, v47 op_sel:[1,1,0] op_sel_hi:[0,1,1] neg_hi:[1,0,0]\n\t"
    "s_nop 1\n\t"
    "v_mfma_f32_32x32x16_f16 v[48:63], a[236:239], v[44:47], v[48:63]\n\t"
    "v_cvt_pk_bf16_f32 v229, v122, v123\n\t"
    "v_cvt_pk_bf16_f32 v230, v124, v125\n\t"
    "v_cvt_pk_bf16_f32 v231, v126, v127\n\t"
    "v_pk_max_i16 v228, v228, 0\n\t"
    "v_pk_max_i16 v229, v229, 0\n\t"
    "v_pk_max_i16 v230, v230, 0\n\t"
    "v_mfma_f32_32x32x16_f16 v[64:79], a[252:255], v[44:47], v[64:79]\n\t"
    "v_pk_max_i16 v231, v231, 0\n\t"
    "global_store_dwordx4 v240, v[228:231], s[46:47] offset:3072 sc1\n\t"
    "v_accvgpr_write_b32 a60, v228\n\t"
    "v_accvgpr_write_b32 a61, v229\n\t"
    "v_accvgpr_write_b32 a62, v230\n\t"
    "v_accvgpr_write_b32 a63, v231\n\t"
    "Ljoin_%=:\n\t"
    "s_nop 5\n\t"
    "v_cvt_pk_f16_f32 v32, v48, v64\n\t"
    "v_cvt_pk_f16_f32 v33, v49, v65\n\t"
    "v_cvt_pk_f16_f32 v34, v50, v66\n\t"
    "v_cvt_pk_f16_f32 v35, v51, v67\n\t"
    "ds_write_b128 v241, v[32:35] offset:0\n\t"
    "v_cvt_pk_f16_f32 v36, v52, v68\n\t"
    "v_cvt_pk_f16_f32 v37, v53, v69\n\t"
    "v_cvt_pk_f16_f32 v38, v54, v70\n\t"
    "v_cvt_pk_f16_f32 v39, v55, v71\n\t"
    "ds_write_b128 v241, v[36:39] offset:1024\n\t"
    "v_cvt_pk_f16_f32 v40, v56, v72\n\t"
    "v_cvt_pk_f16_f32 v41, v57, v73\n\t"
    "v_cvt_pk_f16_f32 v42, v58, v74\n\t"
    "v_cvt_pk_f16_f32 v43, v59, v75\n\t"
    "ds_write_b128 v241, v[40:43] offset:2048\n\t"
    "v_cvt_pk_f16_f32 v44, v60, v76\n\t"
    "v_cvt_pk_f16_f32 v45, v61, v77\n\t"
    "v_cvt_pk_f16_f32 v46, v62, v78\n\t"
    "v_cvt_pk_f16_f32 v47, v63, v79\n\t"
    "ds_write_b128 v241, v[44:47] offset:3072\n\t"
    "v_pk_mul_f16 v208, v32, v128 op_sel:[1,0] op_sel_hi:[0,0]\n\t"
    "v_pk_fma_f16 v208, v32, v128, v208 op_sel:[0,1,0] op_sel_hi:[1,1,1] neg_hi:[0,1,0]\n\t"
    "v_pk_mul_f16 v209, v33, v129 op_sel:[1,0] op_sel_hi:[0,0]\n\t"
    "v_pk_fma_f16 v209, v33, v129, v209 op_sel:[0,1,0] op_sel_hi:[1,1,1] neg_hi:[0,1,0]\n\t"
    "v_pk_mul_f16 v210, v34, v130 op_sel:[1,0] op_sel_hi:[0,0]\n\t"
    "v_pk_fma_f16 v210, v34, v130, v210 op_sel:[0,1,0] op_sel_hi:[1,1,1] neg_hi:[0,1,0]\n\t"
    "v_pk_mul_f16 v211, v35, v131 op_sel:[1,0] op_sel_hi:[0,0]\n\t"
    "v_pk_fma_f16 v211, v35, v131, v211 op_sel:[0,1,0] op_sel_hi:[1,1,1] neg_hi:[0,1,0]\n\t"
    "v_pk_mul_f16 v212, v36, v132 op_sel:[1,0] op_sel_hi:[0,0]\n\t"
    "v_pk_fma_f16 v212, v36, v132, v212 op_sel:[0,1,0] op_sel_hi:[1,1,1] neg_hi:[0,1,0]\n\t"
    "v_pk_mul_f16 v213, v37, v133 op_sel:[1,0] op_sel_hi:[0,0]\n\t"
    "v_pk_fma_f16 v213, v37, v133, v213 op_sel:[0,1,0] op_sel_hi:[1,1,1] neg_hi:[0,1,0]\n\t"
    "s_and_b32 s38, s22, 1\n\t"
    "s_or_b32 s38, s38, s29\n\t"
    "s_waitcnt lgkmcnt(0)\n\t"
    "s_barrier\n\t"
    "s_cbranch_scc0 Lx7_%=\n\t"
    "ds_read_b128 v[48:51], v242 offset:0\n\t"
    "ds_read_b128 v[52:55], v242 offset:1024\n\t"
    "ds_read_b128 v[56:59], v242 offset:2048\n\t"
    "ds_read_b128 v[60:63], v242 offset:3072\n\t"
    "Ly8_%=:\n\t"
    "v_pk_mul_f16 v214, v38, v134 op_sel:[1,0] op_sel_hi:[0,0]\n\t"
    "v_pk_fma_f16 v214, v38, v134, v214 op_sel:[0,1,0] op_sel_hi:[1,1,1] neg_hi:[0,1,0]\n\t"
    "v_pk_mul_f16 v215, v39, v135 op_sel:[1,0] op_sel_hi:[0,0]\n\t"
    "v_pk_fma_f16 v215, v39, v135, v215 op_sel:[0,1,0] op_sel_hi:[1,1,1] neg_hi:[0,1,0]\n\t"
    "v_pk_mul_f16 v216, v40, v136 op_sel:[1,0] op_sel_hi:[0,0]\n\t"
    "v_pk_fma_f16 v216, v40, v136, v216 op_sel:[0,1,0] op_sel_hi:[1,1,1] neg_hi:[0,1,0]\n\t"
    "v_pk_mul_f16 v217, v41, v137 op_sel:[1,0] op_sel_hi:[0,0]\n\t"
    "v_pk_fma_f16 v217, v41, v137, v217 op_sel:[0,1,0] op_sel_hi:[1,1,1] neg_hi:[0,1,0]\n\t"
    "v_pk_mul_f16 v218, v42, v138 op_sel:[1,0] op_sel_hi:[0,0]\n\t"
    "v_pk_fma_f16 v218, v42, v138, v218 op_sel:[0,1,0] op_sel_hi:[1,1,1] neg_hi:[0,1,0]\n\t"
    "v_pk_mul_f16 v219, v43, v139 op_sel:[1,0] op_sel_hi:[0,0]\n\t"
    "v_pk_fma_f16 v219, v43, v139, v219 op_sel:[0,1,0] op_sel_hi:[1,1,1] neg_hi:[0,1,0]\n\t"
    "v_pk_mul_f16 v220, v44, v140 op_sel:[1,0] op_sel_hi:[0,0]\n\t"
    "v_pk_fma_f16 v220, v44, v140, v220 op_sel:[0,1,0] op_sel_hi:[1,1,1] neg_hi:[0,1,0]\n\t"
    "v_pk_mul_f16 v221, v45, v141 op_sel:[1,0] op_sel_hi:[0,0]\n\t"
    "v_pk_fma_f16 v221, v45, v141, v221 op_sel:[0,1,0] op_sel_hi:[1,1,1] neg_hi:[0,1,0]\n\t"
    "v_pk_mul_f16 v222, v46, v142 op_sel:[1,0] op_sel_hi:[0,0]\n\t"
    "v_pk_fma_f16 v222, v46, v142, v222 op_sel:[0,1,0] op_sel_hi:[1,1,1] neg_hi:[0,1,0]\n\t"
    "v_pk_mul_f16 v223, v47, v143 op_sel:[1,0] op_sel_hi:[0,0]\n\t"
    "v_pk_fma_f16 v223, v47, v143, v223 op_sel:[0,1,0] op_sel_hi:[1,1,1] neg_hi:[0,1,0]\n\t"
    "s_waitcnt lgkmcnt(3)\n\t"
    "v_pk_fma_f16 v208, v48, v144, v208 op_sel:[1,0,0] op_sel_hi:[0,0,1]\n\t"
    "v_pk_fma_f16 v209, v49, v145, v209 op_sel:[1,0,0] op_sel_hi:[0,0,1]\n\t"
    "v_pk_fma_f16 v210, v50, v146, v210 op_sel:[1,0,0] op_sel_hi:[0,0,1]\n\t"
    "v_pk_fma_f16 v211, v51, v147, v211 op_sel:[1,0,0] op_sel_hi:[0,0,1]\n\t"
    "v_pk_fma_f16 v208, v48, v144, v208 op_sel:[0,1,0] op_sel_hi:[1,1,1] neg_hi:[0,1,0]\n\t"
    "v_pk_fma_f16 v209, v49, v145, v209 op_sel:[0,1,0] op_sel_hi:[1,1,1] neg_hi:[0,1,0]\n\t"
    "v_pk_fma_f16 v210, v50, v146, v210 op_sel:[0,1,0] op_sel_hi:[1,1,1] neg_hi:[0,1,0]\n\t"
    "v_pk_fma_f16 v211, v51, v147, v211 op_sel:[0,1,0] op_sel_hi:[1,1,1] neg_hi:[0,1,0]\n\t"
    "s_nop 1\n\t"
    "v_mfma_f32_32x32x16_f16 v[0:15], v[208:211], a[224:227], 0\n\t"
    "ds_read_b64_tr_b16 a[192:193], v232 offset:32896\n\t"
    "s_waitcnt lgkmcnt(3)\n\t"
    "v_pk_fma_f16 v212, v52, v148, v212 op_sel:[1,0,0] op_sel_hi:[0,0,1]\n\t"
    "v_pk_fma_f16 v213, v53, v149, v213 op_sel:[1,0,0] op_sel_hi:[0,0,1]\n\t"
    "v_pk_fma_f16 v214, v54, v150, v214 op_sel:[1,0,0] op_sel_hi:[0,0,1]\n\t"
    "v_pk_fma_f16 v215, v55, v151, v215 op_sel:[1,0,0] op_sel_hi:[0,0,1]\n\t"
    "v_pk_fma_f16 v212, v52, v148, v212 op_sel:[0,1,0] op_sel_hi:[1,1,1] neg_hi:[0,1,0]\n\t"
    "v_mfma_f32_32x32x16_f16 v[16:31], v[208:211], a[240:243], 0\n\t"
    "ds_read_b64_tr_b16 a[194:195], v232 offset:33920\n\t"
    "v_pk_fma_f16 v213, v53, v149, v213 op_sel:[0,1,0] op_sel_hi:[1,1,1] neg_hi:[0,1,0]\n\t"
    "v_pk_fma_f16 v214, v54, v150, v214 op_sel:[0,1,0] op_sel_hi:[1,1,1] neg_hi:[0,1,0]\n\t"
    "v_pk_fma_f16 v215, v55, v151, v215 op_sel:[0,1,0] op_sel_hi:[1,1,1] neg_hi:[0,1,0]\n\t"
    "s_nop 1\n\t"
    "v_mfma_f32_32x32x16_f16 v[0:15], v[212:215], a[228:231], v[0:15]\n\t"
    "ds_read_b64_tr_b16 a[196:197], v232 offset:32904\n\t"
    "s_waitcnt lgkmcnt(4)\n\t"
    "v_pk_fma_f16 v216, v56, v152, v216 op_sel:[1,0,0] op_sel_hi:[0,0,1]\n\t"
    "v_pk_fma_f16 v217, v57, v153, v217 op_sel:[1,0,0] op_sel_hi:[0,0,1]\n\t"
    "v_pk_fma_f16 v218, v58, v154, v218 op_sel:[1,0,0] op_sel_hi:[0,0,1]\n\t"
    "v_pk_fma_f16 v219, v59, v155, v219 op_sel:[1,0,0] op_sel_hi:[0,0,1]\n\t"
    "v_pk_fma_f16 v216, v56, v152, v216 op_sel:[0,1,0] op_sel_hi:[1,1,1] neg_hi:[0,1,0]\n\t"
    "v_mfma_f32_32x32x16_f16 v[16:31], v[212:215], a[244:247], v[16:31]\n\t"
    "ds_read_b64_tr_b16 a[198:199], v232 offset:33928\n\t"
    "v_pk_fma_f16 v217, v57, v153, v217 op_sel:[0,1,0] op_sel_hi:[1,1,1] neg_hi:[0,1,0]\n\t"
    "v_pk_fma_f16 v218, v58, v154, v218 op_sel:[0,1,0] op_sel_hi:[1,1,1] neg_hi:[0,1,0]\n\t"
    "v_pk_fma_f16 v219, v59, v155, v219 op_sel:[0,1,0] op_sel_hi:[1,1,1] neg_hi:[0,1,0]\n\t"
    "s_nop 1\n\t"
    "v_mfma_f32_32x32x16_f16 v[0:15], v[216:219], a[232:235], v[0:15]\n\t"
    "ds_read_b64_tr_b16 a[200:201], v233 offset:32896\n\t"
    "s_waitcnt lgkmcnt(5)\n\t"
    "v_pk_fma_f16 v220, v60, v156, v220 op_sel:[1,0,0] op_sel_hi:[0,0,1]\n\t"
    "v_pk_fma_f16 v221, v61, v157, v221 op_sel:[1,0,0] op_sel_hi:[0,0,1]\n\t"
    "v_pk_fma_f16 v222, v62, v158, v222 op_sel:[1,0,0] op_sel_hi:[0,0,1]\n\t"
    "v_pk_fma_f16 v223, v63, v159, v223 op_sel:[1,0,0] op_sel_hi:[0,0,1]\n\t"
    "v_pk_fma_f16 v220, v60, v156, v220 op_sel:[0,1,0] op_sel_hi:[1,1,1] neg_hi:[0,1,0]\n\t"
    "v_mfma_f32_32x32x16_f16 v[16:31], v[216:219], a[248:251], v[16:31]\n\t"
    "ds_read_b64_tr_b16 a[202:203], v233 offset:33920\n\t"
    "v_pk_fma_f16 v221, v61, v157, v221 op_sel:[0,1,0] op_sel_hi:[1,1,1] neg_hi:[0,1,0]\n\t"
    "v_pk_fma_f16 v222, v62, v158, v222 op_sel:[0,1,0] op_sel_hi:[1,1,1] neg_hi:[0,1,0]\n\t"
    "v_pk_fma_f16 v223, v63, v159, v223 op_sel:[0,1,0] op_sel_hi:[1,1,1] neg_hi:[0,1,0]\n\t"
    "s_nop 1\n\t"
    "v_mfma_f32_32x32x16_f16 v[0:15], v[220:223], a[236:239], v[0:15]\n\t"
    "ds_read_b64_tr_b16 a[204:205], v233 offset:32904\n\t"
    "v_mfma_f32_32x32x16_f16 v[16:31], v[220:223], a[252:255], v[16:31]\n\t"
    "ds_read_b64_tr_b16 a[206:207], v233 offset:33928\n\t"
    "ds_read_b64_tr_b16 a[208:209], v234 offset:32896\n\t"
    "ds_read_b64_tr_b16 a[210:211], v234 offset:33920\n\t"
    "ds_read_b64_tr_b16 a[212:213], v234 offset:32904\n\t"
    "ds_read_b64_tr_b16 a[214:215], v234 offset:33928\n\t"
    "ds_read_b64_tr_b16 a[216:217], v235 offset:32896\n\t"
    "ds_read_b64_tr_b16 a[218:219], v235 offset:33920\n\t"
    "ds_read_b64_tr_b16 a[220:221], v235 offset:32904\n\t"
    "v_mov_b64 v[250:251], 0\n\t"
    "s_mov_b64 exec, 0x7f\n\t"
    "global_atomic_or_x2 v[250:251], v249, v[250:251], s[20:21] sc0 sc1\n\t"
    "s_mov_b64 exec, -1\n\t"
    "v_cvt_pk_f16_f32 v80, v0, v16\n\t"
    "v_cvt_pk_f16_f32 v81, v1, v17\n\t"
    "v_pk_mul_f16 v32, v80, v160 op_sel_hi:[1,0]\n\t"
    "v_pk_mul_f16 v33, v81, v161 op_sel_hi:[1,0]\n\t"
    "v_pk_fma_f16 v32, v80, v160, v32 op_sel:[1,1,0] op_sel_hi:[0,1,1] neg_hi:[1,0,0]\n\t"
    "v_pk_fma_f16 v33, v81, v161, v33 op_sel:[1,1,0] op_sel_hi:[0,1,1] neg_hi:[1,0,0]\n\t"
    "v_cvt_pk_f16_f32 v82, v2, v18\n\t"
    "v_cvt_pk_f16_f32 v83, v3, v19\n\t"
    "v_pk_mul_f16 v34, v82, v162 op_sel_hi:[1,0]\n\t"
    "Lpad7_%=:\n\t"
    "s_cbranch_execz Lpad11_%=\n\t"
    "v_pk_mul_f16 v35, v83, v163 op_sel_hi:[1,0]\n\t"
    "v_pk_fma_f16 v34, v82, v162, v34 op_sel:[1,1,0] op_sel_hi:[0,1,1] neg_hi:[1,0,0]\n\t"
    "v_pk_fma_f16 v35, v83, v163, v35 op_sel:[1,1,0] op_sel_hi:[0,1,1] neg_hi:[1,0,0]\n\t"
    "v_cvt_pk_f16_f32 v80, v4, v20\n\t"
    "v_cvt_pk_f16_f32 v81, v5, v21\n\t"
    "v_mfma_f32_32x32x16_f16 v[128:143], v[32:35], a[224:227], 0\n\t"
    "v_pk_mul_f16 v36, v80, v164 op_sel_hi:[1,0]\n\t"
    "v_pk_mul_f16 v37, v81, v165 op_sel_hi:[1,0]\n\t"
    "v_pk_fma_f16 v36, v80, v164, v36 op_sel:[1,1,0] op_sel_hi:[0,1,1] neg_hi:[1,0,0]\n\t"
    "v_pk_fma_f16 v37, v81, v165, v37 op_sel:[1,1,0] op_sel_hi:[0,1,1] neg_hi:[1,0,0]\n\t"
    "v_cvt_pk_f16_f32 v82, v6, v22\n\t"
    "v_cvt_pk_f16_f32 v83, v7, v23\n\t"
    "v_mfma_f32_32x32x16_f16 v[144:159], v[32:35], a[240:243], 0\n\t"
    "v_pk_mul_f16 v38, v82, v166 op_sel_hi:[1,0]\n\t"
    "v_pk_mul_f16 v39, v83, v167 op_sel_hi:[1,0]\n\t"
    "v_pk_fma_f16 v38, v82, v166, v38 op_sel:[1,1,0] op_sel_hi:[0,1,1] neg_hi:[1,0,0]\n\t"
    "v_pk_fma_f16 v39, v83, v167, v39 op_sel:[1,1,0] op_sel_hi:[0,1,1] neg_hi:[1,0,0]\n\t"
    "v_cvt_pk_f16_f32 v80, v8, v24\n\t"
    "v_cvt_pk_f16_f32 v81, v9, v25\n\t"
    "v_mfma_f32_32x32x16_f16 v[128:143], v[36:39], a[228:231], v[128:143]\n\t"
    "v_pk_mul_f16 v40, v80, v168 op_sel_hi:[1,0]\n\t"
    "v_pk_mul_f16 v41, v81, v169 op_sel_hi:[1,0]\n\t"
    "v_pk_fma_f16 v40, v80, v168, v40 op_sel:[1,1,0] op_sel_hi:[0,1,1] neg_hi:[1,0,0]\n\t"
    "v_pk_fma_f16 v41, v81, v169, v41 op_sel:[1,1,0] op_sel_hi:[0,1,1] neg_hi:[1,0,0]\n\t"
    "v_cvt_pk_f16_f32 v82, v10, v26\n\t"
    "v_cvt_pk_f16_f32 v83, v11, v27\n\t"
    "v_mfma_f32_32x32x16_f16 v[144:159], v[36:39], a[244:247], v[144:159]\n\t"
    "v_pk_mul_f16 v42, v82, v170 op_sel_hi:[1,0]\n\t"
    "v_pk_mul_f16 v43, v83, v171 op_sel_hi:[1,0]\n\t"
    "v_pk_fma_f16 v42, v82, v170, v42 op_sel:[1,1,0] op_sel_hi:[0,1,1] neg_hi:[1,0,0]\n\t"
    "v_pk_fma_f16 v43, v83, v171, v43 op_sel:[1,1,0] op_sel_hi:[0,1,1] neg_hi:[1,0,0]\n\t"
    "v_cvt_pk_f16_f32 v80, v12, v28\n\t"
    "v_cvt_pk_f16_f32 v81, v13, v29\n\t"
    "v_mfma_f32_32x32x16_f16 v[128:143], v[40:43], a[232:235], v[128:143]\n\t"
    "v_pk_mul_f16 v44, v80, v172 op_sel_hi:[1,0]\n\t"
    "v_pk_mul_f16 v45, v81, v173 op_sel_hi:[1,0]\n\t"
    "v_pk_fma_f16 v44, v80, v172, v44 op_sel:[1,1,0] op_sel_hi:[0,1,1] neg_hi:[1,0,0]\n\t"
    "v_pk_fma_f16 v45, v81, v173, v45 op_sel:[1,1,0] op_sel_hi:[0,1,1] neg_hi:[1,0,0]\n\t"
    "v_cvt_pk_f16_f32 v82, v14, v30\n\t"
    "v_cvt_pk_f16_f32 v83, v15, v31\n\t"
    "v_mfma_f32_32x32x16_f16 v[144:159], v[40:43], a[248:251], v[144:159]\n\t"
    "v_pk_mul_f16 v46, v82, v174 op_sel_hi:[1,0]\n\t"
    "v_pk_mul_f16 v47, v83, v175 op_sel_hi:[1,0]\n\t"
    "v_pk_fma_f16 v46, v82, v174, v46 op_sel:[1,1,0] op_sel_hi:[0,1,1] neg_hi:[1,0,0]\n\t"
    "v_pk_fma_f16 v47, v83, v175, v47 op_sel:[1,1,0] op_sel_hi:[0,1,1] neg_hi:[1,0,0]\n\t"
    "s_nop 1\n\t"
    "v_mfma_f32_32x32x16_f16 v[128:143], v[44:47], a[236:239], v[128:143]\n\t"
    "v_mfma_f32_32x32x16_f16 v[144:159], v[44:47], a[252:255], v[144:159]\n\t"
    "s_cmp_eq_u32 s26, 7\n\t"
    "s_cbranch_scc1 Ld9_%=\n\t"
    "Le10_%=:\n\t"
    "s_waitcnt lgkmcnt(13)\n\t"
    "v_mfma_f32_16x16x32_bf16 v[0:3], a[192:195], a[0:3], 0\n\t"
    "s_barrier\n\t"
    "v_mfma_f32_16x16x32_bf16 v[8:11], a[192:195], a[4:7], 0\n\t"
    "ds_read_b64_tr_b16 a[222:223], v235 offset:33928\n\t"
    "v_and_b32 v226, 31, v245\n\t"
    "v_mfma_f32_16x16x32_bf16 v[16:19], a[192:195], a[8:11], 0\n\t"
    "v_lshrrev_b32 v227, 5, v245\n\t"
    "v_mfma_f32_16x16x32_bf16 v[24:27], a[192:195], a[12:15], 0\n\t"
    "s_waitcnt lgkmcnt(12)\n\t"
    "ds_read_b64_tr_b16 a[192:193], v232 offset:41120\n\t"
    "v_mfma_f32_16x16x32_bf16 v[4:7], a[196:199], a[0:3], 0\n\t"
    "ds_read_b64_tr_b16 a[194:195], v232 offset:42144\n\t"
    "v_mul_u32_u24 v224, 528, v226\n\t"
    "v_mfma_f32_16x16x32_bf16 v[12:15], a[196:199], a[4:7], 0\n\t"
    "v_lshl_add_u32 v224, v227, 6, v224\n\t"
    "v_mfma_f32_16x16x32_bf16 v[20:23], a[196:199], a[8:11], 0\n\t"
    "s_lshl_b32 s38, s22, 4\n\t"
    "v_mfma_f32_16x16x32_bf16 v[28:31], a[196:199], a[12:15], 0\n\t"
    "s_waitcnt lgkmcnt(12)\n\t"
    "ds_read_b64_tr_b16 a[196:197], v232 offset:41128\n\t"
    "v_mfma_f32_16x16x32_bf16 v[32:35], a[200:203], a[0:3], 0\n\t"
    "ds_read_b64_tr_b16 a[198:199], v232 offset:42152\n\t"
    "v_readlane_b32 s24, v246, 36\n\t"
    "v_mfma_f32_16x16x32_bf16 v[40:43], a[200:203], a[4:7], 0\n\t"
    "s_add_u32 m0, s31, 65792\n\t"
    "s_add_u32 s38, s38, 115200\n\t"
    "v_mfma_f32_16x16x32_bf16 v[48:51], a[200:203], a[8:11], 0\n\t"
    "s_lshl_b32 s35, s24, 4\n\t"
    "buffer_load_dwordx4 v243, s[96:99], s35 offen sc1 lds\n\t"
    "v_mfma_f32_16x16x32_bf16 v[56:59], a[200:203], a[12:15], 0\n\t"
    "s_waitcnt lgkmcnt(12)\n\t"
    "ds_read_b64_tr_b16 a[200:201], v233 offset:41120\n\t"
    "v_mfma_f32_16x16x32_bf16 v[36:39], a[204:207], a[0:3], 0\n\t"
    "ds_read_b64_tr_b16 a[202:203], v233 offset:42144\n\t"
    "v_add_u32 v224, s38, v224\n\t"
    "v_mfma_f32_16x16x32_bf16 v[44:47], a[204:207], a[4:7], 0\n\t"
    "v_cvt_pk_f16_f32 v208, v128, v144\n\t"
    "v_cvt_pk_f16_f32 v209, v129, v145\n\t"
    "v_mfma_f32_16x16x32_bf16 v[52:55], a[204:207], a[8:11], 0\n\t"
    "v_cvt_pk_f16_f32 v210, v130, v146\n\t"
    "v_cvt_pk_f16_f32 v211, v131, v147\n\t"
    "v_mfma_f32_16x16x32_bf16 v[60:63], a[204:207], a[12:15], 0\n\t"
    "s_waitcnt lgkmcnt(12)\n\t"
    "ds_read_b64_tr_b16 a[204:205], v233 offset:41128\n\t"
    "v_mfma_f32_16x16x32_bf16 v[64:67], a[208:211], a[0:3], 0\n\t"
    "ds_read_b64_tr_b16 a[206:207], v233 offset:42152\n\t"
    "v_mfma_f32_16x16x32_bf16 v[72:75], a[208:211], a[4:7], 0\n\t"
    "ds_write_b128 v224, v[208:211] offset:0\n\t"
    "v_mfma_f32_16x16x32_bf16 v[80:83], a[208:211], a[8:11], 0\n\t"
    "v_cvt_pk_f16_f32 v212, v132, v148\n\t"
    "v_cvt_pk_f16_f32 v213, v133, v149\n\t"
    "v_mfma_f32_16x16x32_bf16 v[88:91], a[208:211], a[12:15], 0\n\t"
    "s_waitcnt lgkmcnt(13)\n\t"
    "ds_read_b64_tr_b16 a[208:209], v234 offset:41120\n\t"
    "v_mfma_f32_16x16x32_bf16 v[68:71], a[212:215], a[0:3], 0\n\t"
    "ds_read_b64_tr_b16 a[210:211], v234 offset:42144\n\t"
    "v_cvt_pk_f16_f32 v214, v134, v150\n\t"
    "v_mfma_f32_16x16x32_bf16 v[76:79], a[212:215], a[4:7], 0\n\t"
    "v_cvt_pk_f16_f32 v215, v135, v151\n\t"
    "v_mfma_f32_16x16x32_bf16 v[84:87], a[212:215], a[8:11], 0\n\t"
    "ds_write_b128 v224, v[212:215] offset:128\n\t"
    "v_mfma_f32_16x16x32_bf16 v[92:95], a[212:215], a[12:15], 0\n\t"
    "s_waitcnt lgkmcnt(14)\n\t"
    "ds_read_b64_tr_b16 a[212:213], v234 offset:41128\n\t"
    "v_mfma_f32_16x16x32_bf16 v[96:99], a[216:219], a[0:3], 0\n\t"
    "ds_read_b64_tr_b16 a[214:215], v234 offset:42152\n\t"
    "v_mfma_f32_16x16x32_bf16 v[104:107], a[216:219], a[4:7], 0\n\t"
    "s_waitcnt vmcnt(1)\n\t"
    "v_cmp_ne_u64 vcc, s[74:75], v[250:251]\n\t"
    "v_mfma_f32_16x16x32_bf16 v[112:115], a[216:219], a[8:11], 0\n\t"
    "buffer_load_dwordx4 v244, s[96:99], s35 offen offset:1024 sc1 lds\n\t"
    "v_mfma_f32_16x16x32_bf16 v[120:123], a[216:219], a[12:15], 0\n\t"
    "s_waitcnt lgkmcnt(14)\n\t"
    "ds_read_b64_tr_b16 a[216:217], v235 offset:41120\n\t"
    "v_mfma_f32_16x16x32_bf16 v[100:103], a[220:223], a[0:3], 0\n\t"
    "ds_read_b64_tr_b16 a[218:219], v235 offset:42144\n\t"
    "v_mfma_f32_16x16x32_bf16 v[108:111], a[220:223], a[4:7], 0\n\t"
    "s_and_b64 vcc, vcc, 0x7f\n\t"
    "s_cbranch_scc1 Ls15_%=\n\t"
    "Lb16_%=:\n\t"
    "v_mfma_f32_16x16x32_bf16 v[116:119], a[220:223], a[8:11], 0\n\t"
    "global_store_dwordx2 v248, v[254:255], s[4:5] offset:64 sc1\n\t"
    "v_mfma_f32_16x16x32_bf16 v[124:127], a[220:223], a[12:15], 0\n\t"
    "s_waitcnt lgkmcnt(14)\n\t"
    "ds_read_b64_tr_b16 a[220:221], v235 offset:41128\n\t"
    "v_mfma_f32_16x16x32_bf16 v[0:3], a[192:195], a[16:19], v[0:3]\n\t"
    "ds_read_b64_tr_b16 a[222:223], v235 offset:42152\n\t"
    "v_readlane_b32 s24, v246, 37\n\t"
    "v_mfma_f32_16x16x32_bf16 v[8:11], a[192:195], a[20:23], v[8:11]\n\t"
    "s_add_u32 m0, s31, 74016\n\t"
    "v_mul_u32_u24 v225, 528, v227\n\t"
    "v_mfma_f32_16x16x32_bf16 v[16:19], a[192:195], a[24:27], v[16:19]\n\t"
    "s_lshl_b32 s35, s24, 4\n\t"
    "buffer_load_dwordx4 v243, s[96:99], s35 offen sc1 lds\n\t"
    "v_mfma_f32_16x16x32_bf16 v[24:27], a[192:195], a[28:31], v[24:27]\n\t"
    "s_waitcnt lgkmcnt(14)\n\t"
    "ds_read_b64_tr_b16 a[192:193], v232 offset:49344\n\t"
    "v_mfma_f32_16x16x32_bf16 v[28:31], a[196:199], a[28:31], v[28:31]\n\t"
    "ds_read_b64_tr_b16 a[194:195], v232 offset:50368\n\t"
    "v_lshl_add_u32 v225, v226, 4, v225\n\t"
    "v_mfma_f32_16x16x32_bf16 v[20:23], a[196:199], a[24:27], v[20:23]\n\t"
    "s_mul_i32 s38, s22, 4224\n\t"
    "s_add_u32 s38, s38, 115200\n\t"
    "v_mfma_f32_16x16x32_bf16 v[12:15], a[196:199], a[20:23], v[12:15]\n\t"
    "buffer_load_dwordx4 v244, s[96:99], s35 offen offset:1024 sc1 lds\n\t"
    "v_add_u32 v225, s38, v225\n\t"
    "v_mfma_f32_16x16x32_bf16 v[4:7], a[196:199], a[16:19], v[4:7]\n\t"
    "s_waitcnt lgkmcnt(14)\n\t"
    "ds_read_b64_tr_b16 a[196:197], v232 offset:49352\n\t"
    "v_mfma_f32_16x16x32_bf16 v[32:35], a[200:203], a[16:19], v[32:35]\n\t"
    "ds_read_b64_tr_b16 a[198:199], v232 offset:50376\n\t"
    "v_readlane_b32 s24, v246, 38\n\t"
    "v_mfma_f32_16x16x32_bf16 v[40:43], a[200:203], a[20:23], v[40:43]\n\t"
    "s_add_u32 m0, s31, 82240\n\t"
    "v_cvt_pk_f16_f32 v216, v136, v152\n\t"
    "v_mfma_f32_16x16x32_bf16 v[48:51], a[200:203], a[24:27], v[48:51]\n\t"
    "s_lshl_b32 s35, s24, 4\n\t"
    "buffer_load_dwordx4 v243, s[96:99], s35 offen sc1 lds\n\t"
    "v_mfma_f32_16x16x32_bf16 v[56:59], a[200:203], a[28:31], v[56:59]\n\t"
    "s_waitcnt lgkmcnt(14)\n\t"
    "ds_read_b64_tr_b16 a[200:201], v233 offset:49344\n\t"
    "v_mfma_f32_16x16x32_bf16 v[60:63], a[204:207], a[28:31], v[60:63]\n\t"
    "ds_read_b64_tr_b16 a[202:203], v233 offset:50368\n\t"
    "v_cvt_pk_f16_f32 v217, v137, v153\n\t"
    "v_mfma_f32_16x16x32_bf16 v[52:55], a[204:207], a[24:27], v[52:55]\n\t"
    "v_cvt_pk_f16_f32 v218, v138, v154\n\t"
    "v_cvt_pk_f16_f32 v219, v139, v155\n\t"
    "v_mfma_f32_16x16x32_bf16 v[44:47], a[204:207], a[20:23], v[44:47]\n\t"
    "ds_write_b128 v224, v[216:219] offset:256\n\t"
    "v_mfma_f32_16x16x32_bf16 v[36:39], a[204:207], a[16:19], v[36:39]\n\t"
    "s_waitcnt lgkmcnt(14)\n\t"
    "ds_read_b64_tr_b16 a[204:205], v233 offset:49352\n\t"
    "v_mfma_f32_16x16x32_bf16 v[64:67], a[208:211], a[16:19], v[64:67]\n\t"
    "ds_read_b64_tr_b16 a[206:207], v233 offset:50376\n\t"
    "v_cvt_pk_f16_f32 v220, v140, v156\n\t"
    "v_mfma_f32_16x16x32_bf16 v[72:75], a[208:211], a[20:23], v[72:75]\n\t"
    "v_cvt_pk_f16_f32 v221, v141, v157\n\t"
    "v_cvt_pk_f16_f32 v222, v142, v158\n\t"
    "v_mfma_f32_16x16x32_bf16 v[80:83], a[208:211], a[24:27], v[80:83]\n\t"
    "buffer_load_dwordx4 v244, s[96:99], s35 offen offset:1024 sc1 lds\n\t"
    "v_cvt_pk_f16_f32 v223, v143, v159\n\t"
    "v_mfma_f32_16x16x32_bf16 v[88:91], a[208:211], a[28:31], v[88:91]\n\t"
    "s_waitcnt lgkmcnt(13)\n\t"
    "ds_read_b64_tr_b16 a[208:209], v234 offset:49344\n\t"
    "v_mfma_f32_16x16x32_bf16 v[92:95], a[212:215], a[28:31], v[92:95]\n\t"
    "ds_read_b64_tr_b16 a[210:211], v234 offset:50368\n\t"
    "v_readlane_b32 s24, v246, 4\n\t"
    "v_mfma_f32_16x16x32_bf16 v[84:87], a[212:215], a[24:27], v[84:87]\n\t"
    "ds_write_b128 v224, v[220:223] offset:384\n\t"
    "v_mfma_f32_16x16x32_bf16 v[76:79], a[212:215], a[20:23], v[76:79]\n\t"
    "s_mov_b32 s34, s24\n\t"
    "buffer_load_dwordx4 a[64:67], v240, s[64:67], s34 offen offset:0 sc1\n\t"
    "v_mfma_f32_16x16x32_bf16 v[68:71], a[212:215], a[16:19], v[68:71]\n\t"
    "s_waitcnt lgkmcnt(14)\n\t"
    "ds_read_b64_tr_b16 a[212:213], v234 offset:49352\n\t"
    "v_mfma_f32_16x16x32_bf16 v[96:99], a[216:219], a[16:19], v[96:99]\n\t"
    "ds_read_b64_tr_b16 a[214:215], v234 offset:50376\n\t"
    "v_mfma_f32_16x16x32_bf16 v[104:107], a[216:219], a[20:23], v[104:107]\n\t"
    "v_mfma_f32_16x16x32_bf16 v[112:115], a[216:219], a[24:27], v[112:115]\n\t"
    "buffer_load_dwordx4 a[68:71], v240, s[64:67], s34 offen offset:1024 sc1\n\t"
    "v_mfma_f32_16x16x32_bf16 v[120:123], a[216:219], a[28:31], v[120:123]\n\t"
    "s_waitcnt lgkmcnt(14)\n\t"
    "ds_read_b64_tr_b16 a[216:217], v235 offset:49344\n\t"
    "v_mfma_f32_16x16x32_bf16 v[124:127], a[220:223], a[28:31], v[124:127]\n\t"
    "ds_read_b64_tr_b16 a[218:219], v235 offset:50368\n\t"
    "v_mfma_f32_16x16x32_bf16 v[116:119], a[220:223], a[24:27], v[116:119]\n\t"
    "v_mfma_f32_16x16x32_bf16 v[108:111], a[220:223], a[20:23], v[108:111]\n\t"
    "buffer_load_dwordx4 a[72:75], v240, s[64:67], s34 offen offset:2048 sc1\n\t"
    "v_mfma_f32_16x16x32_bf16 v[100:103], a[220:223], a[16:19], v[100:103]\n\t"
    "s_waitcnt vmcnt(8) lgkmcnt(4)\n\t"
    "ds_read_b64_tr_b16 a[220:221], v235 offset:49352\n\t"
    "v_mfma_f32_16x16x32_bf16 v[0:3], a[192:195], v[176:179], v[0:3]\n\t"
    "s_barrier\n\t"
    "v_mfma_f32_16x16x32_bf16 v[8:11], a[192:195], v[180:183], v[8:11]\n\t"
    "ds_read_b64_tr_b16 a[222:223], v235 offset:50376\n\t"
    "v_mfma_f32_16x16x32_bf16 v[16:19], a[192:195], v[184:187], v[16:19]\n\t"
    "buffer_load_dwordx4 a[76:79], v240, s[64:67], s34 offen offset:3072 sc1\n\t"
    "v_mfma_f32_16x16x32_bf16 v[24:27], a[192:195], v[188:191], v[24:27]\n\t"
    "ds_read_b64_tr_b16 a[192:193], v236 offset:0\n\t"
    "v_mfma_f32_16x16x32_bf16 v[28:31], a[196:199], v[188:191], v[28:31]\n\t"
    "ds_read_b64_tr_b16 a[194:195], v236 offset:1024\n\t"
    "v_mfma_f32_16x16x32_bf16 v[20:23], a[196:199], v[184:187], v[20:23]\n\t"
    "ds_read_b128 v[208:211], v225 offset:0\n\t"
    "v_mfma_f32_16x16x32_bf16 v[12:15], a[196:199], v[180:183], v[12:15]\n\t"
    "ds_read_b128 v[212:215], v225 offset:1056\n\t"
    "v_mfma_f32_16x16x32_bf16 v[4:7], a[196:199], v[176:179], v[4:7]\n\t"
    "ds_read_b64_tr_b16 a[196:197], v236 offset:8\n\t"
    "v_mfma_f32_16x16x32_bf16 v[32:35], a[200:203], v[176:179], v[32:35]\n\t"
    "ds_read_b64_tr_b16 a[198:199], v236 offset:1032\n\t"
    "v_readlane_b32 s24, v246, 5\n\t"
    "v_mfma_f32_16x16x32_bf16 v[40:43], a[200:203], v[180:183], v[40:43]\n\t"
    "v_mfma_f32_16x16x32_bf16 v[48:51], a[200:203], v[184:187], v[48:51]\n\t"
    "s_mov_b32 s34, s24\n\t"
    "buffer_load_dwordx4 a[80:83], v240, s[64:67], s34 offen offset:0 sc1\n\t"
    "v_mfma_f32_16x16x32_bf16 v[56:59], a[200:203], v[188:191], v[56:59]\n\t"
    "ds_read_b64_tr_b16 a[200:201], v237 offset:0\n\t"
    "v_mfma_f32_16x16x32_bf16 v[60:63], a[204:207], v[188:191], v[60:63]\n\t"
    "ds_read_b64_tr_b16 a[202:203], v237 offset:1024\n\t"
    "v_mfma_f32_16x16x32_bf16 v[52:55], a[204:207], v[184:187], v[52:55]\n\t"
    "ds_read_b128 v[216:219], v225 offset:2112\n\t"
    "v_mfma_f32_16x16x32_bf16 v[44:47], a[204:207], v[180:183], v[44:47]\n\t"
    "v_mfma_f32_16x16x32_bf16 v[36:39], a[204:207], v[176:179], v[36:39]\n\t"
    "ds_read_b64_tr_b16 a[204:205], v237 offset:8\n\t"
    "v_mfma_f32_16x16x32_bf16 v[64:67], a[208:211], v[176:179], v[64:67]\n\t"
    "ds_read_b64_tr_b16 a[206:207], v237 offset:1032\n\t"
    "v_mfma_f32_16x16x32_bf16 v[72:75], a[208:211], v[180:183], v[72:75]\n\t"
    "ds_read_b128 v[220:223], v225 offset:3168\n\t"
    "v_mfma_f32_16x16x32_bf16 v[80:83], a[208:211], v[184:187], v[80:83]\n\t"
    "buffer_load_dwordx4 a[84:87], v240, s[64:67], s34 offen offset:1024 sc1\n\t"
    "v_mfma_f32_16x16x32_bf16 v[88:91], a[208:211], v[188:191], v[88:91]\n\t"
    "s_waitcnt lgkmcnt(15)\n\t"
    "ds_read_b64_tr_b16 a[208:209], v238 offset:0\n\t"
    "v_mfma_f32_16x16x32_bf16 v[92:95], a[212:215], v[188:191], v[92:95]\n\t"
    "ds_read_b64_tr_b16 a[210:211], v238 offset:1024\n\t"
    "v_mfma_f32_16x16x32_bf16 v[84:87], a[212:215], v[184:187], v[84:87]\n\t"
    "s_waitcnt lgkmcnt(11)\n\t"
    "global_store_dwordx4 v240, v[208:211], s[50:51] offset:0 sc1\n\t"
    "v_mfma_f32_16x16x32_bf16 v[76:79], a[212:215], v[180:183], v[76:79]\n\t"
    "buffer_load_dwordx4 a[88:91], v240, s[64:67], s34 offen offset:2048 sc1\n\t"
    "v_mfma_f32_16x16x32_bf16 v[68:71], a[212:215], v[176:179], v[68:71]\n\t"
    "ds_read_b64_tr_b16 a[212:213], v238 offset:8\n\t"
    "v_mfma_f32_16x16x32_bf16 v[96:99], a[216:219], v[176:179], v[96:99]\n\t"
    "ds_read_b64_tr_b16 a[214:215], v238 offset:1032\n\t"
    "v_mfma_f32_16x16x32_bf16 v[104:107], a[216:219], v[180:183], v[104:107]\n\t"
    "s_waitcnt lgkmcnt(12)\n\t"
    "global_store_dwordx4 v240, v[212:215], s[50:51] offset:1024 sc1\n\t"
    "v_mfma_f32_16x16x32_bf16 v[112:115], a[216:219], v[184:187], v[112:115]\n\t"
    "v_mfma_f32_16x16x32_bf16 v[120:123], a[216:219], v[188:191], v[120:123]\n\t"
    "ds_read_b64_tr_b16 a[216:217], v239 offset:0\n\t"
    "v_mfma_f32_16x16x32_bf16 v[124:127], a[220:223], v[188:191], v[124:127]\n\t"
    "ds_read_b64_tr_b16 a[218:219], v239 offset:1024\n\t"
    "v_mfma_f32_16x16x32_bf16 v[116:119], a[220:223], v[184:187], v[116:119]\n\t"
    "v_mfma_f32_16x16x32_bf16 v[108:111], a[220:223], v[180:183], v[108:111]\n\t"
    "buffer_load_dwordx4 a[92:95], v240, s[64:67], s34 offen offset:3072 sc1\n\t"
    "v_mfma_f32_16x16x32_bf16 v[100:103], a[220:223], v[176:179], v[100:103]\n\t"
    "ds_read_b64_tr_b16 a[220:221], v239 offset:8\n\t"
    "v_mfma_f32_16x16x32_bf16 v[0:3], a[192:195], a[48:51], v[0:3]\n\t"
    "ds_read_b64_tr_b16 a[222:223], v239 offset:1032\n\t"
    "v_mfma_f32_16x16x32_bf16 v[8:11], a[192:195], a[52:55], v[8:11]\n\t"
    "v_add_u32 v249, 64, v249\n\t"
    "v_mfma_f32_16x16x32_bf16 v[16:19], a[192:195], a[56:59], v[16:19]\n\t"
    "v_mfma_f32_16x16x32_bf16 v[24:27], a[192:195], a[60:63], v[24:27]\n\t"
    "s_waitcnt lgkmcnt(14)\n\t"
    "ds_read_b64_tr_b16 a[192:193], v236 offset:8224\n\t"
    "v_mfma_f32_16x16x32_bf16 v[28:31], a[196:199], a[60:63], v[28:31]\n\t"
    "ds_read_b64_tr_b16 a[194:195], v236 offset:9248\n\t"
    "v_readlane_b32 s24, v246, 39\n\t"
    "v_mfma_f32_16x16x32_bf16 v[20:23], a[196:199], a[56:59], v[20:23]\n\t"
    "s_add_u32 m0, s31, 90464\n\t"
    "v_mfma_f32_16x16x32_bf16 v[12:15], a[196:199], a[52:55], v[12:15]\n\t"
    "s_lshl_b32 s35, s24, 4\n\t"
    "buffer_load_dwordx4 v243, s[96:99], s35 offen sc1 lds\n\t"
    "v_mfma_f32_16x16x32_bf16 v[4:7], a[196:199], a[48:51], v[4:7]\n\t"
    "s_waitcnt lgkmcnt(14)\n\t"
    "ds_read_b64_tr_b16 a[196:197], v236 offset:8232\n\t"
    "v_mfma_f32_16x16x32_bf16 v[32:35], a[200:203], a[48:51], v[32:35]\n\t"
    "ds_read_b64_tr_b16 a[198:199], v236 offset:9256\n\t"
    "v_mfma_f32_16x16x32_bf16 v[40:43], a[200:203], a[52:55], v[40:43]\n\t"
    "v_mov_b64 v[250:251], 0\n\t"
    "s_mov_b64 exec, 0x7f\n\t"
    "global_atomic_or_x2 v[250:251], v249, v[250:251], s[20:21] sc0 sc1\n\t"
    "s_mov_b64 exec, -1\n\t"
    "v_mfma_f32_16x16x32_bf16 v[48:51], a[200:203], a[56:59], v[48:51]\n\t"
    "v_mfma_f32_16x16x32_bf16 v[56:59], a[200:203], a[60:63], v[56:59]\n\t"
    "s_waitcnt lgkmcnt(13)\n\t"
    "ds_read_b64_tr_b16 a[200:201], v237 offset:8224\n\t"
    "v_mfma_f32_16x16x32_bf16 v[60:63], a[204:207], a[60:63], v[60:63]\n\t"
    "ds_read_b64_tr_b16 a[202:203], v237 offset:9248\n\t"
    "v_mfma_f32_16x16x32_bf16 v[52:55], a[204:207], a[56:59], v[52:55]\n\t"
    "v_mfma_f32_16x16x32_bf16 v[44:47], a[204:207], a[52:55], v[44:47]\n\t"
    "buffer_load_dwordx4 v244, s[96:99], s35 offen offset:1024 sc1 lds\n\t"
    "v_mfma_f32_16x16x32_bf16 v[36:39], a[204:207], a[48:51], v[36:39]\n\t"
    "s_waitcnt lgkmcnt(12)\n\t"
    "ds_read_b64_tr_b16 a[204:205], v237 offset:8232\n\t"
    "v_mfma_f32_16x16x32_bf16 v[64:67], a[208:211], a[48:51], v[64:67]\n\t"
    "ds_read_b64_tr_b16 a[206:207], v237 offset:9256\n\t"
    "v_mfma_f32_16x16x32_bf16 v[72:75], a[208:211], a[52:55], v[72:75]\n\t"
    "global_store_dwordx4 v240, v[216:219], s[50:51] offset:2048 sc1\n\t"
    "v_mfma_f32_16x16x32_bf16 v[80:83], a[208:211], a[56:59], v[80:83]\n\t"
    "v_mfma_f32_16x16x32_bf16 v[88:91], a[208:211], a[60:63], v[88:91]\n\t"
    "s_waitcnt lgkmcnt(12)\n\t"
    "ds_read_b64_tr_b16 a[208:209], v238 offset:8224\n\t"
    "v_mfma_f32_16x16x32_bf16 v[92:95], a[212:215], a[60:63], v[92:95]\n\t"
    "ds_read_b64_tr_b16 a[210:211], v238 offset:9248\n\t"
    "v_readlane_b32 s24, v246, 40\n\t"
    "v_mfma_f32_16x16x32_bf16 v[84:87], a[212:215], a[56:59], v[84:87]\n\t"
    "s_add_u32 m0, s31, 98688\n\t"
    "v_mfma_f32_16x16x32_bf16 v[76:79], a[212:215], a[52:55], v[76:79]\n\t"
    "s_lshl_b32 s35, s24, 4\n\t"
    "buffer_load_dwordx4 v243, s[96:99], s35 offen sc1 lds\n\t"
    "v_mfma_f32_16x16x32_bf16 v[68:71], a[212:215], a[48:51], v[68:71]\n\t"
    "s_waitcnt lgkmcnt(12)\n\t"
    "ds_read_b64_tr_b16 a[212:213], v238 offset:8232\n\t"
    "v_mfma_f32_16x16x32_bf16 v[96:99], a[216:219], a[48:51], v[96:99]\n\t"
    "ds_read_b64_tr_b16 a[214:215], v238 offset:9256\n\t"
    "v_mfma_f32_16x16x32_bf16 v[104:107], a[216:219], a[52:55], v[104:107]\n\t"
    "global_store_dwordx4 v240, v[220:223], s[50:51] offset:3072 sc1\n\t"
    "v_mfma_f32_16x16x32_bf16 v[112:115], a[216:219], a[56:59], v[112:115]\n\t"
    "v_mfma_f32_16x16x32_bf16 v[120:123], a[216:219], a[60:63], v[120:123]\n\t"
    "s_waitcnt lgkmcnt(12)\n\t"
    "ds_read_b64_tr_b16 a[216:217], v239 offset:8224\n\t"
    "v_mfma_f32_16x16x32_bf16 v[124:127], a[220:223], a[60:63], v[124:127]\n\t"
    "ds_read_b64_tr_b16 a[218:219], v239 offset:9248\n\t"
    "v_mfma_f32_16x16x32_bf16 v[116:119], a[220:223], a[56:59], v[116:119]\n\t"
    "v_mfma_f32_16x16x32_bf16 v[108:111], a[220:223], a[52:55], v[108:111]\n\t"
    "buffer_load_dwordx4 v244, s[96:99], s35 offen offset:1024 sc1 lds\n\t"
    "v_mfma_f32_16x16x32_bf16 v[100:103], a[220:223], a[48:51], v[100:103]\n\t"
    "s_waitcnt vmcnt(13) lgkmcnt(12)\n\t"
    "ds_read_b64_tr_b16 a[220:221], v239 offset:8232\n\t"
    "v_mfma_f32_16x16x32_bf16 v[0:3], a[192:195], a[64:67], v[0:3]\n\t"
    "s_barrier\n\t"
    "v_readlane_b32 s24, v246, 6\n\t"
    "v_mfma_f32_16x16x32_bf16 v[8:11], a[192:195], a[68:71], v[8:11]\n\t"
    "ds_read_b64_tr_b16 a[222:223], v239 offset:9256\n\t"
    "v_mfma_f32_16x16x32_bf16 v[16:19], a[192:195], a[72:75], v[16:19]\n\t"
    "s_mov_b32 s34, s24\n\t"
    "buffer_load_dwordx4 a[96:99], v240, s[64:67], s34 offen offset:0 sc1\n\t"
    "v_mfma_f32_16x16x32_bf16 v[24:27], a[192:195], a[76:79], v[24:27]\n\t"
    "s_waitcnt lgkmcnt(12)\n\t"
    "ds_read_b64_tr_b16 a[192:193], v236 offset:16448\n\t"
    "v_mfma_f32_16x16x32_bf16 v[28:31], a[196:199], a[76:79], v[28:31]\n\t"
    "ds_read_b64_tr_b16 a[194:195], v236 offset:17472\n\t"
    "v_mfma_f32_16x16x32_bf16 v[20:23], a[196:199], a[72:75], v[20:23]\n\t"
    "v_mfma_f32_16x16x32_bf16 v[12:15], a[196:199], a[68:71], v[12:15]\n\t"
    "buffer_load_dwordx4 a[100:103], v240, s[64:67], s34 offen offset:1024 sc1\n\t"
    "v_mfma_f32_16x16x32_bf16 v[4:7], a[196:199], a[64:67], v[4:7]\n\t"
    "s_waitcnt lgkmcnt(12)\n\t"
    "ds_read_b64_tr_b16 a[196:197], v236 offset:16456\n\t"
    "v_mfma_f32_16x16x32_bf16 v[32:35], a[200:203], a[64:67], v[32:35]\n\t"
    "ds_read_b64_tr_b16 a[198:199], v236 offset:17480\n\t"
    "v_mfma_f32_16x16x32_bf16 v[40:43], a[200:203], a[68:71], v[40:43]\n\t"
    "v_mfma_f32_16x16x32_bf16 v[48:51], a[200:203], a[72:75], v[48:51]\n\t"
    "buffer_load_dwordx4 a[104:107], v240, s[64:67], s34 offen offset:2048 sc1\n\t"
    "v_mfma_f32_16x16x32_bf16 v[56:59], a[200:203], a[76:79], v[56:59]\n\t"
    "s_waitcnt lgkmcnt(12)\n\t"
    "ds_read_b64_tr_b16 a[200:201], v237 offset:16448\n\t"
    "v_mfma_f32_16x16x32_bf16 v[60:63], a[204:207], a[76:79], v[60:63]\n\t"
    "ds_read_b64_tr_b16 a[202:203], v237 offset:17472\n\t"
    "v_mfma_f32_16x16x32_bf16 v[52:55], a[204:207], a[72:75], v[52:55]\n\t"
    "v_mfma_f32_16x16x32_bf16 v[44:47], a[204:207], a[68:71], v[44:47]\n\t"
    "v_mfma_f32_16x16x32_bf16 v[36:39], a[204:207], a[64:67], v[36:39]\n\t"
    "s_waitcnt lgkmcnt(12)\n\t"
    "ds_read_b64_tr_b16 a[204:205], v237 offset:16456\n\t"
    "v_mfma_f32_16x16x32_bf16 v[64:67], a[208:211], a[64:67], v[64:67]\n\t"
    "ds_read_b64_tr_b16 a[206:207], v237 offset:17480\n\t"
    "v_mfma_f32_16x16x32_bf16 v[72:75], a[208:211], a[68:71], v[72:75]\n\t"
    "v_mfma_f32_16x16x32_bf16 v[80:83], a[208:211], a[72:75], v[80:83]\n\t"
    "buffer_load_dwordx4 a[108:111], v240, s[64:67], s34 offen offset:3072 sc1\n\t"
    "v_mfma_f32_16x16x32_bf16 v[88:91], a[208:211], a[76:79], v[88:91]\n\t"
    "s_waitcnt lgkmcnt(12)\n\t"
    "ds_read_b64_tr_b16 a[208:209], v238 offset:16448\n\t"
    "v_mfma_f32_16x16x32_bf16 v[92:95], a[212:215], a[76:79], v[92:95]\n\t"
    "ds_read_b64_tr_b16 a[210:211], v238 offset:17472\n\t"
    "v_readlane_b32 s24, v246, 41\n\t"
    "v_mfma_f32_16x16x32_bf16 v[84:87], a[212:215], a[72:75], v[84:87]\n\t"
    "s_add_u32 m0, s31, 106912\n\t"
    "v_mfma_f32_16x16x32_bf16 v[76:79], a[212:215], a[68:71], v[76:79]\n\t"
    "s_lshl_b32 s35, s24, 4\n\t"
    "buffer_load_dwordx4 v243, s[96:99], s35 offen sc1 lds\n\t"
    "v_mfma_f32_16x16x32_bf16 v[68:71], a[212:215], a[64:67], v[68:71]\n\t"
    "s_waitcnt lgkmcnt(12)\n\t"
    "ds_read_b64_tr_b16 a[212:213], v238 offset:16456\n\t"
    "v_mfma_f32_16x16x32_bf16 v[96:99], a[216:219], a[64:67], v[96:99]\n\t"
    "ds_read_b64_tr_b16 a[214:215], v238 offset:17480\n\t"
    "v_mfma_f32_16x16x32_bf16 v[104:107], a[216:219], a[68:71], v[104:107]\n\t"
    "s_waitcnt vmcnt(10)\n\t"
    "v_cmp_ne_u64 vcc, s[74:75], v[250:251]\n\t"
    "v_mfma_f32_16x16x32_bf16 v[112:115], a[216:219], a[72:75], v[112:115]\n\t"
    "buffer_load_dwordx4 v244, s[96:99], s35 offen offset:1024 sc1 lds\n\t"
    "v_mfma_f32_16x16x32_bf16 v[120:123], a[216:219], a[76:79], v[120:123]\n\t"
    "s_waitcnt lgkmcnt(12)\n\t"
    "ds_read_b64_tr_b16 a[216:217], v239 offset:16448\n\t"
    "v_mfma_f32_16x16x32_bf16 v[124:127], a[220:223], a[76:79], v[124:127]\n\t"
    "ds_read_b64_tr_b16 a[218:219], v239 offset:17472\n\t"
    "v_readlane_b32 s24, v246, 42\n\t"
    "v_mfma_f32_16x16x32_bf16 v[116:119], a[220:223], a[72:75], v[116:119]\n\t"
    "s_add_u32 m0, s31, 0\n\t"
    "s_and_b64 vcc, vcc, 0x7f\n\t"
    "s_cbranch_scc1 Ls18_%=\n\t"
    "Lb19_%=:\n\t"
    "v_mfma_f32_16x16x32_bf16 v[108:111], a[220:223], a[68:71], v[108:111]\n\t"
    "s_lshl_b32 s35, s24, 4\n\t"
    "buffer_load_dwordx4 v243, s[96:99], s35 offen sc1 lds\n\t"
    "v_mfma_f32_16x16x32_bf16 v[100:103], a[220:223], a[64:67], v[100:103]\n\t"
    "s_waitcnt lgkmcnt(12)\n\t"
    "ds_read_b64_tr_b16 a[220:221], v239 offset:16456\n\t"
    "v_mfma_f32_16x16x32_bf16 v[0:3], a[192:195], a[80:83], v[0:3]\n\t"
    "ds_read_b64_tr_b16 a[222:223], v239 offset:17480\n\t"
    "v_mfma_f32_16x16x32_bf16 v[8:11], a[192:195], a[84:87], v[8:11]\n\t"
    "v_mfma_f32_16x16x32_bf16 v[16:19], a[192:195], a[88:91], v[16:19]\n\t"
    "buffer_load_dwordx4 v244, s[96:99], s35 offen offset:1024 sc1 lds\n\t"
    "v_mfma_f32_16x16x32_bf16 v[24:27], a[192:195], a[92:95], v[24:27]\n\t"
    "s_waitcnt lgkmcnt(12)\n\t"
    "ds_read_b64_tr_b16 a[192:193], v236 offset:24672\n\t"
    "v_mfma_f32_16x16x32_bf16 v[28:31], a[196:199], a[92:95], v[28:31]\n\t"
    "ds_read_b64_tr_b16 a[194:195], v236 offset:25696\n\t"
    "v_readlane_b32 s24, v246, 7\n\t"
    "v_mfma_f32_16x16x32_bf16 v[20:23], a[196:199], a[88:91], v[20:23]\n\t"
    "v_mfma_f32_16x16x32_bf16 v[12:15], a[196:199], a[84:87], v[12:15]\n\t"
    "s_mov_b32 s34, s24\n\t"
    "buffer_load_dwordx4 a[112:115], v240, s[64:67], s34 offen offset:0 sc1\n\t"
    "v_mfma_f32_16x16x32_bf16 v[4:7], a[196:199], a[80:83], v[4:7]\n\t"
    "s_waitcnt lgkmcnt(12)\n\t"
    "ds_read_b64_tr_b16 a[196:197], v236 offset:24680\n\t"
    "v_mfma_f32_16x16x32_bf16 v[32:35], a[200:203], a[80:83], v[32:35]\n\t"
    "ds_read_b64_tr_b16 a[198:199], v236 offset:25704\n\t"
    "v_mfma_f32_16x16x32_bf16 v[40:43], a[200:203], a[84:87], v[40:43]\n\t"
    "v_mfma_f32_16x16x32_bf16 v[48:51], a[200:203], a[88:91], v[48:51]\n\t"
    "buffer_load_dwordx4 a[116:119], v240, s[64:67], s34 offen offset:1024 sc1\n\t"
    "v_mfma_f32_16x16x32_bf16 v[56:59], a[200:203], a[92:95], v[56:59]\n\t"
    "s_waitcnt lgkmcnt(12)\n\t"
    "ds_read_b64_tr_b16 a[200:201], v237 offset:24672\n\t"
    "v_mfma_f32_16x16x32_bf16 v[60:63], a[204:207], a[92:95], v[60:63]\n\t"
    "ds_read_b64_tr_b16 a[202:203], v237 offset:25696\n\t"
    "v_mfma_f32_16x16x32_bf16 v[52:55], a[204:207], a[88:91], v[52:55]\n\t"
    "v_mfma_f32_16x16x32_bf16 v[44:47], a[204:207], a[84:87], v[44:47]\n\t"
    "buffer_load_dwordx4 a[120:123], v240, s[64:67], s34 offen offset:2048 sc1\n\t"
    "v_mfma_f32_16x16x32_bf16 v[36:39], a[204:207], a[80:83], v[36:39]\n\t"
    "s_waitcnt lgkmcnt(12)\n\t"
    "ds_read_b64_tr_b16 a[204:205], v237 offset:24680\n\t"
    "v_mfma_f32_16x16x32_bf16 v[64:67], a[208:211], a[80:83], v[64:67]\n\t"
    "ds_read_b64_tr_b16 a[206:207], v237 offset:25704\n\t"
    "v_mfma_f32_16x16x32_bf16 v[72:75], a[208:211], a[84:87], v[72:75]\n\t"
    "v_mfma_f32_16x16x32_bf16 v[80:83], a[208:211], a[88:91], v[80:83]\n\t"
    "buffer_load_dwordx4 a[124:127], v240, s[64:67], s34 offen offset:3072 sc1\n\t"
    "v_mfma_f32_16x16x32_bf16 v[88:91], a[208:211], a[92:95], v[88:91]\n\t"
    "s_waitcnt lgkmcnt(12)\n\t"
    "ds_read_b64_tr_b16 a[208:209], v238 offset:24672\n\t"
    "v_mfma_f32_16x16x32_bf16 v[92:95], a[212:215], a[92:95], v[92:95]\n\t"
    "ds_read_b64_tr_b16 a[210:211], v238 offset:25696\n\t"
    "v_readlane_b32 s24, v246, 8\n\t"
    "v_mfma_f32_16x16x32_bf16 v[84:87], a[212:215], a[88:91], v[84:87]\n\t"
    "v_mfma_f32_16x16x32_bf16 v[76:79], a[212:215], a[84:87], v[76:79]\n\t"
    "s_mov_b32 s34, s24\n\t"
    "buffer_load_dwordx4 a[0:3], v240, s[64:67], s34 offen offset:0 sc1\n\t"
    "v_mfma_f32_16x16x32_bf16 v[68:71], a[212:215], a[80:83], v[68:71]\n\t"
    "s_waitcnt lgkmcnt(12)\n\t"
    "ds_read_b64_tr_b16 a[212:213], v238 offset:24680\n\t"
    "v_mfma_f32_16x16x32_bf16 v[96:99], a[216:219], a[80:83], v[96:99]\n\t"
    "ds_read_b64_tr_b16 a[214:215], v238 offset:25704\n\t"
    "v_mfma_f32_16x16x32_bf16 v[104:107], a[216:219], a[84:87], v[104:107]\n\t"
    "v_mfma_f32_16x16x32_bf16 v[112:115], a[216:219], a[88:91], v[112:115]\n\t"
    "buffer_load_dwordx4 a[4:7], v240, s[64:67], s34 offen offset:1024 sc1\n\t"
    "v_mfma_f32_16x16x32_bf16 v[120:123], a[216:219], a[92:95], v[120:123]\n\t"
    "s_waitcnt lgkmcnt(12)\n\t"
    "ds_read_b64_tr_b16 a[216:217], v239 offset:24672\n\t"
    "v_mfma_f32_16x16x32_bf16 v[124:127], a[220:223], a[92:95], v[124:127]\n\t"
    "ds_read_b64_tr_b16 a[218:219], v239 offset:25696\n\t"
    "v_mfma_f32_16x16x32_bf16 v[116:119], a[220:223], a[88:91], v[116:119]\n\t"
    "v_mfma_f32_16x16x32_bf16 v[108:111], a[220:223], a[84:87], v[108:111]\n\t"
    "buffer_load_dwordx4 a[8:11], v240, s[64:67], s34 offen offset:2048 sc1\n\t"
    "v_mfma_f32_16x16x32_bf16 v[100:103], a[220:223], a[80:83], v[100:103]\n\t"
    "s_waitcnt vmcnt(11) lgkmcnt(12)\n\t"
    "ds_read_b64_tr_b16 a[220:221], v239 offset:24680\n\t"
    "v_mfma_f32_16x16x32_bf16 v[0:3], a[192:195], a[96:99], v[0:3]\n\t"
    "s_barrier\n\t"
    "v_mfma_f32_16x16x32_bf16 v[8:11], a[192:195], a[100:103], v[8:11]\n\t"
    "s_lshl_b32 s38, s25, 4\n\t"
    "ds_read_b64_tr_b16 a[222:223], v239 offset:25704\n\t"
    "v_mfma_f32_16x16x32_bf16 v[16:19], a[192:195], a[104:107], v[16:19]\n\t"
    "s_and_b32 s39, s27, 15\n\t"
    "buffer_load_dwordx4 a[12:15], v240, s[64:67], s34 offen offset:3072 sc1\n\t"
    "v_mfma_f32_16x16x32_bf16 v[24:27], a[192:195], a[108:111], v[24:27]\n\t"
    "s_waitcnt lgkmcnt(12)\n\t"
    "ds_read_b64_tr_b16 a[192:193], v236 offset:32896\n\t"
    "v_mfma_f32_16x16x32_bf16 v[28:31], a[196:199], a[108:111], v[28:31]\n\t"
    "s_add_u32 s38, s38, s39\n\t"
    "ds_read_b64_tr_b16 a[194:195], v236 offset:33920\n\t"
    "v_readlane_b32 s24, v246, 9\n\t"
    "v_mfma_f32_16x16x32_bf16 v[20:23], a[196:199], a[104:107], v[20:23]\n\t"
    "s_lshl_b32 s38, s38, 3\n\t"
    "v_mfma_f32_16x16x32_bf16 v[12:15], a[196:199], a[100:103], v[12:15]\n\t"
    "s_mov_b32 s34, s24\n\t"
    "buffer_load_dwordx4 a[16:19], v240, s[64:67], s34 offen offset:0 sc1\n\t"
    "v_mfma_f32_16x16x32_bf16 v[4:7], a[196:199], a[96:99], v[4:7]\n\t"
    "s_waitcnt lgkmcnt(12)\n\t"
    "ds_read_b64_tr_b16 a[196:197], v236 offset:32904\n\t"
    "s_add_u32 s38, s38, s23\n\t"
    "v_mfma_f32_16x16x32_bf16 v[32:35], a[200:203], a[96:99], v[32:35]\n\t"
    "s_lshl_b32 s38, s38, 15\n\t"
    "ds_read_b64_tr_b16 a[198:199], v236 offset:33928\n\t"
    "v_mfma_f32_16x16x32_bf16 v[40:43], a[200:203], a[100:103], v[40:43]\n\t"
    "v_mfma_f32_16x16x32_bf16 v[48:51], a[200:203], a[104:107], v[48:51]\n\t"
    "s_lshr_b32 s39, s38, 8\n\t"
    "buffer_load_dwordx4 a[20:23], v240, s[64:67], s34 offen offset:1024 sc1\n\t"
    "v_mfma_f32_16x16x32_bf16 v[56:59], a[200:203], a[108:111], v[56:59]\n\t"
    "s_waitcnt lgkmcnt(12)\n\t"
    "ds_read_b64_tr_b16 a[200:201], v237 offset:32896\n\t"
    "s_xor_b32 s39, s39, s30\n\t"
    "v_mfma_f32_16x16x32_bf16 v[60:63], a[204:207], a[108:111], v[60:63]\n\t"
    "ds_read_b64_tr_b16 a[202:203], v237 offset:33920\n\t"
    "v_mfma_f32_16x16x32_bf16 v[52:55], a[204:207], a[104:107], v[52:55]\n\t"
    "s_and_b32 s39, s39, 0x2000\n\t"
    "v_mfma_f32_16x16x32_bf16 v[44:47], a[204:207], a[100:103], v[44:47]\n\t"
    "buffer_load_dwordx4 a[24:27], v240, s[64:67], s34 offen offset:2048 sc1\n\t"
    "v_mfma_f32_16x16x32_bf16 v[36:39], a[204:207], a[96:99], v[36:39]\n\t"
    "s_waitcnt lgkmcnt(12)\n\t"
    "ds_read_b64_tr_b16 a[204:205], v237 offset:32904\n\t"
    "s_or_b32 s38, s38, s39\n\t"
    "v_mfma_f32_16x16x32_bf16 v[64:67], a[208:211], a[96:99], v[64:67]\n\t"
    "ds_read_b64_tr_b16 a[206:207], v237 offset:33928\n\t"
    "v_mfma_f32_16x16x32_bf16 v[72:75], a[208:211], a[100:103], v[72:75]\n\t"
    "s_add_u32 s38, s38, 0x2400000\n\t"
    "s_add_u32 s46, s20, s38\n\t"
    "s_addc_u32 s47, s21, 0\n\t"
    "v_mfma_f32_16x16x32_bf16 v[80:83], a[208:211], a[104:107], v[80:83]\n\t"
    "buffer_load_dwordx4 a[28:31], v240, s[64:67], s34 offen offset:3072 sc1\n\t"
    "v_and_b32 v241, 15, v245\n\t"
    "v_mfma_f32_16x16x32_bf16 v[88:91], a[208:211], a[108:111], v[88:91]\n\t"
    "s_waitcnt lgkmcnt(12)\n\t"
    "ds_read_b64_tr_b16 a[208:209], v238 offset:32896\n\t"
    "v_lshrrev_b32 v242, 4, v245\n\t"
    "v_mfma_f32_16x16x32_bf16 v[92:95], a[212:215], a[108:111], v[92:95]\n\t"
    "Lpad8_%=:\n\t"
    "s_cbranch_execz Lpad12_%=\n\t"
    "ds_read_b64_tr_b16 a[210:211], v238 offset:33920\n\t"
    "v_readlane_b32 s24, v246, 43\n\t"
    "v_mfma_f32_16x16x32_bf16 v[84:87], a[212:215], a[104:107], v[84:87]\n\t"
    "s_add_u32 m0, s31, 8224\n\t"
    "v_lshlrev_b32 v242, 4, v242\n\t"
    "v_mfma_f32_16x16x32_bf16 v[76:79], a[212:215], a[100:103], v[76:79]\n\t"
    "s_lshl_b32 s35, s24, 4\n\t"
    "buffer_load_dwordx4 v243, s[96:99], s35 offen sc1 lds\n\t"
    "v_lshl_or_b32 v241, v241, 6, v242\n\t"
    "v_mfma_f32_16x16x32_bf16 v[68:71], a[212:215], a[96:99], v[68:71]\n\t"
    "s_waitcnt lgkmcnt(12)\n\t"
    "ds_read_b64_tr_b16 a[212:213], v238 offset:32904\n\t"
    "v_mfma_f32_16x16x32_bf16 v[96:99], a[216:219], a[96:99], v[96:99]\n\t"
    "ds_read_b64_tr_b16 a[214:215], v238 offset:33928\n\t"
    "v_and_b32 v242, 0x7ff, v247\n\t"
    "v_mfma_f32_16x16x32_bf16 v[104:107], a[216:219], a[100:103], v[104:107]\n\t"
    "s_lshl_b32 s38, s22, 9\n\t"
    "v_mfma_f32_16x16x32_bf16 v[112:115], a[216:219], a[104:107], v[112:115]\n\t"
    "buffer_load_dwordx4 v244, s[96:99], s35 offen offset:1024 sc1 lds\n\t"
    "v_mfma_f32_16x16x32_bf16 v[120:123], a[216:219], a[108:111], v[120:123]\n\t"
    "s_waitcnt lgkmcnt(12)\n\t"
    "ds_read_b64_tr_b16 a[216:217], v239 offset:32896\n\t"
    "v_mfma_f32_16x16x32_bf16 v[124:127], a[220:223], a[108:111], v[124:127]\n\t"
    "ds_read_b64_tr_b16 a[218:219], v239 offset:33920\n\t"
    "v_readlane_b32 s24, v246, 44\n\t"
    "v_mfma_f32_16x16x32_bf16 v[116:119], a[220:223], a[104:107], v[116:119]\n\t"
    "s_add_u32 m0, s31, 16448\n\t"
    "s_add_u32 s44, s16, s38\n\t"
    "s_addc_u32 s45, s17, 0\n\t"
    "v_mfma_f32_16x16x32_bf16 v[108:111], a[220:223], a[100:103], v[108:111]\n\t"
    "s_lshl_b32 s35, s24, 4\n\t"
    "buffer_load_dwordx4 v243, s[96:99], s35 offen sc1 lds\n\t"
    "v_mfma_f32_16x16x32_bf16 v[100:103], a[220:223], a[96:99], v[100:103]\n\t"
    "s_waitcnt vmcnt(11) lgkmcnt(12)\n\t"
    "ds_read_b64_tr_b16 a[220:221], v239 offset:32904\n\t"
    "v_mfma_f32_16x16x32_bf16 v[0:3], a[192:195], a[112:115], v[0:3]\n\t"
    "ds_read_b64_tr_b16 a[222:223], v239 offset:33928\n\t"
    "v_mfma_f32_16x16x32_bf16 v[8:11], a[192:195], a[116:119], v[8:11]\n\t"
    "v_mfma_f32_16x16x32_bf16 v[16:19], a[192:195], a[120:123], v[16:19]\n\t"
    "buffer_load_dwordx4 v244, s[96:99], s35 offen offset:1024 sc1 lds\n\t"
    "v_mfma_f32_16x16x32_bf16 v[24:27], a[192:195], a[124:127], v[24:27]\n\t"
    "s_waitcnt lgkmcnt(12)\n\t"
    "ds_read_b64_tr_b16 a[192:193], v236 offset:41120\n\t"
    "v_mfma_f32_16x16x32_bf16 v[28:31], a[196:199], a[124:127], v[28:31]\n\t"
    "ds_read_b64_tr_b16 a[194:195], v236 offset:42144\n\t"
    "v_readlane_b32 s24, v246, 10\n\t"
    "v_mfma_f32_16x16x32_bf16 v[20:23], a[196:199], a[120:123], v[20:23]\n\t"
    "v_mfma_f32_16x16x32_bf16 v[12:15], a[196:199], a[116:119], v[12:15]\n\t"
    "s_mov_b32 s34, s24\n\t"
    "buffer_load_dwordx4 a[32:35], v240, s[64:67], s34 offen offset:0 sc1\n\t"
    "v_mfma_f32_16x16x32_bf16 v[4:7], a[196:199], a[112:115], v[4:7]\n\t"
    "s_waitcnt lgkmcnt(12)\n\t"
    "ds_read_b64_tr_b16 a[196:197], v236 offset:41128\n\t"
    "v_mfma_f32_16x16x32_bf16 v[32:35], a[200:203], a[112:115], v[32:35]\n\t"
    "ds_read_b64_tr_b16 a[198:199], v236 offset:42152\n\t"
    "v_mfma_f32_16x16x32_bf16 v[40:43], a[200:203], a[116:119], v[40:43]\n\t"
    "v_mfma_f32_16x16x32_bf16 v[48:51], a[200:203], a[120:123], v[48:51]\n\t"
    "buffer_load_dwordx4 a[36:39], v240, s[64:67], s34 offen offset:1024 sc1\n\t"
    "v_mfma_f32_16x16x32_bf16 v[56:59], a[200:203], a[124:127], v[56:59]\n\t"
    "s_waitcnt lgkmcnt(12)\n\t"
    "ds_read_b64_tr_b16 a[200:201], v237 offset:41120\n\t"
    "v_mfma_f32_16x16x32_bf16 v[60:63], a[204:207], a[124:127], v[60:63]\n\t"
    "ds_read_b64_tr_b16 a[202:203], v237 offset:42144\n\t"
    "v_mfma_f32_16x16x32_bf16 v[52:55], a[204:207], a[120:123], v[52:55]\n\t"
    "v_mfma_f32_16x16x32_bf16 v[44:47], a[204:207], a[116:119], v[44:47]\n\t"
    "buffer_load_dwordx4 a[40:43], v240, s[64:67], s34 offen offset:2048 sc1\n\t"
    "v_mfma_f32_16x16x32_bf16 v[36:39], a[204:207], a[112:115], v[36:39]\n\t"
    "s_waitcnt lgkmcnt(12)\n\t"
    "ds_read_b64_tr_b16 a[204:205], v237 offset:41128\n\t"
    "v_mfma_f32_16x16x32_bf16 v[64:67], a[208:211], a[112:115], v[64:67]\n\t"
    "ds_read_b64_tr_b16 a[206:207], v237 offset:42152\n\t"
    "v_mfma_f32_16x16x32_bf16 v[72:75], a[208:211], a[116:119], v[72:75]\n\t"
    "v_mfma_f32_16x16x32_bf16 v[80:83], a[208:211], a[120:123], v[80:83]\n\t"
    "buffer_load_dwordx4 a[44:47], v240, s[64:67], s34 offen offset:3072 sc1\n\t"
    "v_mfma_f32_16x16x32_bf16 v[88:91], a[208:211], a[124:127], v[88:91]\n\t"
    "s_waitcnt lgkmcnt(12)\n\t"
    "ds_read_b64_tr_b16 a[208:209], v238 offset:41120\n\t"
    "v_mfma_f32_16x16x32_bf16 v[92:95], a[212:215], a[124:127], v[92:95]\n\t"
    "ds_read_b64_tr_b16 a[210:211], v238 offset:42144\n\t"
    "v_readlane_b32 s24, v246, 11\n\t"
    "v_mfma_f32_16x16x32_bf16 v[84:87], a[212:215], a[120:123], v[84:87]\n\t"
    "v_mfma_f32_16x16x32_bf16 v[76:79], a[212:215], a[116:119], v[76:79]\n\t"
    "s_mov_b32 s34, s24\n\t"
    "buffer_load_dwordx4 a[48:51], v240, s[64:67], s34 offen offset:0 sc1\n\t"
    "v_mfma_f32_16x16x32_bf16 v[68:71], a[212:215], a[112:115], v[68:71]\n\t"
    "s_waitcnt lgkmcnt(12)\n\t"
    "ds_read_b64_tr_b16 a[212:213], v238 offset:41128\n\t"
    "v_mfma_f32_16x16x32_bf16 v[96:99], a[216:219], a[112:115], v[96:99]\n\t"
    "ds_read_b64_tr_b16 a[214:215], v238 offset:42152\n\t"
    "v_mfma_f32_16x16x32_bf16 v[104:107], a[216:219], a[116:119], v[104:107]\n\t"
    "v_mfma_f32_16x16x32_bf16 v[112:115], a[216:219], a[120:123], v[112:115]\n\t"
    "buffer_load_dwordx4 a[52:55], v240, s[64:67], s34 offen offset:1024 sc1\n\t"
    "v_mfma_f32_16x16x32_bf16 v[120:123], a[216:219], a[124:127], v[120:123]\n\t"
    "s_waitcnt lgkmcnt(12)\n\t"
    "ds_read_b64_tr_b16 a[216:217], v239 offset:41120\n\t"
    "v_mfma_f32_16x16x32_bf16 v[124:127], a[220:223], a[124:127], v[124:127]\n\t"
    "ds_read_b64_tr_b16 a[218:219], v239 offset:42144\n\t"
    "v_mfma_f32_16x16x32_bf16 v[116:119], a[220:223], a[120:123], v[116:119]\n\t"
    "v_mfma_f32_16x16x32_bf16 v[108:111], a[220:223], a[116:119], v[108:111]\n\t"
    "buffer_load_dwordx4 a[56:59], v240, s[64:67], s34 offen offset:2048 sc1\n\t"
    "v_mfma_f32_16x16x32_bf16 v[100:103], a[220:223], a[112:115], v[100:103]\n\t"
    "s_waitcnt vmcnt(15) lgkmcnt(12)\n\t"
    "ds_read_b64_tr_b16 a[220:221], v239 offset:41128\n\t"
    "v_mfma_f32_16x16x32_bf16 v[0:3], a[192:195], a[0:3], v[0:3]\n\t"
    "s_barrier\n\t"
    "v_mfma_f32_16x16x32_bf16 v[8:11], a[192:195], a[4:7], v[8:11]\n\t"
    "ds_read_b64_tr_b16 a[222:223], v239 offset:42152\n\t"
    "v_mfma_f32_16x16x32_bf16 v[16:19], a[192:195], a[8:11], v[16:19]\n\t"
    "buffer_load_dwordx4 a[60:63], v240, s[64:67], s34 offen offset:3072 sc1\n\t"
    "v_mfma_f32_16x16x32_bf16 v[24:27], a[192:195], a[12:15], v[24:27]\n\t"
    "s_waitcnt lgkmcnt(12)\n\t"
    "ds_read_b64_tr_b16 a[192:193], v236 offset:49344\n\t"
    "v_mfma_f32_16x16x32_bf16 v[28:31], a[196:199], a[12:15], v[28:31]\n\t"
    "ds_read_b64_tr_b16 a[194:195], v236 offset:50368\n\t"
    "v_readlane_b32 s24, v246, 45\n\t"
    "v_mfma_f32_16x16x32_bf16 v[20:23], a[196:199], a[8:11], v[20:23]\n\t"
    "s_add_u32 m0, s31, 24672\n\t"
    "v_mfma_f32_16x16x32_bf16 v[12:15], a[196:199], a[4:7], v[12:15]\n\t"
    "s_lshl_b32 s35, s24, 4\n\t"
    "buffer_load_dwordx4 v243, s[96:99], s35 offen sc1 lds\n\t"
    "v_mfma_f32_16x16x32_bf16 v[4:7], a[196:199], a[0:3], v[4:7]\n\t"
    "s_waitcnt lgkmcnt(12)\n\t"
    "ds_read_b64_tr_b16 a[196:197], v236 offset:49352\n\t"
    "v_mfma_f32_16x16x32_bf16 v[32:35], a[200:203], a[0:3], v[32:35]\n\t"
    "ds_read_b64_tr_b16 a[198:199], v236 offset:50376\n\t"
    "v_mfma_f32_16x16x32_bf16 v[40:43], a[200:203], a[4:7], v[40:43]\n\t"
    "s_mov_b64 exec, s[58:59]\n\t"
    "global_store_dwordx2 v248, v[254:255], s[52:53] offset:0 sc1\n\t"
    "s_mov_b64 exec, -1\n\t"
    "v_mfma_f32_16x16x32_bf16 v[48:51], a[200:203], a[8:11], v[48:51]\n\t"
    "buffer_load_dwordx4 v244, s[96:99], s35 offen offset:1024 sc1 lds\n\t"
    "v_mfma_f32_16x16x32_bf16 v[56:59], a[200:203], a[12:15], v[56:59]\n\t"
    "s_waitcnt lgkmcnt(12)\n\t"
    "ds_read_b64_tr_b16 a[200:201], v237 offset:49344\n\t"
    "v_mfma_f32_16x16x32_bf16 v[60:63], a[204:207], a[12:15], v[60:63]\n\t"
    "ds_read_b64_tr_b16 a[202:203], v237 offset:50368\n\t"
    "v_mfma_f32_16x16x32_bf16 v[52:55], a[204:207], a[8:11], v[52:55]\n\t"
    "v_mfma_f32_16x16x32_bf16 v[44:47], a[204:207], a[4:7], v[44:47]\n\t"
    "v_mfma_f32_16x16x32_bf16 v[36:39], a[204:207], a[0:3], v[36:39]\n\t"
    "s_waitcnt lgkmcnt(12)\n\t"
    "ds_read_b64_tr_b16 a[204:205], v237 offset:49352\n\t"
    "v_mfma_f32_16x16x32_bf16 v[64:67], a[208:211], a[0:3], v[64:67]\n\t"
    "ds_read_b64_tr_b16 a[206:207], v237 offset:50376\n\t"
    "v_readlane_b32 s24, v246, 46\n\t"
    "v_mfma_f32_16x16x32_bf16 v[72:75], a[208:211], a[4:7], v[72:75]\n\t"
    "s_add_u32 m0, s31, 32896\n\t"
    "v_mfma_f32_16x16x32_bf16 v[80:83], a[208:211], a[8:11], v[80:83]\n\t"
    "s_lshl_b32 s35, s24, 4\n\t"
    "buffer_load_dwordx4 v243, s[96:99], s35 offen sc1 lds\n\t"
    "v_mfma_f32_16x16x32_bf16 v[88:91], a[208:211], a[12:15], v[88:91]\n\t"
    "s_waitcnt lgkmcnt(12)\n\t"
    "ds_read_b64_tr_b16 a[208:209], v238 offset:49344\n\t"
    "v_mfma_f32_16x16x32_bf16 v[92:95], a[212:215], a[12:15], v[92:95]\n\t"
    "ds_read_b64_tr_b16 a[210:211], v238 offset:50368\n\t"
    "v_mfma_f32_16x16x32_bf16 v[84:87], a[212:215], a[8:11], v[84:87]\n\t"
    "v_mfma_f32_16x16x32_bf16 v[76:79], a[212:215], a[4:7], v[76:79]\n\t"
    "buffer_load_dwordx4 v244, s[96:99], s35 offen offset:1024 sc1 lds\n\t"
    "v_mfma_f32_16x16x32_bf16 v[68:71], a[212:215], a[0:3], v[68:71]\n\t"
    "s_waitcnt lgkmcnt(12)\n\t"
    "ds_read_b64_tr_b16 a[212:213], v238 offset:49352\n\t"
    "v_mfma_f32_16x16x32_bf16 v[96:99], a[216:219], a[0:3], v[96:99]\n\t"
    "ds_read_b64_tr_b16 a[214:215], v238 offset:50376\n\t"
    "v_readlane_b32 s24, v246, 12\n\t"
    "v_mfma_f32_16x16x32_bf16 v[104:107], a[216:219], a[4:7], v[104:107]\n\t"
    "v_mfma_f32_16x16x32_bf16 v[112:115], a[216:219], a[8:11], v[112:115]\n\t"
    "s_mov_b32 s34, s24\n\t"
    "buffer_load_dwordx4 a[64:67], v240, s[64:67], s34 offen offset:0 sc1\n\t"
    "v_mfma_f32_16x16x32_bf16 v[120:123], a[216:219], a[12:15], v[120:123]\n\t"
    "s_waitcnt lgkmcnt(12)\n\t"
    "ds_read_b64_tr_b16 a[216:217], v239 offset:49344\n\t"
    "v_mfma_f32_16x16x32_bf16 v[124:127], a[220:223], a[12:15], v[124:127]\n\t"
    "ds_read_b64_tr_b16 a[218:219], v239 offset:50368\n\t"
    "v_mfma_f32_16x16x32_bf16 v[116:119], a[220:223], a[8:11], v[116:119]\n\t"
    "v_mfma_f32_16x16x32_bf16 v[108:111], a[220:223], a[4:7], v[108:111]\n\t"
    "buffer_load_dwordx4 a[68:71], v240, s[64:67], s34 offen offset:1024 sc1\n\t"
    "v_mfma_f32_16x16x32_bf16 v[100:103], a[220:223], a[0:3], v[100:103]\n\t"
    "s_waitcnt vmcnt(18) lgkmcnt(12)\n\t"
    "ds_read_b64_tr_b16 a[220:221], v239 offset:49352\n\t"
    "v_mfma_f32_16x16x32_bf16 v[0:3], a[192:195], a[16:19], v[0:3]\n\t"
    "ds_read_b64_tr_b16 a[222:223], v239 offset:50376\n\t"
    "v_mfma_f32_16x16x32_bf16 v[8:11], a[192:195], a[20:23], v[8:11]\n\t"
    "v_lshrrev_b32 v177, 2, v245\n\t"
    "v_mfma_f32_16x16x32_bf16 v[16:19], a[192:195], a[24:27], v[16:19]\n\t"
    "buffer_load_dwordx4 a[72:75], v240, s[64:67], s34 offen offset:2048 sc1\n\t"
    "v_mfma_f32_16x16x32_bf16 v[24:27], a[192:195], a[28:31], v[24:27]\n\t"
    "s_waitcnt lgkmcnt(12)\n\t"
    "ds_read_b64_tr_b16 a[192:193], v232 offset:0\n\t"
    "v_mfma_f32_16x16x32_bf16 v[28:31], a[196:199], a[28:31], v[28:31]\n\t"
    "ds_read_b64_tr_b16 a[194:195], v232 offset:1024\n\t"
    "v_and_b32 v178, 3, v245\n\t"
    "v_mfma_f32_16x16x32_bf16 v[20:23], a[196:199], a[24:27], v[20:23]\n\t"
    "v_mfma_f32_16x16x32_bf16 v[12:15], a[196:199], a[20:23], v[12:15]\n\t"
    "buffer_load_dwordx4 a[76:79], v240, s[64:67], s34 offen offset:3072 sc1\n\t"
    "v_lshl_add_u32 v176, v177, 4, v178\n\t"
    "v_mfma_f32_16x16x32_bf16 v[4:7], a[196:199], a[16:19], v[4:7]\n\t"
    "s_waitcnt lgkmcnt(12)\n\t"
    "ds_read_b64_tr_b16 a[196:197], v232 offset:8\n\t"
    "v_mfma_f32_16x16x32_bf16 v[32:35], a[200:203], a[16:19], v[32:35]\n\t"
    "ds_read_b64_tr_b16 a[198:199], v232 offset:1032\n\t"
    "v_readlane_b32 s24, v246, 13\n\t"
    "v_mfma_f32_16x16x32_bf16 v[40:43], a[200:203], a[20:23], v[40:43]\n\t"
    "s_lshl_b32 s38, s22, 8\n\t"
    "v_mfma_f32_16x16x32_bf16 v[48:51], a[200:203], a[24:27], v[48:51]\n\t"
    "s_mov_b32 s34, s24\n\t"
    "buffer_load_dwordx4 a[80:83], v240, s[64:67], s34 offen offset:0 sc1\n\t"
    "v_mfma_f32_16x16x32_bf16 v[56:59], a[200:203], a[28:31], v[56:59]\n\t"
    "s_waitcnt lgkmcnt(12)\n\t"
    "ds_read_b64_tr_b16 a[200:201], v233 offset:0\n\t"
    "v_mfma_f32_16x16x32_bf16 v[60:63], a[204:207], a[28:31], v[60:63]\n\t"
    "s_add_u32 s38, s38, 114688\n\t"
    "ds_read_b64_tr_b16 a[202:203], v233 offset:1024\n\t"
    "v_mfma_f32_16x16x32_bf16 v[52:55], a[204:207], a[24:27], v[52:55]\n\t"
    "v_lshl_add_u32 v176, v176, 6, s38\n\t"
    "v_mfma_f32_16x16x32_bf16 v[44:47], a[204:207], a[20:23], v[44:47]\n\t"
    "buffer_load_dwordx4 a[84:87], v240, s[64:67], s34 offen offset:1024 sc1\n\t"
    "v_mfma_f32_16x16x32_bf16 v[36:39], a[204:207], a[16:19], v[36:39]\n\t"
    "s_waitcnt lgkmcnt(12)\n\t"
    "ds_read_b64_tr_b16 a[204:205], v233 offset:8\n\t"
    "v_mfma_f32_16x16x32_bf16 v[64:67], a[208:211], a[16:19], v[64:67]\n\t"
    "ds_read_b64_tr_b16 a[206:207], v233 offset:1032\n\t"
    "v_lshlrev_b32 v179, 4, v178\n\t"
    "v_mfma_f32_16x16x32_bf16 v[72:75], a[208:211], a[20:23], v[72:75]\n\t"
    "v_mfma_f32_16x16x32_bf16 v[80:83], a[208:211], a[24:27], v[80:83]\n\t"
    "buffer_load_dwordx4 a[88:91], v240, s[64:67], s34 offen offset:2048 sc1\n\t"
    "v_and_b32 v184, 15, v177\n\t"
    "v_mfma_f32_16x16x32_bf16 v[88:91], a[208:211], a[28:31], v[88:91]\n\t"
    "s_waitcnt lgkmcnt(12)\n\t"
    "ds_read_b64_tr_b16 a[208:209], v234 offset:0\n\t"
    "v_mfma_f32_16x16x32_bf16 v[92:95], a[212:215], a[28:31], v[92:95]\n\t"
    "ds_read_b64_tr_b16 a[210:211], v234 offset:1024\n\t"
    "v_mfma_f32_16x16x32_bf16 v[84:87], a[212:215], a[24:27], v[84:87]\n\t"
    "v_lshl_or_b32 v179, v184, 18, v179\n\t"
    "v_mfma_f32_16x16x32_bf16 v[76:79], a[212:215], a[20:23], v[76:79]\n\t"
    "buffer_load_dwordx4 a[92:95], v240, s[64:67], s34 offen offset:3072 sc1\n\t"
    "v_mfma_f32_16x16x32_bf16 v[68:71], a[212:215], a[16:19], v[68:71]\n\t"
    "s_waitcnt lgkmcnt(12)\n\t"
    "ds_read_b64_tr_b16 a[212:213], v234 offset:8\n\t"
    "v_mfma_f32_16x16x32_bf16 v[96:99], a[216:219], a[16:19], v[96:99]\n\t"
    "ds_read_b64_tr_b16 a[214:215], v234 offset:1032\n\t"
    "v_readlane_b32 s24, v246, 47\n\t"
    "v_mfma_f32_16x16x32_bf16 v[104:107], a[216:219], a[20:23], v[104:107]\n\t"
    "s_add_u32 m0, s31, 41120\n\t"
    "v_lshrrev_b32 v184, 5, v245\n\t"
    "v_mfma_f32_16x16x32_bf16 v[112:115], a[216:219], a[24:27], v[112:115]\n\t"
    "s_lshl_b32 s35, s24, 4\n\t"
    "buffer_load_dwordx4 v243, s[96:99], s35 offen sc1 lds\n\t"
    "v_mfma_f32_16x16x32_bf16 v[120:123], a[216:219], a[28:31], v[120:123]\n\t"
    "s_waitcnt lgkmcnt(12)\n\t"
    "ds_read_b64_tr_b16 a[216:217], v235 offset:0\n\t"
    "v_mfma_f32_16x16x32_bf16 v[124:127], a[220:223], a[28:31], v[124:127]\n\t"
    "ds_read_b64_tr_b16 a[218:219], v235 offset:1024\n\t"
    "v_lshl_or_b32 v179, v184, 13, v179\n\t"
    "v_mfma_f32_16x16x32_bf16 v[116:119], a[220:223], a[24:27], v[116:119]\n\t"
    "v_mfma_f32_16x16x32_bf16 v[108:111], a[220:223], a[20:23], v[108:111]\n\t"
    "buffer_load_dwordx4 v244, s[96:99], s35 offen offset:1024 sc1 lds\n\t"
    "v_mfma_f32_16x16x32_bf16 v[100:103], a[220:223], a[16:19], v[100:103]\n\t"
    "s_waitcnt vmcnt(18) lgkmcnt(12)\n\t"
    "ds_read_b64_tr_b16 a[220:221], v235 offset:8\n\t"
    "v_mfma_f32_16x16x32_bf16 v[0:3], a[192:195], a[32:35], v[0:3]\n\t"
    "s_barrier\n\t"
    "v_readlane_b32 s24, v246, 48\n\t"
    "v_mfma_f32_16x16x32_bf16 v[8:11], a[192:195], a[36:39], v[8:11]\n\t"
    "s_add_u32 m0, s31, 49344\n\t"
    "ds_read_b64_tr_b16 a[222:223], v235 offset:1032\n\t"
    "v_mfma_f32_16x16x32_bf16 v[16:19], a[192:195], a[40:43], v[16:19]\n\t"
    "s_lshl_b32 s35, s24, 4\n\t"
    "buffer_load_dwordx4 v243, s[96:99], s35 offen sc1 lds\n\t"
    "v_mfma_f32_16x16x32_bf16 v[24:27], a[192:195], a[44:47], v[24:27]\n\t"
    "s_waitcnt lgkmcnt(12)\n\t"
    "ds_read_b64_tr_b16 a[192:193], v232 offset:8224\n\t"
    "v_mfma_f32_16x16x32_bf16 v[28:31], a[196:199], a[44:47], v[28:31]\n\t"
    "ds_read_b64_tr_b16 a[194:195], v232 offset:9248\n\t"
    "v_xor_b32 v179, s30, v179\n\t"
    "v_mfma_f32_16x16x32_bf16 v[20:23], a[196:199], a[40:43], v[20:23]\n\t"
    "s_lshl_b32 s38, s22, 16\n\t"
    "v_mfma_f32_16x16x32_bf16 v[12:15], a[196:199], a[36:39], v[12:15]\n\t"
    "s_and_b32 s39, s28, 127\n\t"
    "buffer_load_dwordx4 v244, s[96:99], s35 offen offset:1024 sc1 lds\n\t"
    "v_mfma_f32_16x16x32_bf16 v[4:7], a[196:199], a[32:35], v[4:7]\n\t"
    "s_waitcnt lgkmcnt(12)\n\t"
    "ds_read_b64_tr_b16 a[196:197], v232 offset:8232\n\t"
    "v_mfma_f32_16x16x32_bf16 v[32:35], a[200:203], a[32:35], v[32:35]\n\t"
    "ds_read_b64_tr_b16 a[198:199], v232 offset:9256\n\t"
    "v_readlane_b32 s24, v246, 14\n\t"
    "v_mfma_f32_16x16x32_bf16 v[40:43], a[200:203], a[36:39], v[40:43]\n\t"
    "s_lshl_b32 s39, s39, 6\n\t"
    "v_mfma_f32_16x16x32_bf16 v[48:51], a[200:203], a[40:43], v[48:51]\n\t"
    "s_mov_b32 s34, s24\n\t"
    "buffer_load_dwordx4 a[96:99], v240, s[64:67], s34 offen offset:0 sc1\n\t"
    "v_mfma_f32_16x16x32_bf16 v[56:59], a[200:203], a[44:47], v[56:59]\n\t"
    "s_waitcnt lgkmcnt(12)\n\t"
    "ds_read_b64_tr_b16 a[200:201], v233 offset:8224\n\t"
    "v_mfma_f32_16x16x32_bf16 v[60:63], a[204:207], a[44:47], v[60:63]\n\t"
    "s_add_u32 s38, s38, s39\n\t"
    "ds_read_b64_tr_b16 a[202:203], v233 offset:9248\n\t"
    "v_mfma_f32_16x16x32_bf16 v[52:55], a[204:207], a[40:43], v[52:55]\n\t"
    "s_add_u32 s38, s38, 0x1000000\n\t"
    "s_add_u32 s42, s20, s38\n\t"
    "s_addc_u32 s43, s21, 0\n\t"
    "v_mfma_f32_16x16x32_bf16 v[44:47], a[204:207], a[36:39], v[44:47]\n\t"
    "buffer_load_dwordx4 a[100:103], v240, s[64:67], s34 offen offset:1024 sc1\n\t"
    "v_mfma_f32_16x16x32_bf16 v[36:39], a[204:207], a[32:35], v[36:39]\n\t"
    "s_waitcnt lgkmcnt(12)\n\t"
    "ds_read_b64_tr_b16 a[204:205], v233 offset:8232\n\t"
    "v_mfma_f32_16x16x32_bf16 v[64:67], a[208:211], a[32:35], v[64:67]\n\t"
    "ds_read_b64_tr_b16 a[206:207], v233 offset:9256\n\t"
    "v_lshlrev_b32 v177, 5, v177\n\t"
    "v_mfma_f32_16x16x32_bf16 v[72:75], a[208:211], a[36:39], v[72:75]\n\t"
    "v_mfma_f32_16x16x32_bf16 v[80:83], a[208:211], a[40:43], v[80:83]\n\t"
    "buffer_load_dwordx4 a[104:107], v240, s[64:67], s34 offen offset:2048 sc1\n\t"
    "v_and_or_b32 v177, v178, 1, v177\n\t"
    "v_mfma_f32_16x16x32_bf16 v[88:91], a[208:211], a[44:47], v[88:91]\n\t"
    "s_waitcnt lgkmcnt(12)\n\t"
    "ds_read_b64_tr_b16 a[208:209], v234 offset:8224\n\t"
    "v_mfma_f32_16x16x32_bf16 v[92:95], a[212:215], a[44:47], v[92:95]\n\t"
    "ds_read_b64_tr_b16 a[210:211], v234 offset:9248\n\t"
    "v_mfma_f32_16x16x32_bf16 v[84:87], a[212:215], a[40:43], v[84:87]\n\t"
    "s_lshl_b32 s38, s22, 3\n\t"
    "v_mfma_f32_16x16x32_bf16 v[76:79], a[212:215], a[36:39], v[76:79]\n\t"
    "buffer_load_dwordx4 a[108:111], v240, s[64:67], s34 offen offset:3072 sc1\n\t"
    "v_mfma_f32_16x16x32_bf16 v[68:71], a[212:215], a[32:35], v[68:71]\n\t"
    "s_waitcnt lgkmcnt(12)\n\t"
    "ds_read_b64_tr_b16 a[212:213], v234 offset:8232\n\t"
    "v_mfma_f32_16x16x32_bf16 v[96:99], a[216:219], a[32:35], v[96:99]\n\t"
    "ds_read_b64_tr_b16 a[214:215], v234 offset:9256\n\t"
    "v_readlane_b32 s24, v246, 15\n\t"
    "v_mfma_f32_16x16x32_bf16 v[104:107], a[216:219], a[36:39], v[104:107]\n\t"
    "v_add_u32 v177, s38, v177\n\t"
    "v_mfma_f32_16x16x32_bf16 v[112:115], a[216:219], a[40:43], v[112:115]\n\t"
    "s_mov_b32 s34, s24\n\t"
    "buffer_load_dwordx4 a[112:115], v240, s[64:67], s34 offen offset:0 sc1\n\t"
    "v_mfma_f32_16x16x32_bf16 v[120:123], a[216:219], a[44:47], v[120:123]\n\t"
    "s_waitcnt lgkmcnt(12)\n\t"
    "ds_read_b64_tr_b16 a[216:217], v235 offset:8224\n\t"
    "v_mfma_f32_16x16x32_bf16 v[124:127], a[220:223], a[44:47], v[124:127]\n\t"
    "ds_read_b64_tr_b16 a[218:219], v235 offset:9248\n\t"
    "v_cmp_lt_u32 s[94:95], 1, v178\n\t"
    "v_mfma_f32_16x16x32_bf16 v[116:119], a[220:223], a[40:43], v[116:119]\n\t"
    "v_mfma_f32_16x16x32_bf16 v[108:111], a[220:223], a[36:39], v[108:111]\n\t"
    "buffer_load_dwordx4 a[116:119], v240, s[64:67], s34 offen offset:1024 sc1\n\t"
    "v_mfma_f32_16x16x32_bf16 v[100:103], a[220:223], a[32:35], v[100:103]\n\t"
    "s_waitcnt vmcnt(22) lgkmcnt(12)\n\t"
    "ds_read_b64_tr_b16 a[220:221], v235 offset:8232\n\t"
    "v_mfma_f32_16x16x32_bf16 v[0:3], a[192:195], a[48:51], v[0:3]\n\t"
    "ds_read_b64_tr_b16 a[222:223], v235 offset:9256\n\t"
    "v_mfma_f32_16x16x32_bf16 v[8:11], a[192:195], a[52:55], v[8:11]\n\t"
    "s_mov_b32 s40, 115200\n\t"
    "v_mfma_f32_16x16x32_bf16 v[16:19], a[192:195], a[56:59], v[16:19]\n\t"
    "buffer_load_dwordx4 a[120:123], v240, s[64:67], s34 offen offset:2048 sc1\n\t"
    "v_mfma_f32_16x16x32_bf16 v[24:27], a[192:195], a[60:63], v[24:27]\n\t"
    "s_waitcnt lgkmcnt(12)\n\t"
    "ds_read_b64_tr_b16 a[192:193], v232 offset:16448\n\t"
    "v_mfma_f32_16x16x32_bf16 v[28:31], a[196:199], a[60:63], v[28:31]\n\t"
    "ds_read_b64_tr_b16 a[194:195], v232 offset:17472\n\t"
    "v_add_u32 v180, 0, v177\n\t"
    "v_mfma_f32_16x16x32_bf16 v[20:23], a[196:199], a[56:59], v[20:23]\n\t"
    "v_mfma_f32_16x16x32_bf16 v[12:15], a[196:199], a[52:55], v[12:15]\n\t"
    "buffer_load_dwordx4 a[124:127], v240, s[64:67], s34 offen offset:3072 sc1\n\t"
    "v_sub_u32 v184, 1024, v180\n\t"
    "v_mfma_f32_16x16x32_bf16 v[4:7], a[196:199], a[48:51], v[4:7]\n\t"
    "s_waitcnt lgkmcnt(12)\n\t"
    "ds_read_b64_tr_b16 a[196:197], v232 offset:16456\n\t"
    "v_mfma_f32_16x16x32_bf16 v[32:35], a[200:203], a[48:51], v[32:35]\n\t"
    "ds_read_b64_tr_b16 a[198:199], v232 offset:17480\n\t"
    "v_readlane_b32 s24, v246, 49\n\t"
    "v_mfma_f32_16x16x32_bf16 v[40:43], a[200:203], a[52:55], v[40:43]\n\t"
    "s_add_u32 m0, s31, 57568\n\t"
    "v_lshrrev_b32 v178, 10, v184\n\t"
    "v_mfma_f32_16x16x32_bf16 v[48:51], a[200:203], a[56:59], v[48:51]\n\t"
    "s_lshl_b32 s35, s24, 4\n\t"
    "buffer_load_dwordx4 v243, s[96:99], s35 offen sc1 lds\n\t"
    "v_mfma_f32_16x16x32_bf16 v[56:59], a[200:203], a[60:63], v[56:59]\n\t"
    "s_waitcnt lgkmcnt(12)\n\t"
    "ds_read_b64_tr_b16 a[200:201], v233 offset:16448\n\t"
    "v_mfma_f32_16x16x32_bf16 v[60:63], a[204:207], a[60:63], v[60:63]\n\t"
    "ds_read_b64_tr_b16 a[202:203], v233 offset:17472\n\t"
    "v_lshlrev_b32 v178, 9, v178\n\t"
    "v_mfma_f32_16x16x32_bf16 v[52:55], a[204:207], a[56:59], v[52:55]\n\t"
    "v_sub_u32 v184, v184, v178\n\t"
    "v_mfma_f32_16x16x32_bf16 v[44:47], a[204:207], a[52:55], v[44:47]\n\t"
    "buffer_load_dwordx4 v244, s[96:99], s35 offen offset:1024 sc1 lds\n\t"
    "v_mfma_f32_16x16x32_bf16 v[36:39], a[204:207], a[48:51], v[36:39]\n\t"
    "s_waitcnt lgkmcnt(12)\n\t"
    "ds_read_b64_tr_b16 a[204:205], v233 offset:16456\n\t"
    "v_mfma_f32_16x16x32_bf16 v[64:67], a[208:211], a[48:51], v[64:67]\n\t"
    "ds_read_b64_tr_b16 a[206:207], v233 offset:17480\n\t"
    "v_readlane_b32 s24, v246, 50\n\t"
    "v_mfma_f32_16x16x32_bf16 v[72:75], a[208:211], a[52:55], v[72:75]\n\t"
    "s_add_u32 m0, s31, 65792\n\t"
    "v_cndmask_b32 v180, v180, v184, s[94:95]\n\t"
    "v_mfma_f32_16x16x32_bf16 v[80:83], a[208:211], a[56:59], v[80:83]\n\t"
    "s_lshl_b32 s35, s24, 4\n\t"
    "buffer_load_dwordx4 v243, s[96:99], s35 offen sc1 lds\n\t"
    "v_mfma_f32_16x16x32_bf16 v[88:91], a[208:211], a[60:63], v[88:91]\n\t"
    "s_waitcnt lgkmcnt(12)\n\t"
    "ds_read_b64_tr_b16 a[208:209], v234 offset:16448\n\t"
    "v_mfma_f32_16x16x32_bf16 v[92:95], a[212:215], a[60:63], v[92:95]\n\t"
    "ds_read_b64_tr_b16 a[210:211], v234 offset:17472\n\t"
    "v_and_b32 v184, 31, v180\n\t"
    "v_mfma_f32_16x16x32_bf16 v[84:87], a[212:215], a[56:59], v[84:87]\n\t"
    "v_lshrrev_b32 v180, 5, v180\n\t"
    "v_mfma_f32_16x16x32_bf16 v[76:79], a[212:215], a[52:55], v[76:79]\n\t"
    "buffer_load_dwordx4 v244, s[96:99], s35 offen offset:1024 sc1 lds\n\t"
    "v_mfma_f32_16x16x32_bf16 v[68:71], a[212:215], a[48:51], v[68:71]\n\t"
    "s_waitcnt lgkmcnt(12)\n\t"
    "ds_read_b64_tr_b16 a[212:213], v234 offset:16456\n\t"
    "v_mfma_f32_16x16x32_bf16 v[96:99], a[216:219], a[48:51], v[96:99]\n\t"
    "ds_read_b64_tr_b16 a[214:215], v234 offset:17480\n\t"
    "v_readlane_b32 s24, v246, 16\n\t"
    "v_mfma_f32_16x16x32_bf16 v[104:107], a[216:219], a[52:55], v[104:107]\n\t"
    "v_mad_u32_u24 v180, v184, 36, v180\n\t"
    "v_mfma_f32_16x16x32_bf16 v[112:115], a[216:219], a[56:59], v[112:115]\n\t"
    "s_mov_b32 s34, s24\n\t"
    "buffer_load_dwordx4 a[0:3], v240, s[64:67], s34 offen offset:0 sc1\n\t"
    "v_mfma_f32_16x16x32_bf16 v[120:123], a[216:219], a[60:63], v[120:123]\n\t"
    "s_waitcnt lgkmcnt(12)\n\t"
    "ds_read_b64_tr_b16 a[216:217], v235 offset:16448\n\t"
    "v_mfma_f32_16x16x32_bf16 v[124:127], a[220:223], a[60:63], v[124:127]\n\t"
    "ds_read_b64_tr_b16 a[218:219], v235 offset:17472\n\t"
    "v_lshl_add_u32 v180, v180, 2, s40\n\t"
    "v_mfma_f32_16x16x32_bf16 v[116:119], a[220:223], a[56:59], v[116:119]\n\t"
    "v_mfma_f32_16x16x32_bf16 v[108:111], a[220:223], a[52:55], v[108:111]\n\t"
    "buffer_load_dwordx4 a[4:7], v240, s[64:67], s34 offen offset:1024 sc1\n\t"
    "v_mfma_f32_16x16x32_bf16 v[100:103], a[220:223], a[48:51], v[100:103]\n\t"
    "s_waitcnt vmcnt(22) lgkmcnt(12)\n\t"
    "ds_read_b64_tr_b16 a[220:221], v235 offset:16456\n\t"
    "v_mfma_f32_16x16x32_bf16 v[0:3], a[192:195], a[64:67], v[0:3]\n\t"
    "s_barrier\n\t"
    "v_mfma_f32_16x16x32_bf16 v[8:11], a[192:195], a[68:71], v[8:11]\n\t"
    "ds_read_b64_tr_b16 a[222:223], v235 offset:17480\n\t"
    "v_add_u32 v181, 2, v177\n\t"
    "v_mfma_f32_16x16x32_bf16 v[16:19], a[192:195], a[72:75], v[16:19]\n\t"
    "buffer_load_dwordx4 a[8:11], v240, s[64:67], s34 offen offset:2048 sc1\n\t"
    "v_mfma_f32_16x16x32_bf16 v[24:27], a[192:195], a[76:79], v[24:27]\n\t"
    "s_waitcnt lgkmcnt(12)\n\t"
    "ds_read_b64_tr_b16 a[192:193], v232 offset:24672\n\t"
    "v_mfma_f32_16x16x32_bf16 v[28:31], a[196:199], a[76:79], v[28:31]\n\t"
    "ds_read_b64_tr_b16 a[194:195], v232 offset:25696\n\t"
    "v_sub_u32 v184, 1024, v181\n\t"
    "v_mfma_f32_16x16x32_bf16 v[20:23], a[196:199], a[72:75], v[20:23]\n\t"
    "v_mfma_f32_16x16x32_bf16 v[12:15], a[196:199], a[68:71], v[12:15]\n\t"
    "buffer_load_dwordx4 a[12:15], v240, s[64:67], s34 offen offset:3072 sc1\n\t"
    "v_cndmask_b32 v181, v181, v184, s[94:95]\n\t"
    "v_mfma_f32_16x16x32_bf16 v[4:7], a[196:199], a[64:67], v[4:7]\n\t"
    "s_waitcnt lgkmcnt(12)\n\t"
    "ds_read_b64_tr_b16 a[196:197], v232 offset:24680\n\t"
    "v_mfma_f32_16x16x32_bf16 v[32:35], a[200:203], a[64:67], v[32:35]\n\t"
    "ds_read_b64_tr_b16 a[198:199], v232 offset:25704\n\t"
    "v_readlane_b32 s24, v246, 17\n\t"
    "v_mfma_f32_16x16x32_bf16 v[40:43], a[200:203], a[68:71], v[40:43]\n\t"
    "v_and_b32 v184, 31, v181\n\t"
    "v_mfma_f32_16x16x32_bf16 v[48:51], a[200:203], a[72:75], v[48:51]\n\t"
    "s_mov_b32 s34, s24\n\t"
    "buffer_load_dwordx4 a[16:19], v240, s[64:67], s34 offen offset:0 sc1\n\t"
    "v_mfma_f32_16x16x32_bf16 v[56:59], a[200:203], a[76:79], v[56:59]\n\t"
    "s_waitcnt lgkmcnt(12)\n\t"
    "ds_read_b64_tr_b16 a[200:201], v233 offset:24672\n\t"
    "v_mfma_f32_16x16x32_bf16 v[60:63], a[204:207], a[76:79], v[60:63]\n\t"
    "ds_read_b64_tr_b16 a[202:203], v233 offset:25696\n\t"
    "v_lshrrev_b32 v181, 5, v181\n\t"
    "v_mfma_f32_16x16x32_bf16 v[52:55], a[204:207], a[72:75], v[52:55]\n\t"
    "v_mad_u32_u24 v181, v184, 36, v181\n\t"
    "v_mfma_f32_16x16x32_bf16 v[44:47], a[204:207], a[68:71], v[44:47]\n\t"
    "buffer_load_dwordx4 a[20:23], v240, s[64:67], s34 offen offset:1024 sc1\n\t"
    "v_mfma_f32_16x16x32_bf16 v[36:39], a[204:207], a[64:67], v[36:39]\n\t"
    "s_waitcnt lgkmcnt(12)\n\t"
    "ds_read_b64_tr_b16 a[204:205], v233 offset:24680\n\t"
    "v_mfma_f32_16x16x32_bf16 v[64:67], a[208:211], a[64:67], v[64:67]\n\t"
    "ds_read_b64_tr_b16 a[206:207], v233 offset:25704\n\t"
    "v_lshl_add_u32 v181, v181, 2, s40\n\t"
    "v_mfma_f32_16x16x32_bf16 v[72:75], a[208:211], a[68:71], v[72:75]\n\t"
    "v_mfma_f32_16x16x32_bf16 v[80:83], a[208:211], a[72:75], v[80:83]\n\t"
    "buffer_load_dwordx4 a[24:27], v240, s[64:67], s34 offen offset:2048 sc1\n\t"
    "v_add_u32 v182, 4, v177\n\t"
    "v_mfma_f32_16x16x32_bf16 v[88:91], a[208:211], a[76:79], v[88:91]\n\t"
    "s_waitcnt lgkmcnt(12)\n\t"
    "ds_read_b64_tr_b16 a[208:209], v234 offset:24672\n\t"
    "v_mfma_f32_16x16x32_bf16 v[92:95], a[212:215], a[76:79], v[92:95]\n\t"
    "ds_read_b64_tr_b16 a[210:211], v234 offset:25696\n\t"
    "v_mfma_f32_16x16x32_bf16 v[84:87], a[212:215], a[72:75], v[84:87]\n\t"
    "v_sub_u32 v184, 1024, v182\n\t"
    "v_mfma_f32_16x16x32_bf16 v[76:79], a[212:215], a[68:71], v[76:79]\n\t"
    "buffer_load_dwordx4 a[28:31], v240, s[64:67], s34 offen offset:3072 sc1\n\t"
    "v_mfma_f32_16x16x32_bf16 v[68:71], a[212:215], a[64:67], v[68:71]\n\t"
    "s_waitcnt lgkmcnt(12)\n\t"
    "ds_read_b64_tr_b16 a[212:213], v234 offset:24680\n\t"
    "v_mfma_f32_16x16x32_bf16 v[96:99], a[216:219], a[64:67], v[96:99]\n\t"
    "ds_read_b64_tr_b16 a[214:215], v234 offset:25704\n\t"
    "v_readlane_b32 s24, v246, 51\n\t"
    "v_mfma_f32_16x16x32_bf16 v[104:107], a[216:219], a[68:71], v[104:107]\n\t"
    "s_add_u32 m0, s31, 74016\n\t"
    "v_cndmask_b32 v182, v182, v184, s[94:95]\n\t"
    "v_mfma_f32_16x16x32_bf16 v[112:115], a[216:219], a[72:75], v[112:115]\n\t"
    "s_lshl_b32 s35, s24, 4\n\t"
    "buffer_load_dwordx4 v243, s[96:99], s35 offen sc1 lds\n\t"
    "v_mfma_f32_16x16x32_bf16 v[120:123], a[216:219], a[76:79], v[120:123]\n\t"
    "s_waitcnt lgkmcnt(12)\n\t"
    "ds_read_b64_tr_b16 a[216:217], v235 offset:24672\n\t"
    "v_mfma_f32_16x16x32_bf16 v[124:127], a[220:223], a[76:79], v[124:127]\n\t"
    "ds_read_b64_tr_b16 a[218:219], v235 offset:25696\n\t"
    "v_and_b32 v184, 31, v182\n\t"
    "v_mfma_f32_16x16x32_bf16 v[116:119], a[220:223], a[72:75], v[116:119]\n\t"
    "v_mfma_f32_16x16x32_bf16 v[108:111], a[220:223], a[68:71], v[108:111]\n\t"
    "buffer_load_dwordx4 v244, s[96:99], s35 offen offset:1024 sc1 lds\n\t"
    "v_mfma_f32_16x16x32_bf16 v[100:103], a[220:223], a[64:67], v[100:103]\n\t"
    "s_waitcnt vmcnt(26) lgkmcnt(12)\n\t"
    "ds_read_b64_tr_b16 a[220:221], v235 offset:24680\n\t"
    "v_mfma_f32_16x16x32_bf16 v[0:3], a[192:195], a[80:83], v[0:3]\n\t"
    "ds_read_b64_tr_b16 a[222:223], v235 offset:25704\n\t"
    "v_readlane_b32 s24, v246, 52\n\t"
    "v_mfma_f32_16x16x32_bf16 v[8:11], a[192:195], a[84:87], v[8:11]\n\t"
    "s_add_u32 m0, s31, 82240\n\t"
    "v_lshrrev_b32 v182, 5, v182\n\t"
    "v_mfma_f32_16x16x32_bf16 v[16:19], a[192:195], a[88:91], v[16:19]\n\t"
    "s_lshl_b32 s35, s24, 4\n\t"
    "buffer_load_dwordx4 v243, s[96:99], s35 offen sc1 lds\n\t"
    "v_mfma_f32_16x16x32_bf16 v[24:27], a[192:195], a[92:95], v[24:27]\n\t"
    "s_waitcnt lgkmcnt(12)\n\t"
    "ds_read_b64_tr_b16 a[192:193], v232 offset:32896\n\t"
    "v_mfma_f32_16x16x32_bf16 v[28:31], a[196:199], a[92:95], v[28:31]\n\t"
    "ds_read_b64_tr_b16 a[194:195], v232 offset:33920\n\t"
    "v_mad_u32_u24 v182, v184, 36, v182\n\t"
    "v_mfma_f32_16x16x32_bf16 v[20:23], a[196:199], a[88:91], v[20:23]\n\t"
    "v_mfma_f32_16x16x32_bf16 v[12:15], a[196:199], a[84:87], v[12:15]\n\t"
    "buffer_load_dwordx4 v244, s[96:99], s35 offen offset:1024 sc1 lds\n\t"
    "v_lshl_add_u32 v182, v182, 2, s40\n\t"
    "v_mfma_f32_16x16x32_bf16 v[4:7], a[196:199], a[80:83], v[4:7]\n\t"
    "s_waitcnt lgkmcnt(12)\n\t"
    "ds_read_b64_tr_b16 a[196:197], v232 offset:32904\n\t"
    "v_mfma_f32_16x16x32_bf16 v[32:35], a[200:203], a[80:83], v[32:35]\n\t"
    "ds_read_b64_tr_b16 a[198:199], v232 offset:33928\n\t"
    "v_readlane_b32 s24, v246, 18\n\t"
    "v_mfma_f32_16x16x32_bf16 v[40:43], a[200:203], a[84:87], v[40:43]\n\t"
    "v_add_u32 v183, 6, v177\n\t"
    "v_mfma_f32_16x16x32_bf16 v[48:51], a[200:203], a[88:91], v[48:51]\n\t"
    "s_mov_b32 s34, s24\n\t"
    "buffer_load_dwordx4 a[32:35], v240, s[64:67], s34 offen offset:0 sc1\n\t"
    "v_mfma_f32_16x16x32_bf16 v[56:59], a[200:203], a[92:95], v[56:59]\n\t"
    "s_waitcnt lgkmcnt(12)\n\t"
    "ds_read_b64_tr_b16 a[200:201], v233 offset:32896\n\t"
    "v_mfma_f32_16x16x32_bf16 v[60:63], a[204:207], a[92:95], v[60:63]\n\t"
    "ds_read_b64_tr_b16 a[202:203], v233 offset:33920\n\t"
    "v_sub_u32 v184, 1024, v183\n\t"
    "v_mfma_f32_16x16x32_bf16 v[52:55], a[204:207], a[88:91], v[52:55]\n\t"
    "v_cndmask_b32 v183, v183, v184, s[94:95]\n\t"
    "v_mfma_f32_16x16x32_bf16 v[44:47], a[204:207], a[84:87], v[44:47]\n\t"
    "buffer_load_dwordx4 a[36:39], v240, s[64:67], s34 offen offset:1024 sc1\n\t"
    "v_mfma_f32_16x16x32_bf16 v[36:39], a[204:207], a[80:83], v[36:39]\n\t"
    "s_waitcnt lgkmcnt(12)\n\t"
    "ds_read_b64_tr_b16 a[204:205], v233 offset:32904\n\t"
    "v_mfma_f32_16x16x32_bf16 v[64:67], a[208:211], a[80:83], v[64:67]\n\t"
    "ds_read_b64_tr_b16 a[206:207], v233 offset:33928\n\t"
    "v_and_b32 v184, 31, v183\n\t"
    "v_mfma_f32_16x16x32_bf16 v[72:75], a[208:211], a[84:87], v[72:75]\n\t"
    "v_mfma_f32_16x16x32_bf16 v[80:83], a[208:211], a[88:91], v[80:83]\n\t"
    "buffer_load_dwordx4 a[40:43], v240, s[64:67], s34 offen offset:2048 sc1\n\t"
    "v_lshrrev_b32 v183, 5, v183\n\t"
    "v_mfma_f32_16x16x32_bf16 v[88:91], a[208:211], a[92:95], v[88:91]\n\t"
    "s_waitcnt lgkmcnt(12)\n\t"
    "ds_read_b64_tr_b16 a[208:209], v234 offset:32896\n\t"
    "v_mfma_f32_16x16x32_bf16 v[92:95], a[212:215], a[92:95], v[92:95]\n\t"
    "ds_read_b64_tr_b16 a[210:211], v234 offset:33920\n\t"
    "v_mfma_f32_16x16x32_bf16 v[84:87], a[212:215], a[88:91], v[84:87]\n\t"
    "v_mad_u32_u24 v183, v184, 36, v183\n\t"
    "v_mfma_f32_16x16x32_bf16 v[76:79], a[212:215], a[84:87], v[76:79]\n\t"
    "buffer_load_dwordx4 a[44:47], v240, s[64:67], s34 offen offset:3072 sc1\n\t"
    "v_mfma_f32_16x16x32_bf16 v[68:71], a[212:215], a[80:83], v[68:71]\n\t"
    "Lpad9_%=:\n\t"
    "s_cbranch_execz LretB_%=\n\t"
    "s_waitcnt lgkmcnt(12)\n\t"
    "ds_read_b64_tr_b16 a[212:213], v234 offset:32904\n\t"
    "v_mfma_f32_16x16x32_bf16 v[96:99], a[216:219], a[80:83], v[96:99]\n\t"
    "ds_read_b64_tr_b16 a[214:215], v234 offset:33928\n\t"
    "v_readlane_b32 s24, v246, 19\n\t"
    "v_mfma_f32_16x16x32_bf16 v[104:107], a[216:219], a[84:87], v[104:107]\n\t"
    "v_lshl_add_u32 v183, v183, 2, s40\n\t"
    "v_mfma_f32_16x16x32_bf16 v[112:115], a[216:219], a[88:91], v[112:115]\n\t"
    "s_mov_b32 s34, s24\n\t"
    "buffer_load_dwordx4 a[48:51], v240, s[64:67], s34 offen offset:0 sc1\n\t"
    "v_mfma_f32_16x16x32_bf16 v[120:123], a[216:219], a[92:95], v[120:123]\n\t"
    "s_waitcnt lgkmcnt(12)\n\t"
    "ds_read_b64_tr_b16 a[216:217], v235 offset:32896\n\t"
    "v_mfma_f32_16x16x32_bf16 v[124:127], a[220:223], a[92:95], v[124:127]\n\t"
    "ds_read_b64_tr_b16 a[218:219], v235 offset:33920\n\t"
    "v_and_b32 v184, 31, v245\n\t"
    "v_mfma_f32_16x16x32_bf16 v[116:119], a[220:223], a[88:91], v[116:119]\n\t"
    "v_mfma_f32_16x16x32_bf16 v[108:111], a[220:223], a[84:87], v[108:111]\n\t"
    "buffer_load_dwordx4 a[52:55], v240, s[64:67], s34 offen offset:1024 sc1\n\t"
    "v_mfma_f32_16x16x32_bf16 v[100:103], a[220:223], a[80:83], v[100:103]\n\t"
    "s_waitcnt vmcnt(26) lgkmcnt(12)\n\t"
    "ds_read_b64_tr_b16 a[220:221], v235 offset:32904\n\t"
    "v_mfma_f32_16x16x32_bf16 v[0:3], a[192:195], a[96:99], v[0:3]\n\t"
    "s_barrier\n\t"
    "v_mfma_f32_16x16x32_bf16 v[8:11], a[192:195], a[100:103], v[8:11]\n\t"
    "ds_read_b64_tr_b16 a[222:223], v235 offset:33928\n\t"
    "v_lshrrev_b32 v185, 5, v245\n\t"
    "v_mfma_f32_16x16x32_bf16 v[16:19], a[192:195], a[104:107], v[16:19]\n\t"
    "buffer_load_dwordx4 a[56:59], v240, s[64:67], s34 offen offset:2048 sc1\n\t"
    "v_mfma_f32_16x16x32_bf16 v[24:27], a[192:195], a[108:111], v[24:27]\n\t"
    "s_waitcnt lgkmcnt(12)\n\t"
    "ds_read_b64_tr_b16 a[192:193], v232 offset:41120\n\t"
    "v_mfma_f32_16x16x32_bf16 v[28:31], a[196:199], a[108:111], v[28:31]\n\t"
    "ds_read_b64_tr_b16 a[194:195], v232 offset:42144\n\t"
    "v_mul_u32_u24 v184, 144, v184\n\t"
    "v_mfma_f32_16x16x32_bf16 v[20:23], a[196:199], a[104:107], v[20:23]\n\t"
    "v_mfma_f32_16x16x32_bf16 v[12:15], a[196:199], a[100:103], v[12:15]\n\t"
    "buffer_load_dwordx4 a[60:63], v240, s[64:67], s34 offen offset:3072 sc1\n\t"
    "v_lshl_add_u32 v185, v185, 4, v184\n\t"
    "v_mfma_f32_16x16x32_bf16 v[4:7], a[196:199], a[96:99], v[4:7]\n\t"
    "s_waitcnt lgkmcnt(12)\n\t"
    "ds_read_b64_tr_b16 a[196:197], v232 offset:41128\n\t"
    "v_mfma_f32_16x16x32_bf16 v[32:35], a[200:203], a[96:99], v[32:35]\n\t"
    "ds_read_b64_tr_b16 a[198:199], v232 offset:42152\n\t"
    "v_readlane_b32 s24, v246, 53\n\t"
    "v_mfma_f32_16x16x32_bf16 v[40:43], a[200:203], a[100:103], v[40:43]\n\t"
    "s_add_u32 m0, s31, 90464\n\t"
    "s_mul_i32 s38, s22, 4608\n\t"
    "v_mfma_f32_16x16x32_bf16 v[48:51], a[200:203], a[104:107], v[48:51]\n\t"
    "s_lshl_b32 s35, s24, 4\n\t"
    "buffer_load_dwordx4 v243, s[96:99], s35 offen sc1 lds\n\t"
    "v_mfma_f32_16x16x32_bf16 v[56:59], a[200:203], a[108:111], v[56:59]\n\t"
    "s_waitcnt lgkmcnt(12)\n\t"
    "ds_read_b64_tr_b16 a[200:201], v233 offset:41120\n\t"
    "v_mfma_f32_16x16x32_bf16 v[60:63], a[204:207], a[108:111], v[60:63]\n\t"
    "s_add_u32 s38, s38, 115200\n\t"
    "ds_read_b64_tr_b16 a[202:203], v233 offset:42144\n\t"
    "v_mfma_f32_16x16x32_bf16 v[52:55], a[204:207], a[104:107], v[52:55]\n\t"
    "v_add_u32 v184, s38, v185\n\t"
    "v_mfma_f32_16x16x32_bf16 v[44:47], a[204:207], a[100:103], v[44:47]\n\t"
    "buffer_load_dwordx4 v244, s[96:99], s35 offen offset:1024 sc1 lds\n\t"
    "v_mfma_f32_16x16x32_bf16 v[36:39], a[204:207], a[96:99], v[36:39]\n\t"
    "s_waitcnt lgkmcnt(12)\n\t"
    "ds_read_b64_tr_b16 a[204:205], v233 offset:41128\n\t"
    "v_mfma_f32_16x16x32_bf16 v[64:67], a[208:211], a[96:99], v[64:67]\n\t"
    "ds_read_b64_tr_b16 a[206:207], v233 offset:42152\n\t"
    "v_readlane_b32 s24, v246, 54\n\t"
    "v_mfma_f32_16x16x32_bf16 v[72:75], a[208:211], a[100:103], v[72:75]\n\t"
    "s_add_u32 m0, s31, 98688\n\t"
    "s_bfe_u32 s38, s28, 0x40003\n\t"
    "v_mfma_f32_16x16x32_bf16 v[80:83], a[208:211], a[104:107], v[80:83]\n\t"
    "s_lshl_b32 s35, s24, 4\n\t"
    "buffer_load_dwordx4 v243, s[96:99], s35 offen sc1 lds\n\t"
    "v_mfma_f32_16x16x32_bf16 v[88:91], a[208:211], a[108:111], v[88:91]\n\t"
    "s_waitcnt lgkmcnt(12)\n\t"
    "ds_read_b64_tr_b16 a[208:209], v234 offset:41120\n\t"
    "v_mfma_f32_16x16x32_bf16 v[92:95], a[212:215], a[108:111], v[92:95]\n\t"
    "s_lshl_b32 s38, s38, 2\n\t"
    "ds_read_b64_tr_b16 a[210:211], v234 offset:42144\n\t"
    "v_mfma_f32_16x16x32_bf16 v[84:87], a[212:215], a[104:107], v[84:87]\n\t"
    "s_add_u32 s38, s38, s22\n\t"
    "v_mfma_f32_16x16x32_bf16 v[76:79], a[212:215], a[100:103], v[76:79]\n\t"
    "buffer_load_dwordx4 v244, s[96:99], s35 offen offset:1024 sc1 lds\n\t"
    "v_mfma_f32_16x16x32_bf16 v[68:71], a[212:215], a[96:99], v[68:71]\n\t"
    "s_waitcnt lgkmcnt(12)\n\t"
    "ds_read_b64_tr_b16 a[212:213], v234 offset:41128\n\t"
    "v_mfma_f32_16x16x32_bf16 v[96:99], a[216:219], a[96:99], v[96:99]\n\t"
    "ds_read_b64_tr_b16 a[214:215], v234 offset:42152\n\t"
    "v_readlane_b32 s24, v246, 55\n\t"
    "v_mfma_f32_16x16x32_bf16 v[104:107], a[216:219], a[100:103], v[104:107]\n\t"
    "s_add_u32 m0, s31, 106912\n\t"
    "s_lshl_b32 s38, s38, 16\n\t"
    "v_mfma_f32_16x16x32_bf16 v[112:115], a[216:219], a[104:107], v[112:115]\n\t"
    "s_lshl_b32 s35, s24, 4\n\t"
    "buffer_load_dwordx4 v243, s[96:99], s35 offen sc1 lds\n\t"
    "v_mfma_f32_16x16x32_bf16 v[120:123], a[216:219], a[108:111], v[120:123]\n\t"
    "s_waitcnt lgkmcnt(12)\n\t"
    "ds_read_b64_tr_b16 a[216:217], v235 offset:41120\n\t"
    "v_mfma_f32_16x16x32_bf16 v[124:127], a[220:223], a[108:111], v[124:127]\n\t"
    "s_lshr_b32 s39, s38, 8\n\t"
    "ds_read_b64_tr_b16 a[218:219], v235 offset:42144\n\t"
    "v_mfma_f32_16x16x32_bf16 v[116:119], a[220:223], a[104:107], v[116:119]\n\t"
    "v_mfma_f32_16x16x32_bf16 v[108:111], a[220:223], a[100:103], v[108:111]\n\t"
    "buffer_load_dwordx4 v244, s[96:99], s35 offen offset:1024 sc1 lds\n\t"
    "v_mfma_f32_16x16x32_bf16 v[100:103], a[220:223], a[96:99], v[100:103]\n\t"
    "s_waitcnt vmcnt(30) lgkmcnt(12)\n\t"
    "ds_read_b64_tr_b16 a[220:221], v235 offset:41128\n\t"
    "v_mfma_f32_16x16x32_bf16 v[0:3], a[192:195], a[112:115], v[0:3]\n\t"
    "ds_read_b64_tr_b16 a[222:223], v235 offset:42152\n\t"
    "v_readlane_b32 s24, v246, 56\n\t"
    "v_mfma_f32_16x16x32_bf16 v[8:11], a[192:195], a[116:119], v[8:11]\n\t"
    "s_add_u32 m0, s31, 0\n\t"
    "s_xor_b32 s39, s39, s30\n\t"
    "v_mfma_f32_16x16x32_bf16 v[16:19], a[192:195], a[120:123], v[16:19]\n\t"
    "s_lshl_b32 s35, s24, 4\n\t"
    "buffer_load_dwordx4 v243, s[96:99], s35 offen sc1 lds\n\t"
    "v_mfma_f32_16x16x32_bf16 v[24:27], a[192:195], a[124:127], v[24:27]\n\t"
    "s_waitcnt lgkmcnt(12)\n\t"
    "ds_read_b64_tr_b16 a[192:193], v232 offset:49344\n\t"
    "s_and_b32 s39, s39, 0x2000\n\t"
    "v_mfma_f32_16x16x32_bf16 v[28:31], a[196:199], a[124:127], v[28:31]\n\t"
    "ds_read_b64_tr_b16 a[194:195], v232 offset:50368\n\t"
    "v_mfma_f32_16x16x32_bf16 v[20:23], a[196:199], a[120:123], v[20:23]\n\t"
    "v_mfma_f32_16x16x32_bf16 v[12:15], a[196:199], a[116:119], v[12:15]\n\t"
    "s_or_b32 s38, s38, s39\n\t"
    "buffer_load_dwordx4 v244, s[96:99], s35 offen offset:1024 sc1 lds\n\t"
    "v_mfma_f32_16x16x32_bf16 v[4:7], a[196:199], a[112:115], v[4:7]\n\t"
    "s_waitcnt lgkmcnt(12)\n\t"
    "ds_read_b64_tr_b16 a[196:197], v232 offset:49352\n\t"
    "v_mfma_f32_16x16x32_bf16 v[32:35], a[200:203], a[112:115], v[32:35]\n\t"
    "s_and_b32 s39, s28, 7\n\t"
    "ds_read_b64_tr_b16 a[198:199], v232 offset:50376\n\t"
    "v_readlane_b32 s24, v246, 57\n\t"
    "v_mfma_f32_16x16x32_bf16 v[40:43], a[200:203], a[116:119], v[40:43]\n\t"
    "s_add_u32 m0, s31, 8224\n\t"
    "v_mfma_f32_16x16x32_bf16 v[48:51], a[200:203], a[120:123], v[48:51]\n\t"
    "s_lshl_b32 s35, s24, 4\n\t"
    "buffer_load_dwordx4 v243, s[96:99], s35 offen sc1 lds\n\t"
    "v_mfma_f32_16x16x32_bf16 v[56:59], a[200:203], a[124:127], v[56:59]\n\t"
    "s_waitcnt lgkmcnt(12)\n\t"
    "ds_read_b64_tr_b16 a[200:201], v233 offset:49344\n\t"
    "s_lshl_b32 s39, s39, 9\n\t"
    "v_mfma_f32_16x16x32_bf16 v[60:63], a[204:207], a[124:127], v[60:63]\n\t"
    "ds_read_b64_tr_b16 a[202:203], v233 offset:50368\n\t"
    "v_mfma_f32_16x16x32_bf16 v[52:55], a[204:207], a[120:123], v[52:55]\n\t"
    "s_add_u32 s38, s38, s39\n\t"
    "v_mfma_f32_16x16x32_bf16 v[44:47], a[204:207], a[116:119], v[44:47]\n\t"
    "buffer_load_dwordx4 v244, s[96:99], s35 offen offset:1024 sc1 lds\n\t"
    "v_mfma_f32_16x16x32_bf16 v[36:39], a[204:207], a[112:115], v[36:39]\n\t"
    "s_waitcnt lgkmcnt(12)\n\t"
    "ds_read_b64_tr_b16 a[204:205], v233 offset:49352\n\t"
    "v_mfma_f32_16x16x32_bf16 v[64:67], a[208:211], a[112:115], v[64:67]\n\t"
    "ds_read_b64_tr_b16 a[206:207], v233 offset:50376\n\t"
    "v_readlane_b32 s24, v246, 58\n\t"
    "v_mfma_f32_16x16x32_bf16 v[72:75], a[208:211], a[116:119], v[72:75]\n\t"
    "s_add_u32 m0, s31, 16448\n\t"
    "s_add_u32 s38, s38, 0x2401000\n\t"
    "s_add_u32 s50, s20, s38\n\t"
    "s_addc_u32 s51, s21, 0\n\t"
    "v_mfma_f32_16x16x32_bf16 v[80:83], a[208:211], a[120:123], v[80:83]\n\t"
    "s_lshl_b32 s35, s24, 4\n\t"
    "buffer_load_dwordx4 v243, s[96:99], s35 offen sc1 lds\n\t"
    "v_mfma_f32_16x16x32_bf16 v[88:91], a[208:211], a[124:127], v[88:91]\n\t"
    "s_waitcnt lgkmcnt(12)\n\t"
    "ds_read_b64_tr_b16 a[208:209], v234 offset:49344\n\t"
    "v_mfma_f32_16x16x32_bf16 v[92:95], a[212:215], a[124:127], v[92:95]\n\t"
    "s_add_u32 s52, s50, 0x400000\n\t"
    "s_addc_u32 s53, s51, 0\n\t"
    "ds_read_b64_tr_b16 a[210:211], v234 offset:50368\n\t"
    "v_mfma_f32_16x16x32_bf16 v[84:87], a[212:215], a[120:123], v[84:87]\n\t"
    "s_add_u32 s54, s52, 0x400000\n\t"
    "s_addc_u32 s55, s53, 0\n\t"
    "v_mfma_f32_16x16x32_bf16 v[76:79], a[212:215], a[116:119], v[76:79]\n\t"
    "buffer_load_dwordx4 v244, s[96:99], s35 offen offset:1024 sc1 lds\n\t"
    "v_mfma_f32_16x16x32_bf16 v[68:71], a[212:215], a[112:115], v[68:71]\n\t"
    "s_waitcnt lgkmcnt(12)\n\t"
    "ds_read_b64_tr_b16 a[212:213], v234 offset:49352\n\t"
    "v_mfma_f32_16x16x32_bf16 v[96:99], a[216:219], a[112:115], v[96:99]\n\t"
    "ds_read_b64_tr_b16 a[214:215], v234 offset:50376\n\t"
    "v_readlane_b32 s24, v246, 59\n\t"
    "v_mfma_f32_16x16x32_bf16 v[104:107], a[216:219], a[116:119], v[104:107]\n\t"
    "s_add_u32 m0, s31, 24672\n\t"
    "s_add_u32 s56, s54, 0x400000\n\t"
    "s_addc_u32 s57, s55, 0\n\t"
    "v_mfma_f32_16x16x32_bf16 v[112:115], a[216:219], a[120:123], v[112:115]\n\t"
    "s_lshl_b32 s35, s24, 4\n\t"
    "buffer_load_dwordx4 v243, s[96:99], s35 offen sc1 lds\n\t"
    "s_bfe_u32 s38, s28, 0x20005\n\t"
    "v_mfma_f32_16x16x32_bf16 v[120:123], a[216:219], a[124:127], v[120:123]\n\t"
    "s_waitcnt lgkmcnt(12)\n\t"
    "ds_read_b64_tr_b16 a[216:217], v235 offset:49344\n\t"
    "v_mfma_f32_16x16x32_bf16 v[124:127], a[220:223], a[124:127], v[124:127]\n\t"
    "ds_read_b64_tr_b16 a[218:219], v235 offset:50368\n\t"
    "v_mfma_f32_16x16x32_bf16 v[116:119], a[220:223], a[120:123], v[116:119]\n\t"
    "v_mfma_f32_16x16x32_bf16 v[108:111], a[220:223], a[116:119], v[108:111]\n\t"
    "buffer_load_dwordx4 v244, s[96:99], s35 offen offset:1024 sc1 lds\n\t"
    "v_mfma_f32_16x16x32_bf16 v[100:103], a[220:223], a[112:115], v[100:103]\n\t"
    "s_waitcnt vmcnt(30) lgkmcnt(12)\n\t"
    "ds_read_b64_tr_b16 a[220:221], v235 offset:49352\n\t"
    "v_mfma_f32_16x16x32_bf16 v[0:3], a[192:195], a[0:3], v[0:3]\n\t"
    "s_barrier\n\t"
    "v_readlane_b32 s24, v246, 20\n\t"
    "v_mfma_f32_16x16x32_bf16 v[8:11], a[192:195], a[4:7], v[8:11]\n\t"
    "s_lshl_b32 s38, s38, 4\n\t"
    "ds_read_b64_tr_b16 a[222:223], v235 offset:50376\n\t"
    "v_mfma_f32_16x16x32_bf16 v[16:19], a[192:195], a[8:11], v[16:19]\n\t"
    "s_mov_b32 s34, s24\n\t"
    "buffer_load_dwordx4 a[64:67], v240, s[64:67], s34 offen offset:0 sc1\n\t"
    "v_mfma_f32_16x16x32_bf16 v[24:27], a[192:195], a[12:15], v[24:27]\n\t"
    "s_waitcnt lgkmcnt(12)\n\t"
    "ds_read_b64_tr_b16 a[192:193], v236 offset:0\n\t"
    "v_mfma_f32_16x16x32_bf16 v[28:31], a[196:199], a[12:15], v[28:31]\n\t"
    "s_lshr_b32 s39, s28, 7\n\t"
    "ds_read_b64_tr_b16 a[194:195], v236 offset:1024\n\t"
    "v_mfma_f32_16x16x32_bf16 v[20:23], a[196:199], a[8:11], v[20:23]\n\t"
    "v_mfma_f32_16x16x32_bf16 v[12:15], a[196:199], a[4:7], v[12:15]\n\t"
    "s_add_u32 s38, s38, s39\n\t"
    "buffer_load_dwordx4 a[68:71], v240, s[64:67], s34 offen offset:1024 sc1\n\t"
    "v_mfma_f32_16x16x32_bf16 v[4:7], a[196:199], a[0:3], v[4:7]\n\t"
    "s_waitcnt lgkmcnt(12)\n\t"
    "ds_read_b64_tr_b16 a[196:197], v236 offset:8\n\t"
    "v_mfma_f32_16x16x32_bf16 v[32:35], a[200:203], a[0:3], v[32:35]\n\t"
    "s_lshl_b32 s38, s38, 4\n\t"
    "ds_read_b64_tr_b16 a[198:199], v236 offset:1032\n\t"
    "v_mfma_f32_16x16x32_bf16 v[40:43], a[200:203], a[4:7], v[40:43]\n\t"
    "v_mfma_f32_16x16x32_bf16 v[48:51], a[200:203], a[8:11], v[48:51]\n\t"
    "buffer_load_dwordx4 a[72:75], v240, s[64:67], s34 offen offset:2048 sc1\n\t"
    "v_mfma_f32_16x16x32_bf16 v[56:59], a[200:203], a[12:15], v[56:59]\n\t"
    "s_waitcnt lgkmcnt(12)\n\t"
    "ds_read_b64_tr_b16 a[200:201], v237 offset:0\n\t"
    "v_mfma_f32_16x16x32_bf16 v[60:63], a[204:207], a[12:15], v[60:63]\n\t"
    "s_bfe_u32 s39, s28, 0x20003\n\t"
    "ds_read_b64_tr_b16 a[202:203], v237 offset:1024\n\t"
    "v_mfma_f32_16x16x32_bf16 v[52:55], a[204:207], a[8:11], v[52:55]\n\t"
    "s_add_u32 s38, s38, s39\n\t"
    "v_mfma_f32_16x16x32_bf16 v[44:47], a[204:207], a[4:7], v[44:47]\n\t"
    "buffer_load_dwordx4 a[76:79], v240, s[64:67], s34 offen offset:3072 sc1\n\t"
    "v_mfma_f32_16x16x32_bf16 v[36:39], a[204:207], a[0:3], v[36:39]\n\t"
    "s_waitcnt lgkmcnt(12)\n\t"
    "ds_read_b64_tr_b16 a[204:205], v237 offset:8\n\t"
    "v_mfma_f32_16x16x32_bf16 v[64:67], a[208:211], a[0:3], v[64:67]\n\t"
    "ds_read_b64_tr_b16 a[206:207], v237 offset:1032\n\t"
    "v_readlane_b32 s24, v246, 21\n\t"
    "v_mfma_f32_16x16x32_bf16 v[72:75], a[208:211], a[4:7], v[72:75]\n\t"
    "s_lshl_b32 s38, s38, 9\n\t"
    "v_mfma_f32_16x16x32_bf16 v[80:83], a[208:211], a[8:11], v[80:83]\n\t"
    "s_mov_b32 s34, s24\n\t"
    "buffer_load_dwordx4 a[80:83], v240, s[64:67], s34 offen offset:0 sc1\n\t"
    "v_mfma_f32_16x16x32_bf16 v[88:91], a[208:211], a[12:15], v[88:91]\n\t"
    "s_waitcnt lgkmcnt(12)\n\t"
    "ds_read_b64_tr_b16 a[208:209], v238 offset:0\n\t"
    "v_mfma_f32_16x16x32_bf16 v[92:95], a[212:215], a[12:15], v[92:95]\n\t"
    "s_add_u32 s38, s38, 262272\n\t"
    "ds_read_b64_tr_b16 a[210:211], v238 offset:1024\n\t"
    "v_mfma_f32_16x16x32_bf16 v[84:87], a[212:215], a[8:11], v[84:87]\n\t"
    "s_add_u32 s38, s38, s6\n\t"
    "v_mfma_f32_16x16x32_bf16 v[76:79], a[212:215], a[4:7], v[76:79]\n\t"
    "buffer_load_dwordx4 a[84:87], v240, s[64:67], s34 offen offset:1024 sc1\n\t"
    "v_mfma_f32_16x16x32_bf16 v[68:71], a[212:215], a[0:3], v[68:71]\n\t"
    "s_waitcnt lgkmcnt(12)\n\t"
    "ds_read_b64_tr_b16 a[212:213], v238 offset:8\n\t"
    "v_mfma_f32_16x16x32_bf16 v[96:99], a[216:219], a[0:3], v[96:99]\n\t"
    "s_lshl_b32 s39, s22, 1\n\t"
    "ds_read_b64_tr_b16 a[214:215], v238 offset:1032\n\t"
    "v_mfma_f32_16x16x32_bf16 v[104:107], a[216:219], a[4:7], v[104:107]\n\t"
    "v_mfma_f32_16x16x32_bf16 v[112:115], a[216:219], a[8:11], v[112:115]\n\t"
    "s_lshr_b32 s40, s28, 5\n\t"
    "buffer_load_dwordx4 a[88:91], v240, s[64:67], s34 offen offset:2048 sc1\n\t"
    "v_mfma_f32_16x16x32_bf16 v[120:123], a[216:219], a[12:15], v[120:123]\n\t"
    "s_waitcnt lgkmcnt(12)\n\t"
    "ds_read_b64_tr_b16 a[216:217], v239 offset:0\n\t"
    "v_mfma_f32_16x16x32_bf16 v[124:127], a[220:223], a[12:15], v[124:127]\n\t"
    "ds_read_b64_tr_b16 a[218:219], v239 offset:1024\n\t"
    "v_mfma_f32_16x16x32_bf16 v[116:119], a[220:223], a[8:11], v[116:119]\n\t"
    "v_mfma_f32_16x16x32_bf16 v[108:111], a[220:223], a[4:7], v[108:111]\n\t"
    "buffer_load_dwordx4 a[92:95], v240, s[64:67], s34 offen offset:3072 sc1\n\t"
    "v_mfma_f32_16x16x32_bf16 v[100:103], a[220:223], a[0:3], v[100:103]\n\t"
    "s_waitcnt vmcnt(34) lgkmcnt(12)\n\t"
    "ds_read_b64_tr_b16 a[220:221], v239 offset:8\n\t"
    "v_mfma_f32_16x16x32_bf16 v[0:3], a[192:195], a[16:19], v[0:3]\n\t"
    "ds_read_b64_tr_b16 a[222:223], v239 offset:1032\n\t"
    "v_readlane_b32 s24, v246, 22\n\t"
    "v_mfma_f32_16x16x32_bf16 v[8:11], a[192:195], a[20:23], v[8:11]\n\t"
    "s_sub_u32 s39, s39, s40\n\t"
    "v_mfma_f32_16x16x32_bf16 v[16:19], a[192:195], a[24:27], v[16:19]\n\t"
    "s_mov_b32 s34, s24\n\t"
    "buffer_load_dwordx4 a[96:99], v240, s[64:67], s34 offen offset:0 sc1\n\t"
    "v_mfma_f32_16x16x32_bf16 v[24:27], a[192:195], a[28:31], v[24:27]\n\t"
    "s_waitcnt lgkmcnt(12)\n\t"
    "ds_read_b64_tr_b16 a[192:193], v236 offset:8224\n\t"
    "v_mfma_f32_16x16x32_bf16 v[28:31], a[196:199], a[28:31], v[28:31]\n\t"
    "ds_read_b64_tr_b16 a[194:195], v236 offset:9248\n\t"
    "v_and_b32 v188, 1, v245\n\t"
    "v_mfma_f32_16x16x32_bf16 v[20:23], a[196:199], a[24:27], v[20:23]\n\t"
    "v_mfma_f32_16x16x32_bf16 v[12:15], a[196:199], a[20:23], v[12:15]\n\t"
    "buffer_load_dwordx4 a[100:103], v240, s[64:67], s34 offen offset:1024 sc1\n\t"
    "v_add_u32 v188, s39, v188\n\t"
    "v_mfma_f32_16x16x32_bf16 v[4:7], a[196:199], a[16:19], v[4:7]\n\t"
    "s_waitcnt lgkmcnt(12)\n\t"
    "ds_read_b64_tr_b16 a[196:197], v236 offset:8232\n\t"
    "v_mfma_f32_16x16x32_bf16 v[32:35], a[200:203], a[16:19], v[32:35]\n\t"
    "ds_read_b64_tr_b16 a[198:199], v236 offset:9256\n\t"
    "v_and_b32 v188, 7, v188\n\t"
    "v_mfma_f32_16x16x32_bf16 v[40:43], a[200:203], a[20:23], v[40:43]\n\t"
    "v_mfma_f32_16x16x32_bf16 v[48:51], a[200:203], a[24:27], v[48:51]\n\t"
    "buffer_load_dwordx4 a[104:107], v240, s[64:67], s34 offen offset:2048 sc1\n\t"
    "v_mfma_f32_16x16x32_bf16 v[56:59], a[200:203], a[28:31], v[56:59]\n\t"
    "s_waitcnt lgkmcnt(12)\n\t"
    "ds_read_b64_tr_b16 a[200:201], v237 offset:8224\n\t"
    "v_mfma_f32_16x16x32_bf16 v[60:63], a[204:207], a[28:31], v[60:63]\n\t"
    "ds_read_b64_tr_b16 a[202:203], v237 offset:9248\n\t"
    "v_bfe_u32 v189, v245, 1, 2\n\t"
    "v_mfma_f32_16x16x32_bf16 v[52:55], a[204:207], a[24:27], v[52:55]\n\t"
    "v_lshl_add_u32 v188, v188, 3, v189\n\t"
    "v_mfma_f32_16x16x32_bf16 v[44:47], a[204:207], a[20:23], v[44:47]\n\t"
    "v_mfma_f32_16x16x32_bf16 v[36:39], a[204:207], a[16:19], v[36:39]\n\t"
    "s_waitcnt lgkmcnt(12)\n\t"
    "ds_read_b64_tr_b16 a[204:205], v237 offset:8232\n\t"
    "v_mfma_f32_16x16x32_bf16 v[64:67], a[208:211], a[16:19], v[64:67]\n\t"
    "ds_read_b64_tr_b16 a[206:207], v237 offset:9256\n\t"
    "v_lshl_add_u32 v249, v188, 11, s38\n\t"
    "v_mfma_f32_16x16x32_bf16 v[72:75], a[208:211], a[20:23], v[72:75]\n\t"
    "v_mfma_f32_16x16x32_bf16 v[80:83], a[208:211], a[24:27], v[80:83]\n\t"
    "buffer_load_dwordx4 a[108:111], v240, s[64:67], s34 offen offset:3072 sc1\n\t"
    "v_bfe_u32 v189, v245, 3, 2\n\t"
    "v_mfma_f32_16x16x32_bf16 v[88:91], a[208:211], a[28:31], v[88:91]\n\t"
    "s_waitcnt lgkmcnt(12)\n\t"
    "ds_read_b64_tr_b16 a[208:209], v238 offset:8224\n\t"
    "v_mfma_f32_16x16x32_bf16 v[92:95], a[212:215], a[28:31], v[92:95]\n\t"
    "ds_read_b64_tr_b16 a[210:211], v238 offset:9248\n\t"
    "v_readlane_b32 s24, v246, 23\n\t"
    "v_mfma_f32_16x16x32_bf16 v[84:87], a[212:215], a[24:27], v[84:87]\n\t"
    "v_lshl_add_u32 v249, v189, 6, v249\n\t"
    "v_mfma_f32_16x16x32_bf16 v[76:79], a[212:215], a[20:23], v[76:79]\n\t"
    "s_mov_b32 s34, s24\n\t"
    "buffer_load_dwordx4 a[112:115], v240, s[64:67], s34 offen offset:0 sc1\n\t"
    "v_mfma_f32_16x16x32_bf16 v[68:71], a[212:215], a[16:19], v[68:71]\n\t"
    "s_waitcnt lgkmcnt(12)\n\t"
    "ds_read_b64_tr_b16 a[212:213], v238 offset:8232\n\t"
    "v_mfma_f32_16x16x32_bf16 v[96:99], a[216:219], a[16:19], v[96:99]\n\t"
    "ds_read_b64_tr_b16 a[214:215], v238 offset:9256\n\t"
    "v_mfma_f32_16x16x32_bf16 v[104:107], a[216:219], a[20:23], v[104:107]\n\t"
    "s_add_u32 s48, s18, s36\n\t"
    "s_addc_u32 s49, s19, 0\n\t"
    "v_mfma_f32_16x16x32_bf16 v[112:115], a[216:219], a[24:27], v[112:115]\n\t"
    "s_mov_b32 s60, 0x3f80\n\t"
    "buffer_load_dwordx4 a[116:119], v240, s[64:67], s34 offen offset:1024 sc1\n\t"
    "v_mfma_f32_16x16x32_bf16 v[120:123], a[216:219], a[28:31], v[120:123]\n\t"
    "s_waitcnt lgkmcnt(12)\n\t"
    "ds_read_b64_tr_b16 a[216:217], v239 offset:8224\n\t"
    "v_mfma_f32_16x16x32_bf16 v[124:127], a[220:223], a[28:31], v[124:127]\n\t"
    "ds_read_b64_tr_b16 a[218:219], v239 offset:9248\n\t"
    "v_mfma_f32_16x16x32_bf16 v[116:119], a[220:223], a[24:27], v[116:119]\n\t"
    "v_mfma_f32_16x16x32_bf16 v[108:111], a[220:223], a[20:23], v[108:111]\n\t"
    "buffer_load_dwordx4 a[120:123], v240, s[64:67], s34 offen offset:2048 sc1\n\t"
    "v_mfma_f32_16x16x32_bf16 v[100:103], a[220:223], a[16:19], v[100:103]\n\t"
    "s_waitcnt vmcnt(33) lgkmcnt(12)\n\t"
    "ds_read_b64_tr_b16 a[220:221], v239 offset:8232\n\t"
    "v_mfma_f32_16x16x32_bf16 v[0:3], a[192:195], a[32:35], v[0:3]\n\t"
    "s_barrier\n\t"
    "v_mfma_f32_16x16x32_bf16 v[8:11], a[192:195], a[36:39], v[8:11]\n\t"
    "s_mov_b32 s61, 0x3f800000\n\t"
    "ds_read_b64_tr_b16 a[222:223], v239 offset:9256\n\t"
    "v_mfma_f32_16x16x32_bf16 v[16:19], a[192:195], a[40:43], v[16:19]\n\t"
    "buffer_load_dwordx4 a[124:127], v240, s[64:67], s34 offen offset:3072 sc1\n\t"
    "v_mfma_f32_16x16x32_bf16 v[24:27], a[192:195], a[44:47], v[24:27]\n\t"
    "s_waitcnt lgkmcnt(12)\n\t"
    "ds_read_b64_tr_b16 a[192:193], v236 offset:16448\n\t"
    "v_mfma_f32_16x16x32_bf16 v[28:31], a[196:199], a[44:47], v[28:31]\n\t"
    "ds_read_b64_tr_b16 a[194:195], v236 offset:17472\n\t"
    "v_readlane_b32 s24, v246, 24\n\t"
    "v_mfma_f32_16x16x32_bf16 v[20:23], a[196:199], a[40:43], v[20:23]\n\t"
    "v_mfma_f32_16x16x32_bf16 v[12:15], a[196:199], a[36:39], v[12:15]\n\t"
    "s_mov_b32 s34, s24\n\t"
    "buffer_load_dwordx4 a[0:3], v240, s[64:67], s34 offen offset:0 sc1\n\t"
    "v_mfma_f32_16x16x32_bf16 v[4:7], a[196:199], a[32:35], v[4:7]\n\t"
    "s_waitcnt lgkmcnt(12)\n\t"
    "ds_read_b64_tr_b16 a[196:197], v236 offset:16456\n\t"
    "v_mfma_f32_16x16x32_bf16 v[32:35], a[200:203], a[32:35], v[32:35]\n\t"
    "ds_read_b64_tr_b16 a[198:199], v236 offset:17480\n\t"
    "v_mfma_f32_16x16x32_bf16 v[40:43], a[200:203], a[36:39], v[40:43]\n\t"
    "v_mfma_f32_16x16x32_bf16 v[48:51], a[200:203], a[40:43], v[48:51]\n\t"
    "s_mov_b32 s72, 0xbfb8aa3b\n\t"
    "buffer_load_dwordx4 a[4:7], v240, s[64:67], s34 offen offset:1024 sc1\n\t"
    "v_mfma_f32_16x16x32_bf16 v[56:59], a[200:203], a[44:47], v[56:59]\n\t"
    "s_waitcnt lgkmcnt(12)\n\t"
    "ds_read_b64_tr_b16 a[200:201], v237 offset:16448\n\t"
    "v_mfma_f32_16x16x32_bf16 v[60:63], a[204:207], a[44:47], v[60:63]\n\t"
    "ds_read_b64_tr_b16 a[202:203], v237 offset:17472\n\t"
    "v_mfma_f32_16x16x32_bf16 v[52:55], a[204:207], a[40:43], v[52:55]\n\t"
    "v_mfma_f32_16x16x32_bf16 v[44:47], a[204:207], a[36:39], v[44:47]\n\t"
    "v_mfma_f32_16x16x32_bf16 v[36:39], a[204:207], a[32:35], v[36:39]\n\t"
    "s_waitcnt lgkmcnt(12)\n\t"
    "ds_read_b64_tr_b16 a[204:205], v237 offset:16456\n\t"
    "v_mfma_f32_16x16x32_bf16 v[64:67], a[208:211], a[32:35], v[64:67]\n\t"
    "ds_read_b64_tr_b16 a[206:207], v237 offset:17480\n\t"
    "v_mfma_f32_16x16x32_bf16 v[72:75], a[208:211], a[36:39], v[72:75]\n\t"
    "v_mfma_f32_16x16x32_bf16 v[80:83], a[208:211], a[40:43], v[80:83]\n\t"
    "buffer_load_dwordx4 a[8:11], v240, s[64:67], s34 offen offset:2048 sc1\n\t"
    "v_mfma_f32_16x16x32_bf16 v[88:91], a[208:211], a[44:47], v[88:91]\n\t"
    "s_waitcnt lgkmcnt(12)\n\t"
    "ds_read_b64_tr_b16 a[208:209], v238 offset:16448\n\t"
    "v_mfma_f32_16x16x32_bf16 v[92:95], a[212:215], a[44:47], v[92:95]\n\t"
    "s_mov_b32 s73, s72\n\t"
    "ds_read_b64_tr_b16 a[210:211], v238 offset:17472\n\t"
    "v_mfma_f32_16x16x32_bf16 v[84:87], a[212:215], a[40:43], v[84:87]\n\t"
    "v_mfma_f32_16x16x32_bf16 v[76:79], a[212:215], a[36:39], v[76:79]\n\t"
    "buffer_load_dwordx4 a[12:15], v240, s[64:67], s34 offen offset:3072 sc1\n\t"
    "v_mfma_f32_16x16x32_bf16 v[68:71], a[212:215], a[32:35], v[68:71]\n\t"
    "s_waitcnt lgkmcnt(12)\n\t"
    "ds_read_b64_tr_b16 a[212:213], v238 offset:16456\n\t"
    "v_mfma_f32_16x16x32_bf16 v[96:99], a[216:219], a[32:35], v[96:99]\n\t"
    "ds_read_b64_tr_b16 a[214:215], v238 offset:17480\n\t"
    "v_readlane_b32 s24, v246, 25\n\t"
    "v_mfma_f32_16x16x32_bf16 v[104:107], a[216:219], a[36:39], v[104:107]\n\t"
    "v_mfma_f32_16x16x32_bf16 v[112:115], a[216:219], a[40:43], v[112:115]\n\t"
    "s_mov_b32 s34, s24\n\t"
    "buffer_load_dwordx4 a[16:19], v240, s[64:67], s34 offen offset:0 sc1\n\t"
    "v_mfma_f32_16x16x32_bf16 v[120:123], a[216:219], a[44:47], v[120:123]\n\t"
    "s_waitcnt lgkmcnt(12)\n\t"
    "ds_read_b64_tr_b16 a[216:217], v239 offset:16448\n\t"
    "v_mfma_f32_16x16x32_bf16 v[124:127], a[220:223], a[44:47], v[124:127]\n\t"
    "ds_read_b64_tr_b16 a[218:219], v239 offset:17472\n\t"
    "v_mfma_f32_16x16x32_bf16 v[116:119], a[220:223], a[40:43], v[116:119]\n\t"
    "v_mfma_f32_16x16x32_bf16 v[108:111], a[220:223], a[36:39], v[108:111]\n\t"
    "buffer_load_dwordx4 a[20:23], v240, s[64:67], s34 offen offset:1024 sc1\n\t"
    "v_mfma_f32_16x16x32_bf16 v[100:103], a[220:223], a[32:35], v[100:103]\n\t"
    "s_waitcnt vmcnt(36) lgkmcnt(12)\n\t"
    "ds_read_b64_tr_b16 a[220:221], v239 offset:16456\n\t"
    "v_mfma_f32_16x16x32_bf16 v[0:3], a[192:195], a[48:51], v[0:3]\n\t"
    "ds_read_b64_tr_b16 a[222:223], v239 offset:17480\n\t"
    "v_mfma_f32_16x16x32_bf16 v[8:11], a[192:195], a[52:55], v[8:11]\n\t"
    "global_load_dwordx2 v[250:251], v176, s[20:21] sc1\n\t"
    "v_mfma_f32_16x16x32_bf16 v[16:19], a[192:195], a[56:59], v[16:19]\n\t"
    "buffer_load_dwordx4 a[24:27], v240, s[64:67], s34 offen offset:2048 sc1\n\t"
    "v_mfma_f32_16x16x32_bf16 v[24:27], a[192:195], a[60:63], v[24:27]\n\t"
    "s_waitcnt lgkmcnt(12)\n\t"
    "ds_read_b64_tr_b16 a[192:193], v236 offset:24672\n\t"
    "v_mfma_f32_16x16x32_bf16 v[28:31], a[196:199], a[60:63], v[28:31]\n\t"
    "ds_read_b64_tr_b16 a[194:195], v236 offset:25696\n\t"
    "v_mfma_f32_16x16x32_bf16 v[20:23], a[196:199], a[56:59], v[20:23]\n\t"
    "v_mfma_f32_16x16x32_bf16 v[12:15], a[196:199], a[52:55], v[12:15]\n\t"
    "v_mfma_f32_16x16x32_bf16 v[4:7], a[196:199], a[48:51], v[4:7]\n\t"
    "s_waitcnt lgkmcnt(12)\n\t"
    "ds_read_b64_tr_b16 a[196:197], v236 offset:24680\n\t"
    "v_mfma_f32_16x16x32_bf16 v[32:35], a[200:203], a[48:51], v[32:35]\n\t"
    "ds_read_b64_tr_b16 a[198:199], v236 offset:25704\n\t"
    "v_mfma_f32_16x16x32_bf16 v[40:43], a[200:203], a[52:55], v[40:43]\n\t"
    "v_mfma_f32_16x16x32_bf16 v[48:51], a[200:203], a[56:59], v[48:51]\n\t"
    "buffer_load_dwordx4 a[28:31], v240, s[64:67], s34 offen offset:3072 sc1\n\t"
    "v_mfma_f32_16x16x32_bf16 v[56:59], a[200:203], a[60:63], v[56:59]\n\t"
    "s_waitcnt lgkmcnt(12)\n\t"
    "ds_read_b64_tr_b16 a[200:201], v237 offset:24672\n\t"
    "v_mfma_f32_16x16x32_bf16 v[60:63], a[204:207], a[60:63], v[60:63]\n\t"
    "ds_read_b64_tr_b16 a[202:203], v237 offset:25696\n\t"
    "v_readlane_b32 s24, v246, 26\n\t"
    "v_mfma_f32_16x16x32_bf16 v[52:55], a[204:207], a[56:59], v[52:55]\n\t"
    "v_mfma_f32_16x16x32_bf16 v[44:47], a[204:207], a[52:55], v[44:47]\n\t"
    "s_mov_b32 s34, s24\n\t"
    "buffer_load_dwordx4 a[32:35], v240, s[64:67], s34 offen offset:0 sc1\n\t"
    "v_mfma_f32_16x16x32_bf16 v[36:39], a[204:207], a[48:51], v[36:39]\n\t"
    "s_waitcnt lgkmcnt(12)\n\t"
    "ds_read_b64_tr_b16 a[204:205], v237 offset:24680\n\t"
    "v_mfma_f32_16x16x32_bf16 v[64:67], a[208:211], a[48:51], v[64:67]\n\t"
    "ds_read_b64_tr_b16 a[206:207], v237 offset:25704\n\t"
    "v_mfma_f32_16x16x32_bf16 v[72:75], a[208:211], a[52:55], v[72:75]\n\t"
    "v_mfma_f32_16x16x32_bf16 v[80:83], a[208:211], a[56:59], v[80:83]\n\t"
    "buffer_load_dwordx4 a[36:39], v240, s[64:67], s34 offen offset:1024 sc1\n\t"
    "v_mfma_f32_16x16x32_bf16 v[88:91], a[208:211], a[60:63], v[88:91]\n\t"
    "s_waitcnt lgkmcnt(12)\n\t"
    "ds_read_b64_tr_b16 a[208:209], v238 offset:24672\n\t"
    "v_mfma_f32_16x16x32_bf16 v[92:95], a[212:215], a[60:63], v[92:95]\n\t"
    "ds_read_b64_tr_b16 a[210:211], v238 offset:25696\n\t"
    "v_mfma_f32_16x16x32_bf16 v[84:87], a[212:215], a[56:59], v[84:87]\n\t"
    "v_mfma_f32_16x16x32_bf16 v[76:79], a[212:215], a[52:55], v[76:79]\n\t"
    "v_mfma_f32_16x16x32_bf16 v[68:71], a[212:215], a[48:51], v[68:71]\n\t"
    "s_waitcnt lgkmcnt(12)\n\t"
    "ds_read_b64_tr_b16 a[212:213], v238 offset:24680\n\t"
    "v_mfma_f32_16x16x32_bf16 v[96:99], a[216:219], a[48:51], v[96:99]\n\t"
    "ds_read_b64_tr_b16 a[214:215], v238 offset:25704\n\t"
    "v_mfma_f32_16x16x32_bf16 v[104:107], a[216:219], a[52:55], v[104:107]\n\t"
    "v_mfma_f32_16x16x32_bf16 v[112:115], a[216:219], a[56:59], v[112:115]\n\t"
    "buffer_load_dwordx4 a[40:43], v240, s[64:67], s34 offen offset:2048 sc1\n\t"
    "v_mfma_f32_16x16x32_bf16 v[120:123], a[216:219], a[60:63], v[120:123]\n\t"
    "s_waitcnt lgkmcnt(12)\n\t"
    "ds_read_b64_tr_b16 a[216:217], v239 offset:24672\n\t"
    "v_mfma_f32_16x16x32_bf16 v[124:127], a[220:223], a[60:63], v[124:127]\n\t"
    "ds_read_b64_tr_b16 a[218:219], v239 offset:25696\n\t"
    "v_mfma_f32_16x16x32_bf16 v[116:119], a[220:223], a[56:59], v[116:119]\n\t"
    "v_mfma_f32_16x16x32_bf16 v[108:111], a[220:223], a[52:55], v[108:111]\n\t"
    "buffer_load_dwordx4 a[44:47], v240, s[64:67], s34 offen offset:3072 sc1\n\t"
    "v_mfma_f32_16x16x32_bf16 v[100:103], a[220:223], a[48:51], v[100:103]\n\t"
    "s_waitcnt vmcnt(25) lgkmcnt(12)\n\t"
    "ds_read_b64_tr_b16 a[220:221], v239 offset:24680\n\t"
    "v_mfma_f32_16x16x32_bf16 v[0:3], a[192:195], a[64:67], v[0:3]\n\t"
    "s_barrier\n\t"
    "v_mfma_f32_16x16x32_bf16 v[8:11], a[192:195], a[68:71], v[8:11]\n\t"
    "ds_read_b64_tr_b16 a[222:223], v239 offset:25704\n\t"
    "v_mfma_f32_16x16x32_bf16 v[16:19], a[192:195], a[72:75], v[16:19]\n\t"
    "v_mfma_f32_16x16x32_bf16 v[24:27], a[192:195], a[76:79], v[24:27]\n\t"
    "s_waitcnt lgkmcnt(12)\n\t"
    "ds_read_b64_tr_b16 a[128:129], v236 offset:32896\n\t"
    "v_mfma_f32_16x16x32_bf16 v[28:31], a[196:199], a[76:79], v[28:31]\n\t"
    "ds_read_b64_tr_b16 a[130:131], v236 offset:33920\n\t"
    "v_readlane_b32 s24, v246, 27\n\t"
    "v_mfma_f32_16x16x32_bf16 v[20:23], a[196:199], a[72:75], v[20:23]\n\t"
    "ds_read_b64_tr_b16 a[132:133], v236 offset:32904\n\t"
    "v_mfma_f32_16x16x32_bf16 v[12:15], a[196:199], a[68:71], v[12:15]\n\t"
    "s_mov_b32 s34, s24\n\t"
    "buffer_load_dwordx4 a[48:51], v240, s[64:67], s34 offen offset:0 sc1\n\t"
    "v_mfma_f32_16x16x32_bf16 v[4:7], a[196:199], a[64:67], v[4:7]\n\t"
    "s_waitcnt lgkmcnt(13)\n\t"
    "ds_read_b64_tr_b16 a[134:135], v236 offset:33928\n\t"
    "v_mfma_f32_16x16x32_bf16 v[32:35], a[200:203], a[64:67], v[32:35]\n\t"
    "ds_read_b64_tr_b16 a[136:137], v236 offset:41128\n\t"
    "v_mfma_f32_16x16x32_bf16 v[40:43], a[200:203], a[68:71], v[40:43]\n\t"
    "ds_read_b64_tr_b16 a[138:139], v236 offset:42152\n\t"
    "v_mfma_f32_16x16x32_bf16 v[48:51], a[200:203], a[72:75], v[48:51]\n\t"
    "v_mfma_f32_16x16x32_bf16 v[56:59], a[200:203], a[76:79], v[56:59]\n\t"
    "s_waitcnt lgkmcnt(14)\n\t"
    "ds_read_b64_tr_b16 a[140:141], v236 offset:41120\n\t"
    "v_mfma_f32_16x16x32_bf16 v[60:63], a[204:207], a[76:79], v[60:63]\n\t"
    "ds_read_b64_tr_b16 a[142:143], v236 offset:42144\n\t"
    "v_mfma_f32_16x16x32_bf16 v[52:55], a[204:207], a[72:75], v[52:55]\n\t"
    "ds_read_b64_tr_b16 a[144:145], v236 offset:49344\n\t"
    "v_mfma_f32_16x16x32_bf16 v[44:47], a[204:207], a[68:71], v[44:47]\n\t"
    "buffer_load_dwordx4 a[52:55], v240, s[64:67], s34 offen offset:1024 sc1\n\t"
    "v_mfma_f32_16x16x32_bf16 v[36:39], a[204:207], a[64:67], v[36:39]\n\t"
    "s_waitcnt lgkmcnt(15)\n\t"
    "ds_read_b64_tr_b16 a[146:147], v236 offset:50368\n\t"
    "v_mfma_f32_16x16x32_bf16 v[64:67], a[208:211], a[64:67], v[64:67]\n\t"
    "ds_read_b64_tr_b16 a[148:149], v236 offset:49352\n\t"
    "v_mfma_f32_16x16x32_bf16 v[72:75], a[208:211], a[68:71], v[72:75]\n\t"
    "ds_read_b64_tr_b16 a[150:151], v236 offset:50376\n\t"
    "v_mfma_f32_16x16x32_bf16 v[80:83], a[208:211], a[72:75], v[80:83]\n\t"
    "v_mfma_f32_16x16x32_bf16 v[88:91], a[208:211], a[76:79], v[88:91]\n\t"
    "s_waitcnt lgkmcnt(15)\n\t"
    "ds_read_b64_tr_b16 a[152:153], v232 offset:8\n\t"
    "v_mfma_f32_16x16x32_bf16 v[92:95], a[212:215], a[76:79], v[92:95]\n\t"
    "ds_read_b64_tr_b16 a[154:155], v232 offset:1032\n\t"
    "v_mfma_f32_16x16x32_bf16 v[84:87], a[212:215], a[72:75], v[84:87]\n\t"
    "ds_read_b64_tr_b16 a[156:157], v232 offset:0\n\t"
    "v_mfma_f32_16x16x32_bf16 v[76:79], a[212:215], a[68:71], v[76:79]\n\t"
    "buffer_load_dwordx4 a[56:59], v240, s[64:67], s34 offen offset:2048 sc1\n\t"
    "v_mfma_f32_16x16x32_bf16 v[68:71], a[212:215], a[64:67], v[68:71]\n\t"
    "s_waitcnt lgkmcnt(15)\n\t"
    "ds_read_b64_tr_b16 a[158:159], v232 offset:1024\n\t"
    "v_mfma_f32_16x16x32_bf16 v[96:99], a[216:219], a[64:67], v[96:99]\n\t"
    "ds_read_b64_tr_b16 a[160:161], v232 offset:8224\n\t"
    "v_mfma_f32_16x16x32_bf16 v[104:107], a[216:219], a[68:71], v[104:107]\n\t"
    "ds_read_b64_tr_b16 a[162:163], v232 offset:9248\n\t"
    "s_waitcnt vmcnt(9)\n\t"
    "v_cmp_ne_u64 vcc, s[74:75], v[250:251]\n\t"
    "v_mfma_f32_16x16x32_bf16 v[112:115], a[216:219], a[72:75], v[112:115]\n\t"
    "s_nop 0\n\t"
    "s_and_b64 vcc, vcc, -1\n\t"
    "s_cbranch_scc1 Ls21_%=\n\t"
    "Lb22_%=:\n\t"
    "v_mfma_f32_16x16x32_bf16 v[120:123], a[216:219], a[76:79], v[120:123]\n\t"
    "ds_read_b64_tr_b16 a[164:165], v232 offset:8232\n\t"
    "v_mfma_f32_16x16x32_bf16 v[124:127], a[220:223], a[76:79], v[124:127]\n\t"
    "ds_read_b64_tr_b16 a[166:167], v232 offset:9256\n\t"
    "v_mfma_f32_16x16x32_bf16 v[116:119], a[220:223], a[72:75], v[116:119]\n\t"
    "ds_read_b64_tr_b16 a[168:169], v232 offset:16456\n\t"
    "v_mfma_f32_16x16x32_bf16 v[108:111], a[220:223], a[68:71], v[108:111]\n\t"
    "buffer_load_dwordx4 a[60:63], v240, s[64:67], s34 offen offset:3072 sc1\n\t"
    "v_mfma_f32_16x16x32_bf16 v[100:103], a[220:223], a[64:67], v[100:103]\n\t"
    "ds_read_b64_tr_b16 a[170:171], v232 offset:17480\n\t"
    "s_waitcnt lgkmcnt(15)\n\t"
    "v_mfma_f32_16x16x32_bf16 v[0:3], a[128:131], a[80:83], v[0:3]\n\t"
    "s_waitcnt lgkmcnt(8)\n\t"
    "ds_read_b64_tr_b16 a[172:173], v232 offset:16448\n\t"
    "v_mfma_f32_16x16x32_bf16 v[4:7], a[132:135], a[80:83], v[4:7]\n\t"
    "Lpad10_%=:\n\t"
    "s_cbranch_execz LretB_%=\n\t"
    "ds_read_b64_tr_b16 a[174:175], v232 offset:17472\n\t"
    "v_mfma_f32_16x16x32_bf16 v[4:7], a[136:139], a[96:99], v[4:7]\n\t"
    "ds_read_b64_tr_b16 a[176:177], v232 offset:24672\n\t"
    "v_mfma_f32_16x16x32_bf16 v[0:3], a[140:143], a[96:99], v[0:3]\n\t"
    "ds_read_b64_tr_b16 a[178:179], v232 offset:25696\n\t"
    "v_mfma_f32_16x16x32_bf16 v[0:3], a[144:147], a[112:115], v[0:3]\n\t"
    "ds_read_b64_tr_b16 a[180:181], v232 offset:24680\n\t"
    "v_mfma_f32_16x16x32_bf16 v[4:7], a[148:151], a[112:115], v[4:7]\n\t"
    "ds_read_b64_tr_b16 a[182:183], v232 offset:25704\n\t"
    "v_mfma_f32_16x16x32_bf16 v[4:7], a[152:155], a[0:3], v[4:7]\n\t"
    "s_waitcnt lgkmcnt(8)\n\t"
    "ds_read_b64_tr_b16 a[184:185], v237 offset:32896\n\t"
    "v_mfma_f32_16x16x32_bf16 v[0:3], a[156:159], a[0:3], v[0:3]\n\t"
    "global_load_dwordx4 v[208:211], v179, s[42:43] sc1\n\t"
    "v_mfma_f32_16x16x32_bf16 v[0:3], a[160:163], a[16:19], v[0:3]\n\t"
    "s_add_u32 s42, s42, 0x4000\n\t"
    "s_addc_u32 s43, s43, 0\n\t"
    "ds_read_b64_tr_b16 a[186:187], v237 offset:33920\n\t"
    "v_mfma_f32_16x16x32_bf16 v[4:7], a[164:167], a[16:19], v[4:7]\n\t"
    "s_waitcnt lgkmcnt(8)\n\t"
    "global_load_dwordx4 v[212:215], v179, s[42:43] sc1\n\t"
    "s_waitcnt vmcnt(9)\n\t"
    "v_mfma_f32_16x16x32_bf16 v[4:7], a[168:171], a[32:35], v[4:7]\n\t"
    "s_waitcnt lgkmcnt(6)\n\t"
    "ds_read_b64_tr_b16 a[188:189], v237 offset:32904\n\t"
    "v_mfma_f32_16x16x32_bf16 v[0:3], a[172:175], a[32:35], v[0:3]\n\t"
    "s_waitcnt lgkmcnt(5)\n\t"
    "s_add_u32 s42, s42, 0x4000\n\t"
    "s_addc_u32 s43, s43, 0\n\t"
    "s_waitcnt vmcnt(5)\n\t"
    "v_mfma_f32_16x16x32_bf16 v[0:3], a[176:179], a[48:51], v[0:3]\n\t"
    "s_waitcnt lgkmcnt(3)\n\t"
    "ds_read_b64_tr_b16 a[190:191], v237 offset:33928\n\t"
    "v_mfma_f32_16x16x32_bf16 v[4:7], a[180:183], a[48:51], v[4:7]\n\t"
    "global_load_dwordx4 v[216:219], v179, s[42:43] sc1\n\t"
    "s_add_u32 s42, s42, 0x4000\n\t"
    "s_addc_u32 s43, s43, 0\n\t"
    "global_load_dwordx4 v[220:223], v179, s[42:43] sc1\n\t"
    "global_load_dwordx4 v[128:131], v242, s[44:45] offset:0\n\t"
    "global_load_dwordx4 v[132:135], v242, s[44:45] offset:64\n\t"
    "global_load_dwordx4 v[136:139], v242, s[44:45] offset:128\n\t"
    "global_load_dwordx4 v[140:143], v242, s[44:45] offset:192\n\t"
    "v_mfma_f32_16x16x32_bf16 v[8:11], a[128:131], a[84:87], v[8:11]\n\t"
    "ds_read_b64_tr_b16 a[192:193], v237 offset:41128\n\t"
    "v_mfma_f32_16x16x32_bf16 v[12:15], a[132:135], a[84:87], v[12:15]\n\t"
    "ds_read_b64_tr_b16 a[194:195], v237 offset:42152\n\t"
    "v_mfma_f32_16x16x32_bf16 v[12:15], a[136:139], a[100:103], v[12:15]\n\t"
    "ds_read_b64_tr_b16 a[196:197], v237 offset:41120\n\t"
    "v_mfma_f32_16x16x32_bf16 v[8:11], a[140:143], a[100:103], v[8:11]\n\t"
    "v_cvt_pk_bf16_f32 v224, v0, v1\n\t"
    "v_cvt_pk_bf16_f32 v225, v2, v3\n\t"
    "v_mfma_f32_16x16x32_bf16 v[8:11], a[144:147], a[116:119], v[8:11]\n\t"
    "ds_read_b64_tr_b16 a[198:199], v237 offset:42144\n\t"
    "v_cvt_pk_bf16_f32 v226, v4, v5\n\t"
    "v_mfma_f32_16x16x32_bf16 v[12:15], a[148:151], a[116:119], v[12:15]\n\t"
    "v_cvt_pk_bf16_f32 v227, v6, v7\n\t"
    "v_mfma_f32_16x16x32_bf16 v[12:15], a[152:155], a[4:7], v[12:15]\n\t"
    "ds_read_b64_tr_b16 a[200:201], v237 offset:49344\n\t"
    "global_store_dwordx4 v241, v[224:227], s[46:47] offset:0 sc1\n\t"
    "v_mfma_f32_16x16x32_bf16 v[8:11], a[156:159], a[4:7], v[8:11]\n\t"
    "v_mfma_f32_16x16x32_bf16 v[8:11], a[160:163], a[20:23], v[8:11]\n\t"
    "ds_read_b64_tr_b16 a[202:203], v237 offset:50368\n\t"
    "v_mfma_f32_16x16x32_bf16 v[12:15], a[164:167], a[20:23], v[12:15]\n\t"
    "v_mfma_f32_16x16x32_bf16 v[12:15], a[168:171], a[36:39], v[12:15]\n\t"
    "ds_read_b64_tr_b16 a[204:205], v237 offset:49352\n\t"
    "v_mfma_f32_16x16x32_bf16 v[8:11], a[172:175], a[36:39], v[8:11]\n\t"
    "s_waitcnt vmcnt(11)\n\t"
    "v_mfma_f32_16x16x32_bf16 v[8:11], a[176:179], a[52:55], v[8:11]\n\t"
    "ds_read_b64_tr_b16 a[206:207], v237 offset:50376\n\t"
    "v_mfma_f32_16x16x32_bf16 v[12:15], a[180:183], a[52:55], v[12:15]\n\t"
    "v_mfma_f32_16x16x32_bf16 v[16:19], a[128:131], a[88:91], v[16:19]\n\t"
    "ds_read_b64_tr_b16 a[208:209], v233 offset:8\n\t"
    "v_and_b32 v244, 0x1f0, v240\n\t"
    "v_mfma_f32_16x16x32_bf16 v[20:23], a[132:135], a[88:91], v[20:23]\n\t"
    "ds_read_b64_tr_b16 a[210:211], v233 offset:1032\n\t"
    "v_mfma_f32_16x16x32_bf16 v[20:23], a[136:139], a[104:107], v[20:23]\n\t"
    "ds_read_b64_tr_b16 a[212:213], v233 offset:0\n\t"
    "v_mfma_f32_16x16x32_bf16 v[16:19], a[140:143], a[104:107], v[16:19]\n\t"
    "v_lshrrev_b32 v243, 5, v245\n\t"
    "v_cvt_pk_bf16_f32 v228, v8, v9\n\t"
    "v_mfma_f32_16x16x32_bf16 v[16:19], a[144:147], a[120:123], v[16:19]\n\t"
    "ds_read_b64_tr_b16 a[214:215], v233 offset:1024\n\t"
    "v_cvt_pk_bf16_f32 v229, v10, v11\n\t"
    "v_mfma_f32_16x16x32_bf16 v[20:23], a[148:151], a[120:123], v[20:23]\n\t"
    "v_cvt_pk_bf16_f32 v230, v12, v13\n\t"
    "v_cvt_pk_bf16_f32 v231, v14, v15\n\t"
    "v_mfma_f32_16x16x32_bf16 v[20:23], a[152:155], a[8:11], v[20:23]\n\t"
    "ds_read_b64_tr_b16 a[216:217], v233 offset:8224\n\t"
    "v_lshl_or_b32 v244, v243, 15, v244\n\t"
    "v_mfma_f32_16x16x32_bf16 v[16:19], a[156:159], a[8:11], v[16:19]\n\t"
    "global_store_dwordx4 v241, v[228:231], s[46:47] offset:1024 sc1\n\t"
    "v_mfma_f32_16x16x32_bf16 v[16:19], a[160:163], a[24:27], v[16:19]\n\t"
    "ds_read_b64_tr_b16 a[218:219], v233 offset:9248\n\t"
    "v_mfma_f32_16x16x32_bf16 v[20:23], a[164:167], a[24:27], v[20:23]\n\t"
    "v_add_u32 v243, 16384, v244\n\t"
    "v_mfma_f32_16x16x32_bf16 v[20:23], a[168:171], a[40:43], v[20:23]\n\t"
    "ds_read_b64_tr_b16 a[220:221], v233 offset:8232\n\t"
    "v_mfma_f32_16x16x32_bf16 v[16:19], a[172:175], a[40:43], v[16:19]\n\t"
    "s_waitcnt vmcnt(11)\n\t"
    "v_mfma_f32_16x16x32_bf16 v[16:19], a[176:179], a[56:59], v[16:19]\n\t"
    "ds_read_b64_tr_b16 a[222:223], v233 offset:9256\n\t"
    "v_mfma_f32_16x16x32_bf16 v[20:23], a[180:183], a[56:59], v[20:23]\n\t"
    "v_mfma_f32_16x16x32_bf16 v[24:27], a[128:131], a[92:95], v[24:27]\n\t"
    "ds_read_b64_tr_b16 a[128:129], v233 offset:16456\n\t"
    "v_mfma_f32_16x16x32_bf16 v[28:31], a[132:135], a[92:95], v[28:31]\n\t"
    "ds_read_b64_tr_b16 a[130:131], v233 offset:17480\n\t"
    "v_mfma_f32_16x16x32_bf16 v[28:31], a[136:139], a[108:111], v[28:31]\n\t"
    "ds_read_b64_tr_b16 a[132:133], v233 offset:16448\n\t"
    "v_mfma_f32_16x16x32_bf16 v[24:27], a[140:143], a[108:111], v[24:27]\n\t"
    "s_waitcnt vmcnt(9)\n\t"
    "ds_write_b32 v180, v208 offset:0\n\t"
    "v_mfma_f32_16x16x32_bf16 v[24:27], a[144:147], a[124:127], v[24:27]\n\t"
    "ds_read_b64_tr_b16 a[134:135], v233 offset:17472\n\t"
    "v_cvt_pk_bf16_f32 v224, v16, v17\n\t"
    "v_mfma_f32_16x16x32_bf16 v[28:31], a[148:151], a[124:127], v[28:31]\n\t"
    "ds_read_b64_tr_b16 a[136:137], v233 offset:24672\n\t"
    "v_cvt_pk_bf16_f32 v225, v18, v19\n\t"
    "v_mfma_f32_16x16x32_bf16 v[28:31], a[152:155], a[12:15], v[28:31]\n\t"
    "ds_read_b64_tr_b16 a[138:139], v233 offset:25696\n\t"
    "v_cvt_pk_bf16_f32 v226, v20, v21\n\t"
    "v_mfma_f32_16x16x32_bf16 v[24:27], a[156:159], a[12:15], v[24:27]\n\t"
    "ds_read_b64_tr_b16 a[140:141], v233 offset:24680\n\t"
    "v_cvt_pk_bf16_f32 v227, v22, v23\n\t"
    "v_mfma_f32_16x16x32_bf16 v[24:27], a[160:163], a[28:31], v[24:27]\n\t"
    "ds_read_b64_tr_b16 a[142:143], v233 offset:25704\n\t"
    "global_store_dwordx4 v241, v[224:227], s[46:47] offset:2048 sc1\n\t"
    "v_mfma_f32_16x16x32_bf16 v[28:31], a[164:167], a[28:31], v[28:31]\n\t"
    "ds_write_b32 v180, v209 offset:4608\n\t"
    "v_mfma_f32_16x16x32_bf16 v[28:31], a[168:171], a[44:47], v[28:31]\n\t"
    "v_mfma_f32_16x16x32_bf16 v[24:27], a[172:175], a[44:47], v[24:27]\n\t"
    "v_mfma_f32_16x16x32_bf16 v[24:27], a[176:179], a[60:63], v[24:27]\n\t"
    "v_mfma_f32_16x16x32_bf16 v[28:31], a[180:183], a[60:63], v[28:31]\n\t"
    "s_waitcnt lgkmcnt(10)\n\t"
    "v_mfma_f32_16x16x32_bf16 v[32:35], a[184:187], a[80:83], v[32:35]\n\t"
    "ds_read_b64_tr_b16 a[144:145], v238 offset:32896\n\t"
    "v_mfma_f32_16x16x32_bf16 v[36:39], a[188:191], a[80:83], v[36:39]\n\t"
    "ds_read_b64_tr_b16 a[146:147], v238 offset:33920\n\t"
    "v_mfma_f32_16x16x32_bf16 v[36:39], a[192:195], a[96:99], v[36:39]\n\t"
    "ds_read_b64_tr_b16 a[148:149], v238 offset:32904\n\t"
    "v_mfma_f32_16x16x32_bf16 v[32:35], a[196:199], a[96:99], v[32:35]\n\t"
    "ds_write_b32 v180, v210 offset:9216\n\t"
    "v_mfma_f32_16x16x32_bf16 v[32:35], a[200:203], a[112:115], v[32:35]\n\t"
    "ds_read_b64_tr_b16 a[150:151], v238 offset:33928\n\t"
    "v_cvt_pk_bf16_f32 v228, v24, v25\n\t"
    "v_mfma_f32_16x16x32_bf16 v[36:39], a[204:207], a[112:115], v[36:39]\n\t"
    "v_cvt_pk_bf16_f32 v229, v26, v27\n\t"
    "v_cvt_pk_bf16_f32 v230, v28, v29\n\t"
    "v_mfma_f32_16x16x32_bf16 v[36:39], a[208:211], a[0:3], v[36:39]\n\t"
    "ds_read_b64_tr_b16 a[152:153], v238 offset:41128\n\t"
    "v_cvt_pk_bf16_f32 v231, v30, v31\n\t"
    "v_mfma_f32_16x16x32_bf16 v[32:35], a[212:215], a[0:3], v[32:35]\n\t"
    "ds_write_b32 v180, v211 offset:13824\n\t"
    "v_mfma_f32_16x16x32_bf16 v[32:35], a[216:219], a[16:19], v[32:35]\n\t"
    "ds_read_b64_tr_b16 a[154:155], v238 offset:42152\n\t"
    "global_store_dwordx4 v241, v[228:231], s[46:47] offset:3072 sc1\n\t"
    "v_mfma_f32_16x16x32_bf16 v[36:39], a[220:223], a[16:19], v[36:39]\n\t"
    "s_waitcnt lgkmcnt(11)\n\t"
    "s_add_u32 s46, s46, 0x1000\n\t"
    "s_addc_u32 s47, s47, 0\n\t"
    "v_mfma_f32_16x16x32_bf16 v[36:39], a[128:131], a[32:35], v[36:39]\n\t"
    "ds_read_b64_tr_b16 a[156:157], v238 offset:41120\n\t"
    "v_mfma_f32_16x16x32_bf16 v[32:35], a[132:135], a[32:35], v[32:35]\n\t"
    "v_mfma_f32_16x16x32_bf16 v[32:35], a[136:139], a[48:51], v[32:35]\n\t"
    "s_waitcnt lgkmcnt(10)\n\t"
    "ds_read_b64_tr_b16 a[158:159], v238 offset:42144\n\t"
    "v_mfma_f32_16x16x32_bf16 v[36:39], a[140:143], a[48:51], v[36:39]\n\t"
    "v_mfma_f32_16x16x32_bf16 v[40:43], a[184:187], a[84:87], v[40:43]\n\t"
    "ds_read_b64_tr_b16 a[160:161], v238 offset:49344\n\t"
    "v_mfma_f32_16x16x32_bf16 v[44:47], a[188:191], a[84:87], v[44:47]\n\t"
    "ds_read_b64_tr_b16 a[162:163], v238 offset:50368\n\t"
    "v_mfma_f32_16x16x32_bf16 v[44:47], a[192:195], a[100:103], v[44:47]\n\t"
    "ds_read_b64_tr_b16 a[164:165], v238 offset:49352\n\t"
    "v_mfma_f32_16x16x32_bf16 v[40:43], a[196:199], a[100:103], v[40:43]\n\t"
    "s_waitcnt vmcnt(10)\n\t"
    "ds_write_b32 v181, v212 offset:0\n\t"
    "v_mfma_f32_16x16x32_bf16 v[40:43], a[200:203], a[116:119], v[40:43]\n\t"
    "ds_read_b64_tr_b16 a[166:167], v238 offset:50376\n\t"
    "v_cvt_pk_bf16_f32 v224, v32, v33\n\t"
    "v_mfma_f32_16x16x32_bf16 v[44:47], a[204:207], a[116:119], v[44:47]\n\t"
    "v_cvt_pk_bf16_f32 v225, v34, v35\n\t"
    "v_cvt_pk_bf16_f32 v226, v36, v37\n\t"
    "v_mfma_f32_16x16x32_bf16 v[44:47], a[208:211], a[4:7], v[44:47]\n\t"
    "ds_read_b64_tr_b16 a[168:169], v234 offset:8\n\t"
    "v_cvt_pk_bf16_f32 v227, v38, v39\n\t"
    "v_mfma_f32_16x16x32_bf16 v[40:43], a[212:215], a[4:7], v[40:43]\n\t"
    "ds_write_b32 v181, v213 offset:4608\n\t"
    "v_mfma_f32_16x16x32_bf16 v[40:43], a[216:219], a[20:23], v[40:43]\n\t"
    "ds_read_b64_tr_b16 a[170:171], v234 offset:1032\n\t"
    "global_store_dwordx4 v241, v[224:227], s[46:47] offset:0 sc1\n\t"
    "v_mfma_f32_16x16x32_bf16 v[44:47], a[220:223], a[20:23], v[44:47]\n\t"
    "v_mfma_f32_16x16x32_bf16 v[44:47], a[128:131], a[36:39], v[44:47]\n\t"
    "ds_read_b64_tr_b16 a[172:173], v234 offset:0\n\t"
    "v_mfma_f32_16x16x32_bf16 v[40:43], a[132:135], a[36:39], v[40:43]\n\t"
    "v_mfma_f32_16x16x32_bf16 v[40:43], a[136:139], a[52:55], v[40:43]\n\t"
    "ds_read_b64_tr_b16 a[174:175], v234 offset:1024\n\t"
    "v_mfma_f32_16x16x32_bf16 v[44:47], a[140:143], a[52:55], v[44:47]\n\t"
    "v_mfma_f32_16x16x32_bf16 v[48:51], a[184:187], a[88:91], v[48:51]\n\t"
    "ds_read_b64_tr_b16 a[176:177], v234 offset:8224\n\t"
    "v_mfma_f32_16x16x32_bf16 v[52:55], a[188:191], a[88:91], v[52:55]\n\t"
    "ds_read_b64_tr_b16 a[178:179], v234 offset:9248\n\t"
    "v_mfma_f32_16x16x32_bf16 v[52:55], a[192:195], a[104:107], v[52:55]\n\t"
    "ds_read_b64_tr_b16 a[180:181], v234 offset:8232\n\t"
    "v_mfma_f32_16x16x32_bf16 v[48:51], a[196:199], a[104:107], v[48:51]\n\t"
    "ds_write_b32 v181, v214 offset:9216\n\t"
    "v_mfma_f32_16x16x32_bf16 v[48:51], a[200:203], a[120:123], v[48:51]\n\t"
    "ds_read_b64_tr_b16 a[182:183], v234 offset:9256\n\t"
    "v_cvt_pk_bf16_f32 v228, v40, v41\n\t"
    "v_mfma_f32_16x16x32_bf16 v[52:55], a[204:207], a[120:123], v[52:55]\n\t"
    "v_cvt_pk_bf16_f32 v229, v42, v43\n\t"
    "v_cvt_pk_bf16_f32 v230, v44, v45\n\t"
    "v_mfma_f32_16x16x32_bf16 v[52:55], a[208:211], a[8:11], v[52:55]\n\t"
    "ds_write_b32 v181, v215 offset:13824\n\t"
    "v_mfma_f32_16x16x32_bf16 v[48:51], a[212:215], a[8:11], v[48:51]\n\t"
    "v_cvt_pk_bf16_f32 v231, v46, v47\n\t"
    "v_mfma_f32_16x16x32_bf16 v[48:51], a[216:219], a[24:27], v[48:51]\n\t"
    "global_store_dwordx4 v241, v[228:231], s[46:47] offset:1024 sc1\n\t"
    "v_mfma_f32_16x16x32_bf16 v[52:55], a[220:223], a[24:27], v[52:55]\n\t"
    "v_mfma_f32_16x16x32_bf16 v[52:55], a[128:131], a[40:43], v[52:55]\n\t"
    "v_mfma_f32_16x16x32_bf16 v[48:51], a[132:135], a[40:43], v[48:51]\n\t"
    "v_mfma_f32_16x16x32_bf16 v[48:51], a[136:139], a[56:59], v[48:51]\n\t"
    "v_mfma_f32_16x16x32_bf16 v[52:55], a[140:143], a[56:59], v[52:55]\n\t"
    "v_mfma_f32_16x16x32_bf16 v[56:59], a[184:187], a[92:95], v[56:59]\n\t"
    "ds_read_b64_tr_b16 a[184:185], v234 offset:16456\n\t"
    "v_mfma_f32_16x16x32_bf16 v[60:63], a[188:191], a[92:95], v[60:63]\n\t"
    "ds_read_b64_tr_b16 a[186:187], v234 offset:17480\n\t"
    "v_mfma_f32_16x16x32_bf16 v[60:63], a[192:195], a[108:111], v[60:63]\n\t"
    "ds_read_b64_tr_b16 a[188:189], v234 offset:16448\n\t"
    "v_mfma_f32_16x16x32_bf16 v[56:59], a[196:199], a[108:111], v[56:59]\n\t"
    "s_waitcnt vmcnt(11)\n\t"
    "ds_write_b32 v182, v216 offset:0\n\t"
    "v_mfma_f32_16x16x32_bf16 v[56:59], a[200:203], a[124:127], v[56:59]\n\t"
    "ds_read_b64_tr_b16 a[190:191], v234 offset:17472\n\t"
    "v_cvt_pk_bf16_f32 v224, v48, v49\n\t"
    "v_mfma_f32_16x16x32_bf16 v[60:63], a[204:207], a[124:127], v[60:63]\n\t"
    "ds_read_b64_tr_b16 a[192:193], v234 offset:24672\n\t"
    "v_cvt_pk_bf16_f32 v225, v50, v51\n\t"
    "v_mfma_f32_16x16x32_bf16 v[60:63], a[208:211], a[12:15], v[60:63]\n\t"
    "ds_read_b64_tr_b16 a[194:195], v234 offset:25696\n\t"
    "v_cvt_pk_bf16_f32 v226, v52, v53\n\t"
    "v_mfma_f32_16x16x32_bf16 v[56:59], a[212:215], a[12:15], v[56:59]\n\t"
    "ds_read_b64_tr_b16 a[196:197], v234 offset:24680\n\t"
    "v_cvt_pk_bf16_f32 v227, v54, v55\n\t"
    "v_mfma_f32_16x16x32_bf16 v[56:59], a[216:219], a[28:31], v[56:59]\n\t"
    "ds_read_b64_tr_b16 a[198:199], v234 offset:25704\n\t"
    "global_store_dwordx4 v241, v[224:227], s[46:47] offset:2048 sc1\n\t"
    "v_mfma_f32_16x16x32_bf16 v[60:63], a[220:223], a[28:31], v[60:63]\n\t"
    "ds_write_b32 v182, v217 offset:4608\n\t"
    "v_mfma_f32_16x16x32_bf16 v[60:63], a[128:131], a[44:47], v[60:63]\n\t"
    "v_mfma_f32_16x16x32_bf16 v[56:59], a[132:135], a[44:47], v[56:59]\n\t"
    "v_mfma_f32_16x16x32_bf16 v[56:59], a[136:139], a[60:63], v[56:59]\n\t"
    "v_mfma_f32_16x16x32_bf16 v[60:63], a[140:143], a[60:63], v[60:63]\n\t"
    "s_waitcnt lgkmcnt(11)\n\t"
    "v_mfma_f32_16x16x32_bf16 v[64:67], a[144:147], a[80:83], v[64:67]\n\t"
    "ds_read_b64_tr_b16 a[200:201], v239 offset:32896\n\t"
    "v_mfma_f32_16x16x32_bf16 v[68:71], a[148:151], a[80:83], v[68:71]\n\t"
    "ds_read_b64_tr_b16 a[202:203], v239 offset:33920\n\t"
    "v_mfma_f32_16x16x32_bf16 v[68:71], a[152:155], a[96:99], v[68:71]\n\t"
    "ds_read_b64_tr_b16 a[204:205], v239 offset:32904\n\t"
    "v_mfma_f32_16x16x32_bf16 v[64:67], a[156:159], a[96:99], v[64:67]\n\t"
    "ds_write_b32 v182, v218 offset:9216\n\t"
    "v_mfma_f32_16x16x32_bf16 v[64:67], a[160:163], a[112:115], v[64:67]\n\t"
    "ds_read_b64_tr_b16 a[206:207], v239 offset:33928\n\t"
    "v_cvt_pk_bf16_f32 v228, v56, v57\n\t"
    "v_mfma_f32_16x16x32_bf16 v[68:71], a[164:167], a[112:115], v[68:71]\n\t"
    "v_cvt_pk_bf16_f32 v229, v58, v59\n\t"
    "v_cvt_pk_bf16_f32 v230, v60, v61\n\t"
    "v_mfma_f32_16x16x32_bf16 v[68:71], a[168:171], a[0:3], v[68:71]\n\t"
    "ds_read_b64_tr_b16 a[208:209], v239 offset:41128\n\t"
    "v_cvt_pk_bf16_f32 v231, v62, v63\n\t"
    "v_mfma_f32_16x16x32_bf16 v[64:67], a[172:175], a[0:3], v[64:67]\n\t"
    "ds_write_b32 v182, v219 offset:13824\n\t"
    "v_mfma_f32_16x16x32_bf16 v[64:67], a[176:179], a[16:19], v[64:67]\n\t"
    "ds_read_b64_tr_b16 a[210:211], v239 offset:42152\n\t"
    "global_store_dwordx4 v241, v[228:231], s[46:47] offset:3072 sc1\n\t"
    "v_mfma_f32_16x16x32_bf16 v[68:71], a[180:183], a[16:19], v[68:71]\n\t"
    "s_waitcnt lgkmcnt(11)\n\t"
    "s_add_u32 s46, s46, 0x3000\n\t"
    "s_addc_u32 s47, s47, 0\n\t"
    "v_mfma_f32_16x16x32_bf16 v[68:71], a[184:187], a[32:35], v[68:71]\n\t"
    "ds_read_b64_tr_b16 a[212:213], v239 offset:41120\n\t"
    "v_mfma_f32_16x16x32_bf16 v[64:67], a[188:191], a[32:35], v[64:67]\n\t"
    "v_mfma_f32_16x16x32_bf16 v[64:67], a[192:195], a[48:51], v[64:67]\n\t"
    "s_waitcnt lgkmcnt(10)\n\t"
    "ds_read_b64_tr_b16 a[214:215], v239 offset:42144\n\t"
    "v_mfma_f32_16x16x32_bf16 v[68:71], a[196:199], a[48:51], v[68:71]\n\t"
    "v_mfma_f32_16x16x32_bf16 v[72:75], a[144:147], a[84:87], v[72:75]\n\t"
    "ds_read_b64_tr_b16 a[216:217], v239 offset:49344\n\t"
    "v_mfma_f32_16x16x32_bf16 v[76:79], a[148:151], a[84:87], v[76:79]\n\t"
    "ds_read_b64_tr_b16 a[218:219], v239 offset:50368\n\t"
    "v_mfma_f32_16x16x32_bf16 v[76:79], a[152:155], a[100:103], v[76:79]\n\t"
    "ds_read_b64_tr_b16 a[220:221], v239 offset:49352\n\t"
    "v_mfma_f32_16x16x32_bf16 v[72:75], a[156:159], a[100:103], v[72:75]\n\t"
    "s_waitcnt vmcnt(12)\n\t"
    "ds_write_b32 v183, v220 offset:0\n\t"
    "v_mfma_f32_16x16x32_bf16 v[72:75], a[160:163], a[116:119], v[72:75]\n\t"
    "ds_read_b64_tr_b16 a[222:223], v239 offset:50376\n\t"
    "v_cvt_pk_bf16_f32 v224, v64, v65\n\t"
    "v_mfma_f32_16x16x32_bf16 v[76:79], a[164:167], a[116:119], v[76:79]\n\t"
    "v_cvt_pk_bf16_f32 v225, v66, v67\n\t"
    "v_cvt_pk_bf16_f32 v226, v68, v69\n\t"
    "v_mfma_f32_16x16x32_bf16 v[76:79], a[168:171], a[4:7], v[76:79]\n\t"
    "ds_read_b64_tr_b16 a[128:129], v235 offset:8\n\t"
    "v_cvt_pk_bf16_f32 v227, v70, v71\n\t"
    "v_mfma_f32_16x16x32_bf16 v[72:75], a[172:175], a[4:7], v[72:75]\n\t"
    "ds_write_b32 v183, v221 offset:4608\n\t"
    "v_mfma_f32_16x16x32_bf16 v[72:75], a[176:179], a[20:23], v[72:75]\n\t"
    "ds_read_b64_tr_b16 a[130:131], v235 offset:1032\n\t"
    "global_store_dwordx4 v241, v[224:227], s[46:47] offset:0 sc1\n\t"
    "v_mfma_f32_16x16x32_bf16 v[76:79], a[180:183], a[20:23], v[76:79]\n\t"
    "v_mfma_f32_16x16x32_bf16 v[76:79], a[184:187], a[36:39], v[76:79]\n\t"
    "ds_read_b64_tr_b16 a[132:133], v235 offset:0\n\t"
    "v_mfma_f32_16x16x32_bf16 v[72:75], a[188:191], a[36:39], v[72:75]\n\t"
    "v_mfma_f32_16x16x32_bf16 v[72:75], a[192:195], a[52:55], v[72:75]\n\t"
    "ds_read_b64_tr_b16 a[134:135], v235 offset:1024\n\t"
    "v_mfma_f32_16x16x32_bf16 v[76:79], a[196:199], a[52:55], v[76:79]\n\t"
    "v_mfma_f32_16x16x32_bf16 v[80:83], a[144:147], a[88:91], v[80:83]\n\t"
    "ds_read_b64_tr_b16 a[136:137], v235 offset:8224\n\t"
    "v_mfma_f32_16x16x32_bf16 v[84:87], a[148:151], a[88:91], v[84:87]\n\t"
    "ds_read_b64_tr_b16 a[138:139], v235 offset:9248\n\t"
    "v_mfma_f32_16x16x32_bf16 v[84:87], a[152:155], a[104:107], v[84:87]\n\t"
    "ds_read_b64_tr_b16 a[140:141], v235 offset:8232\n\t"
    "v_mfma_f32_16x16x32_bf16 v[80:83], a[156:159], a[104:107], v[80:83]\n\t"
    "ds_write_b32 v183, v222 offset:9216\n\t"
    "v_mfma_f32_16x16x32_bf16 v[80:83], a[160:163], a[120:123], v[80:83]\n\t"
    "ds_read_b64_tr_b16 a[142:143], v235 offset:9256\n\t"
    "v_cvt_pk_bf16_f32 v228, v72, v73\n\t"
    "v_mfma_f32_16x16x32_bf16 v[84:87], a[164:167], a[120:123], v[84:87]\n\t"
    "ds_write_b32 v183, v223 offset:13824\n\t"
    "v_mfma_f32_16x16x32_bf16 v[84:87], a[168:171], a[8:11], v[84:87]\n\t"
    "s_mov_b32 s38, 0xff\n\t"
    "s_mov_b32 s39, 0\n\t"
    "v_cvt_pk_bf16_f32 v229, v74, v75\n\t"
    "v_mfma_f32_16x16x32_bf16 v[80:83], a[172:175], a[8:11], v[80:83]\n\t"
    "v_cvt_pk_bf16_f32 v230, v76, v77\n\t"
    "v_cvt_pk_bf16_f32 v231, v78, v79\n\t"
    "v_mfma_f32_16x16x32_bf16 v[80:83], a[176:179], a[24:27], v[80:83]\n\t"
    "s_mov_b32 s41, 0\n\t"
    "global_store_dwordx4 v241, v[228:231], s[46:47] offset:1024 sc1\n\t"
    "v_mfma_f32_16x16x32_bf16 v[84:87], a[180:183], a[24:27], v[84:87]\n\t"
    "v_mfma_f32_16x16x32_bf16 v[84:87], a[184:187], a[40:43], v[84:87]\n\t"
    "s_mov_b32 s37, 0x10000\n\t"
    "v_mfma_f32_16x16x32_bf16 v[80:83], a[188:191], a[40:43], v[80:83]\n\t"
    "v_mfma_f32_16x16x32_bf16 v[80:83], a[192:195], a[56:59], v[80:83]\n\t"
    "v_mfma_f32_16x16x32_bf16 v[84:87], a[196:199], a[56:59], v[84:87]\n\t"
    "v_mfma_f32_16x16x32_bf16 v[88:91], a[144:147], a[92:95], v[88:91]\n\t"
    "ds_read_b64_tr_b16 a[144:145], v235 offset:16456\n\t"
    "v_mfma_f32_16x16x32_bf16 v[92:95], a[148:151], a[92:95], v[92:95]\n\t"
    "ds_read_b64_tr_b16 a[146:147], v235 offset:17480\n\t"
    "v_mfma_f32_16x16x32_bf16 v[92:95], a[152:155], a[108:111], v[92:95]\n\t"
    "ds_read_b64_tr_b16 a[148:149], v235 offset:16448\n\t"
    "v_mfma_f32_16x16x32_bf16 v[88:91], a[156:159], a[108:111], v[88:91]\n\t"
    "v_cvt_pk_bf16_f32 v224, v80, v81\n\t"
    "v_cvt_pk_bf16_f32 v225, v82, v83\n\t"
    "v_mfma_f32_16x16x32_bf16 v[88:91], a[160:163], a[124:127], v[88:91]\n\t"
    "ds_read_b64_tr_b16 a[150:151], v235 offset:17472\n\t"
    "v_cvt_pk_bf16_f32 v226, v84, v85\n\t"
    "v_mfma_f32_16x16x32_bf16 v[92:95], a[164:167], a[124:127], v[92:95]\n\t"
    "ds_read_b64_tr_b16 a[152:153], v235 offset:24672\n\t"
    "v_cvt_pk_bf16_f32 v227, v86, v87\n\t"
    "v_mfma_f32_16x16x32_bf16 v[92:95], a[168:171], a[12:15], v[92:95]\n\t"
    "ds_read_b64_tr_b16 a[154:155], v235 offset:25696\n\t"
    "global_store_dwordx4 v241, v[224:227], s[46:47] offset:2048 sc1\n\t"
    "v_mfma_f32_16x16x32_bf16 v[88:91], a[172:175], a[12:15], v[88:91]\n\t"
    "ds_read_b64_tr_b16 a[156:157], v235 offset:24680\n\t"
    "v_mfma_f32_16x16x32_bf16 v[88:91], a[176:179], a[28:31], v[88:91]\n\t"
    "ds_read_b64_tr_b16 a[158:159], v235 offset:25704\n\t"
    "v_mfma_f32_16x16x32_bf16 v[92:95], a[180:183], a[28:31], v[92:95]\n\t"
    "v_mfma_f32_16x16x32_bf16 v[92:95], a[184:187], a[44:47], v[92:95]\n\t"
    "v_mfma_f32_16x16x32_bf16 v[88:91], a[188:191], a[44:47], v[88:91]\n\t"
    "v_mfma_f32_16x16x32_bf16 v[88:91], a[192:195], a[60:63], v[88:91]\n\t"
    "v_mfma_f32_16x16x32_bf16 v[92:95], a[196:199], a[60:63], v[92:95]\n\t"
    "s_waitcnt lgkmcnt(0)\n\t"
    "v_mfma_f32_16x16x32_bf16 v[96:99], a[200:203], a[80:83], v[96:99]\n\t"
    "v_mfma_f32_16x16x32_bf16 v[100:103], a[204:207], a[80:83], v[100:103]\n\t"
    "v_mfma_f32_16x16x32_bf16 v[100:103], a[208:211], a[96:99], v[100:103]\n\t"
    "v_mfma_f32_16x16x32_bf16 v[96:99], a[212:215], a[96:99], v[96:99]\n\t"
    "s_nop 1\n\t"
    "v_cvt_pk_bf16_f32 v228, v88, v89\n\t"
    "v_cvt_pk_bf16_f32 v229, v90, v91\n\t"
    "v_mfma_f32_16x16x32_bf16 v[96:99], a[216:219], a[112:115], v[96:99]\n\t"
    "v_cvt_pk_bf16_f32 v230, v92, v93\n\t"
    "v_cvt_pk_bf16_f32 v231, v94, v95\n\t"
    "v_mfma_f32_16x16x32_bf16 v[100:103], a[220:223], a[112:115], v[100:103]\n\t"
    "global_store_dwordx4 v241, v[228:231], s[46:47] offset:3072 sc1\n\t"
    "s_add_u32 s46, s46, 0x1000\n\t"
    "s_addc_u32 s47, s47, 0\n\t"
    "v_mfma_f32_16x16x32_bf16 v[100:103], a[128:131], a[0:3], v[100:103]\n\t"
    "s_barrier\n\t"
    "v_mfma_f32_16x16x32_bf16 v[96:99], a[132:135], a[0:3], v[96:99]\n\t"
    "v_mfma_f32_16x16x32_bf16 v[96:99], a[136:139], a[16:19], v[96:99]\n\t"
    "v_mfma_f32_16x16x32_bf16 v[100:103], a[140:143], a[16:19], v[100:103]\n\t"
    "v_mfma_f32_16x16x32_bf16 v[100:103], a[144:147], a[32:35], v[100:103]\n\t"
    "v_mfma_f32_16x16x32_bf16 v[96:99], a[148:151], a[32:35], v[96:99]\n\t"
    "v_mfma_f32_16x16x32_bf16 v[96:99], a[152:155], a[48:51], v[96:99]\n\t"
    "v_mfma_f32_16x16x32_bf16 v[100:103], a[156:159], a[48:51], v[100:103]\n\t"
    "v_mfma_f32_16x16x32_bf16 v[104:107], a[200:203], a[84:87], v[104:107]\n\t"
    "ds_read_b128 v[208:211], v184 offset:0\n\t"
    "v_mfma_f32_16x16x32_bf16 v[108:111], a[204:207], a[84:87], v[108:111]\n\t"
    "v_mfma_f32_16x16x32_bf16 v[108:111], a[208:211], a[100:103], v[108:111]\n\t"
    "v_mfma_f32_16x16x32_bf16 v[104:107], a[212:215], a[100:103], v[104:107]\n\t"
    "ds_read_b128 v[212:215], v184 offset:32\n\t"
    "s_nop 0\n\t"
    "v_cvt_pk_bf16_f32 v224, v96, v97\n\t"
    "v_mfma_f32_16x16x32_bf16 v[104:107], a[216:219], a[116:119], v[104:107]\n\t"
    "v_cvt_pk_bf16_f32 v225, v98, v99\n\t"
    "v_cvt_pk_bf16_f32 v226, v100, v101\n\t"
    "v_mfma_f32_16x16x32_bf16 v[108:111], a[220:223], a[116:119], v[108:111]\n\t"
    "v_cvt_pk_bf16_f32 v227, v102, v103\n\t"
    "v_mfma_f32_16x16x32_bf16 v[108:111], a[128:131], a[4:7], v[108:111]\n\t"
    "ds_read_b128 v[216:219], v184 offset:64\n\t"
    "global_store_dwordx4 v241, v[224:227], s[46:47] offset:0 sc1\n\t"
    "v_mfma_f32_16x16x32_bf16 v[104:107], a[132:135], a[4:7], v[104:107]\n\t"
    "v_mfma_f32_16x16x32_bf16 v[104:107], a[136:139], a[20:23], v[104:107]\n\t"
    "v_mfma_f32_16x16x32_bf16 v[108:111], a[140:143], a[20:23], v[108:111]\n\t"
    "ds_read_b128 v[220:223], v184 offset:96\n\t"
    "v_mfma_f32_16x16x32_bf16 v[108:111], a[144:147], a[36:39], v[108:111]\n\t"
    "v_mfma_f32_16x16x32_bf16 v[104:107], a[148:151], a[36:39], v[104:107]\n\t"
    "v_mfma_f32_16x16x32_bf16 v[104:107], a[152:155], a[52:55], v[104:107]\n\t"
    "v_mfma_f32_16x16x32_bf16 v[108:111], a[156:159], a[52:55], v[108:111]\n\t"
    "v_mfma_f32_16x16x32_bf16 v[112:115], a[200:203], a[88:91], v[112:115]\n\t"
    "v_mfma_f32_16x16x32_bf16 v[116:119], a[204:207], a[88:91], v[116:119]\n\t"
    "v_mfma_f32_16x16x32_bf16 v[116:119], a[208:211], a[104:107], v[116:119]\n\t"
    "v_mfma_f32_16x16x32_bf16 v[112:115], a[212:215], a[104:107], v[112:115]\n\t"
    "s_nop 2\n\t"
    "v_cvt_pk_bf16_f32 v228, v104, v105\n\t"
    "v_cvt_pk_bf16_f32 v229, v106, v107\n\t"
    "v_mfma_f32_16x16x32_bf16 v[112:115], a[216:219], a[120:123], v[112:115]\n\t"
    "v_cvt_pk_bf16_f32 v230, v108, v109\n\t"
    "v_cvt_pk_bf16_f32 v231, v110, v111\n\t"
    "v_mfma_f32_16x16x32_bf16 v[116:119], a[220:223], a[120:123], v[116:119]\n\t"
    "global_store_dwordx4 v241, v[228:231], s[46:47] offset:1024 sc1\n\t"
    "v_mfma_f32_16x16x32_bf16 v[116:119], a[128:131], a[8:11], v[116:119]\n\t"
    "v_mfma_f32_16x16x32_bf16 v[112:115], a[132:135], a[8:11], v[112:115]\n\t"
    "v_mfma_f32_16x16x32_bf16 v[112:115], a[136:139], a[24:27], v[112:115]\n\t"
    "v_mfma_f32_16x16x32_bf16 v[116:119], a[140:143], a[24:27], v[116:119]\n\t"
    "v_mfma_f32_16x16x32_bf16 v[116:119], a[144:147], a[40:43], v[116:119]\n\t"
    "v_mfma_f32_16x16x32_bf16 v[112:115], a[148:151], a[40:43], v[112:115]\n\t"
    "v_mfma_f32_16x16x32_bf16 v[112:115], a[152:155], a[56:59], v[112:115]\n\t"
    "v_mfma_f32_16x16x32_bf16 v[116:119], a[156:159], a[56:59], v[116:119]\n\t"
    "v_mfma_f32_16x16x32_bf16 v[120:123], a[200:203], a[92:95], v[120:123]\n\t"
    "s_waitcnt lgkmcnt(1)\n\t"
    "v_alignbit_b32 v216, v216, v216, 16\n\t"
    "v_mfma_f32_16x16x32_bf16 v[124:127], a[204:207], a[92:95], v[124:127]\n\t"
    "v_alignbit_b32 v217, v217, v217, 16\n\t"
    "v_mfma_f32_16x16x32_bf16 v[124:127], a[208:211], a[108:111], v[124:127]\n\t"
    "v_mfma_f32_16x16x32_bf16 v[120:123], a[212:215], a[108:111], v[120:123]\n\t"
    "v_alignbit_b32 v218, v218, v218, 16\n\t"
    "v_cvt_pk_bf16_f32 v224, v112, v113\n\t"
    "v_mfma_f32_16x16x32_bf16 v[120:123], a[216:219], a[124:127], v[120:123]\n\t"
    "v_alignbit_b32 v219, v219, v219, 16\n\t"
    "v_cvt_pk_bf16_f32 v225, v114, v115\n\t"
    "v_mfma_f32_16x16x32_bf16 v[124:127], a[220:223], a[124:127], v[124:127]\n\t"
    "v_cvt_pk_bf16_f32 v226, v116, v117\n\t"
    "v_cvt_pk_bf16_f32 v227, v118, v119\n\t"
    "v_mfma_f32_16x16x32_bf16 v[124:127], a[128:131], a[12:15], v[124:127]\n\t"
    "s_waitcnt lgkmcnt(0)\n\t"
    "v_alignbit_b32 v220, v220, v220, 16\n\t"
    "v_mfma_f32_16x16x32_bf16 v[120:123], a[132:135], a[12:15], v[120:123]\n\t"
    "v_alignbit_b32 v221, v221, v221, 16\n\t"
    "v_mfma_f32_16x16x32_bf16 v[120:123], a[136:139], a[28:31], v[120:123]\n\t"
    "global_store_dwordx4 v241, v[224:227], s[46:47] offset:2048 sc1\n\t"
    "v_mfma_f32_16x16x32_bf16 v[124:127], a[140:143], a[28:31], v[124:127]\n\t"
    "v_alignbit_b32 v222, v222, v222, 16\n\t"
    "v_mfma_f32_16x16x32_bf16 v[124:127], a[144:147], a[44:47], v[124:127]\n\t"
    "v_alignbit_b32 v223, v223, v223, 16\n\t"
    "v_mfma_f32_16x16x32_bf16 v[120:123], a[148:151], a[44:47], v[120:123]\n\t"
    "v_mfma_f32_16x16x32_bf16 v[120:123], a[152:155], a[60:63], v[120:123]\n\t"
    "v_mfma_f32_16x16x32_bf16 v[124:127], a[156:159], a[60:63], v[124:127]\n\t"
    "v_mfma_f32_32x32x16_f16 v[64:79], v[208:211], a[224:227], 0\n\t"
    "v_and_b32 v152, 31, v245\n\t"
    "v_lshrrev_b32 v153, 5, v245\n\t"
    "s_mul_i32 s34, s22, 4224\n\t"
    "s_add_u32 s34, s34, 133632\n\t"
    "v_lshl_add_u32 v154, v152, 6, s34\n\t"
    "s_nop 0\n\t"
    "v_cvt_pk_bf16_f32 v228, v120, v121\n\t"
    "v_mfma_f32_32x32x16_f16 v[80:95], v[208:211], a[240:243], 0\n\t"
    "v_cvt_pk_bf16_f32 v229, v122, v123\n\t"
    "v_cvt_pk_bf16_f32 v230, v124, v125\n\t"
    "v_cvt_pk_bf16_f32 v231, v126, v127\n\t"
    "global_store_dwordx4 v241, v[228:231], s[46:47] offset:3072 sc1\n\t"
    "v_lshl_add_u32 v154, v153, 3, v154\n\t"
    "v_bfe_u32 v155, v152, 1, 2\n\t"
    "v_mfma_f32_32x32x16_f16 v[64:79], v[212:215], a[228:231], v[64:79]\n\t"
    "v_xor_b32 v156, 0, v155\n\t"
    "v_lshl_add_u32 v144, v156, 4, v154\n\t"
    "v_xor_b32 v156, 1, v155\n\t"
    "v_lshl_add_u32 v145, v156, 4, v154\n\t"
    "v_xor_b32 v156, 2, v155\n\t"
    "v_lshl_add_u32 v146, v156, 4, v154\n\t"
    "v_mfma_f32_32x32x16_f16 v[80:95], v[212:215], a[244:247], v[80:95]\n\t"
    "v_xor_b32 v156, 3, v155\n\t"
    "v_lshl_add_u32 v147, v156, 4, v154\n\t"
    "v_bfe_u32 v155, v245, 2, 3\n\t"
    "v_mul_u32_u24 v154, 2112, v155\n\t"
    "s_lshl_b32 s34, s22, 9\n\t"
    "s_add_u32 s34, s34, 133632\n\t"
    "v_mfma_f32_32x32x16_f16 v[64:79], v[216:219], a[232:235], v[64:79]\n\t"
    "v_lshl_add_u32 v154, v153, 8, v154\n\t"
    "v_add_u32 v154, s34, v154\n\t"
    "v_and_b32 v156, 3, v245\n\t"
    "v_lshlrev_b32 v157, 1, v153\n\t"
    "v_xor_b32 v157, v157, v156\n\t"
    "v_lshl_add_u32 v148, v157, 4, v154\n\t"
    "v_mfma_f32_32x32x16_f16 v[80:95], v[216:219], a[248:251], v[80:95]\n\t"
    "v_xor_b32 v157, 1, v157\n\t"
    "v_lshl_add_u32 v149, v157, 4, v154\n\t"
    "v_mfma_f32_32x32x16_f16 v[64:79], v[220:223], a[236:239], v[64:79]\n\t"
    "v_mfma_f32_32x32x16_f16 v[80:95], v[220:223], a[252:255], v[80:95]\n\t"
    "v_mov_b64 v[250:251], 0\n\t"
    "s_mov_b64 exec, s[38:39]\n\t"
    "global_atomic_or_x2 v[250:251], v249, v[250:251], s[20:21] sc0 sc1\n\t"
    "s_mov_b64 exec, -1\n\t"
    "s_nop 7\n\t"
    "v_cvt_pk_f16_f32 v224, v64, v80\n\t"
    "v_cvt_pk_f16_f32 v225, v65, v81\n\t"
    "v_pk_mul_f16 v176, v224, v160 op_sel_hi:[1,0]\n\t"
    "v_pk_mul_f16 v177, v225, v161 op_sel_hi:[1,0]\n\t"
    "v_pk_fma_f16 v176, v224, v160, v176 op_sel:[1,1,0] op_sel_hi:[0,1,1] neg_hi:[1,0,0]\n\t"
    "v_pk_fma_f16 v177, v225, v161, v177 op_sel:[1,1,0] op_sel_hi:[0,1,1] neg_hi:[1,0,0]\n\t"
    "v_cvt_pk_f16_f32 v226, v66, v82\n\t"
    "v_cvt_pk_f16_f32 v227, v67, v83\n\t"
    "v_pk_mul_f16 v178, v226, v162 op_sel_hi:[1,0]\n\t"
    "v_pk_mul_f16 v179, v227, v163 op_sel_hi:[1,0]\n\t"
    "v_pk_fma_f16 v178, v226, v162, v178 op_sel:[1,1,0] op_sel_hi:[0,1,1] neg_hi:[1,0,0]\n\t"
    "v_pk_fma_f16 v179, v227, v163, v179 op_sel:[1,1,0] op_sel_hi:[0,1,1] neg_hi:[1,0,0]\n\t"
    "v_cvt_pk_f16_f32 v224, v68, v84\n\t"
    "v_cvt_pk_f16_f32 v225, v69, v85\n\t"
    "v_mfma_f32_32x32x16_f16 v[96:111], v[176:179], a[224:227], 0\n\t"
    "v_pk_mul_f16 v180, v224, v164 op_sel_hi:[1,0]\n\t"
    "v_pk_mul_f16 v181, v225, v165 op_sel_hi:[1,0]\n\t"
    "v_pk_fma_f16 v180, v224, v164, v180 op_sel:[1,1,0] op_sel_hi:[0,1,1] neg_hi:[1,0,0]\n\t"
    "v_pk_fma_f16 v181, v225, v165, v181 op_sel:[1,1,0] op_sel_hi:[0,1,1] neg_hi:[1,0,0]\n\t"
    "v_cvt_pk_f16_f32 v226, v70, v86\n\t"
    "v_cvt_pk_f16_f32 v227, v71, v87\n\t"
    "v_mfma_f32_32x32x16_f16 v[112:127], v[176:179], a[240:243], 0\n\t"
    "Lpad11_%=:\n\t"
    "s_cbranch_execz LretB_%=\n\t"
    "v_pk_mul_f16 v182, v226, v166 op_sel_hi:[1,0]\n\t"
    "v_pk_mul_f16 v183, v227, v167 op_sel_hi:[1,0]\n\t"
    "v_pk_fma_f16 v182, v226, v166, v182 op_sel:[1,1,0] op_sel_hi:[0,1,1] neg_hi:[1,0,0]\n\t"
    "v_pk_fma_f16 v183, v227, v167, v183 op_sel:[1,1,0] op_sel_hi:[0,1,1] neg_hi:[1,0,0]\n\t"
    "v_cvt_pk_f16_f32 v224, v72, v88\n\t"
    "v_cvt_pk_f16_f32 v225, v73, v89\n\t"
    "v_mfma_f32_32x32x16_f16 v[96:111], v[180:183], a[228:231], v[96:111]\n\t"
    "v_pk_mul_f16 v184, v224, v168 op_sel_hi:[1,0]\n\t"
    "v_pk_mul_f16 v185, v225, v169 op_sel_hi:[1,0]\n\t"
    "v_pk_fma_f16 v184, v224, v168, v184 op_sel:[1,1,0] op_sel_hi:[0,1,1] neg_hi:[1,0,0]\n\t"
    "v_pk_fma_f16 v185, v225, v169, v185 op_sel:[1,1,0] op_sel_hi:[0,1,1] neg_hi:[1,0,0]\n\t"
    "v_cvt_pk_f16_f32 v226, v74, v90\n\t"
    "v_cvt_pk_f16_f32 v227, v75, v91\n\t"
    "v_mfma_f32_32x32x16_f16 v[112:127], v[180:183], a[244:247], v[112:127]\n\t"
    "v_pk_mul_f16 v186, v226, v170 op_sel_hi:[1,0]\n\t"
    "v_pk_mul_f16 v187, v227, v171 op_sel_hi:[1,0]\n\t"
    "v_pk_fma_f16 v186, v226, v170, v186 op_sel:[1,1,0] op_sel_hi:[0,1,1] neg_hi:[1,0,0]\n\t"
    "v_pk_fma_f16 v187, v227, v171, v187 op_sel:[1,1,0] op_sel_hi:[0,1,1] neg_hi:[1,0,0]\n\t"
    "v_cvt_pk_f16_f32 v224, v76, v92\n\t"
    "v_cvt_pk_f16_f32 v225, v77, v93\n\t"
    "v_mfma_f32_32x32x16_f16 v[96:111], v[184:187], a[232:235], v[96:111]\n\t"
    "v_pk_mul_f16 v188, v224, v172 op_sel_hi:[1,0]\n\t"
    "v_pk_mul_f16 v189, v225, v173 op_sel_hi:[1,0]\n\t"
    "v_pk_fma_f16 v188, v224, v172, v188 op_sel:[1,1,0] op_sel_hi:[0,1,1] neg_hi:[1,0,0]\n\t"
    "v_pk_fma_f16 v189, v225, v173, v189 op_sel:[1,1,0] op_sel_hi:[0,1,1] neg_hi:[1,0,0]\n\t"
    "v_cvt_pk_f16_f32 v226, v78, v94\n\t"
    "v_cvt_pk_f16_f32 v227, v79, v95\n\t"
    "v_mfma_f32_32x32x16_f16 v[112:127], v[184:187], a[248:251], v[112:127]\n\t"
    "v_pk_mul_f16 v190, v226, v174 op_sel_hi:[1,0]\n\t"
    "v_pk_mul_f16 v191, v227, v175 op_sel_hi:[1,0]\n\t"
    "v_pk_fma_f16 v190, v226, v174, v190 op_sel:[1,1,0] op_sel_hi:[0,1,1] neg_hi:[1,0,0]\n\t"
    "v_pk_fma_f16 v191, v227, v175, v191 op_sel:[1,1,0] op_sel_hi:[0,1,1] neg_hi:[1,0,0]\n\t"
    "s_nop 1\n\t"
    "v_mfma_f32_32x32x16_f16 v[96:111], v[188:191], a[236:239], v[96:111]\n\t"
    "v_lshlrev_b32 v64, 16, v192\n\t"
    "v_and_b32 v65, 0xffff0000, v192\n\t"
    "v_lshlrev_b32 v66, 16, v193\n\t"
    "v_and_b32 v67, 0xffff0000, v193\n\t"
    "v_lshlrev_b32 v68, 16, v194\n\t"
    "v_and_b32 v69, 0xffff0000, v194\n\t"
    "v_mfma_f32_32x32x16_f16 v[112:127], v[188:191], a[252:255], v[112:127]\n\t"
    "v_lshlrev_b32 v70, 16, v195\n\t"
    "v_and_b32 v71, 0xffff0000, v195\n\t"
    "v_lshlrev_b32 v72, 16, v196\n\t"
    "v_and_b32 v73, 0xffff0000, v196\n\t"
    "v_lshlrev_b32 v74, 16, v197\n\t"
    "v_and_b32 v75, 0xffff0000, v197\n\t"
    "v_lshlrev_b32 v76, 16, v198\n\t"
    "v_and_b32 v77, 0xffff0000, v198\n\t"
    "v_lshlrev_b32 v78, 16, v199\n\t"
    "v_and_b32 v79, 0xffff0000, v199\n\t"
    "v_lshlrev_b32 v80, 16, v200\n\t"
    "v_and_b32 v81, 0xffff0000, v200\n\t"
    "s_waitcnt vmcnt(1)\n\t"
    "global_store_dwordx2 v248, v[254:255], s[4:5] offset:128 sc1\n\t"
    "s_waitcnt vmcnt(1)\n\t"
    "v_cmp_eq_u64 vcc, s[74:75], v[250:251]\n\t"
    "s_sub_u32 s37, s37, 1\n\t"
    "s_cselect_b32 s40, -1, 0\n\t"
    "s_or_b32 s40, s40, vcc_lo\n\t"
    "s_and_b32 s40, s40, s38\n\t"
    "s_lshr_b32 vcc_lo, s40, 1\n\t"
    "s_and_b32 s40, s40, vcc_lo\n\t"
    "s_and_b32 s40, s40, 0x55555555\n\t"
    "s_bitcmp1_b32 s40, 0\n\t"
    "s_cbranch_scc0 Lk29_%=\n\t"
    "global_load_dwordx4 v[0:3], v244, s[50:51] offset:-4096 sc1\n\t"
    "global_load_dwordx4 v[16:19], v244, s[50:51] offset:0 sc1\n\t"
    "global_load_dwordx4 v[32:35], v243, s[50:51] offset:-4096 sc1\n\t"
    "global_load_dwordx4 v[48:51], v243, s[50:51] offset:0 sc1\n\t"
    "Lk29_%=:\n\t"
    "s_bitcmp1_b32 s40, 2\n\t"
    "s_cbranch_scc0 Lk30_%=\n\t"
    "global_load_dwordx4 v[4:7], v244, s[52:53] offset:-4096 sc1\n\t"
    "global_load_dwordx4 v[20:23], v244, s[52:53] offset:0 sc1\n\t"
    "global_load_dwordx4 v[36:39], v243, s[52:53] offset:-4096 sc1\n\t"
    "global_load_dwordx4 v[52:55], v243, s[52:53] offset:0 sc1\n\t"
    "Lk30_%=:\n\t"
    "s_bitcmp1_b32 s40, 4\n\t"
    "s_cbranch_scc0 Lk31_%=\n\t"
    "global_load_dwordx4 v[8:11], v244, s[54:55] offset:-4096 sc1\n\t"
    "global_load_dwordx4 v[24:27], v244, s[54:55] offset:0 sc1\n\t"
    "global_load_dwordx4 v[40:43], v243, s[54:55] offset:-4096 sc1\n\t"
    "global_load_dwordx4 v[56:59], v243, s[54:55] offset:0 sc1\n\t"
    "Lk31_%=:\n\t"
    "s_bitcmp1_b32 s40, 6\n\t"
    "s_cbranch_scc0 Lk32_%=\n\t"
    "global_load_dwordx4 v[12:15], v244, s[56:57] offset:-4096 sc1\n\t"
    "global_load_dwordx4 v[28:31], v244, s[56:57] offset:0 sc1\n\t"
    "global_load_dwordx4 v[44:47], v243, s[56:57] offset:-4096 sc1\n\t"
    "global_load_dwordx4 v[60:63], v243, s[56:57] offset:0 sc1\n\t"
    "Lk32_%=:\n\t"
    "s_mul_i32 s40, s40, 3\n\t"
    "s_andn2_b32 s38, s38, s40\n\t"
    "s_cbranch_scc0 Lk28_%=\n\t"
    "v_mov_b64 v[250:251], 0\n\t"
    "s_mov_b64 exec, s[38:39]\n\t"
    "global_atomic_or_x2 v[250:251], v249, v[250:251], s[20:21] sc0 sc1\n\t"
    "s_mov_b64 exec, -1\n\t"
    "Lk28_%=:\n\t"
    "v_pk_mul_f32 v[224:225], v[112:113], s[62:63]\n\t"
    "v_pk_mul_f32 v[226:227], v[114:115], s[62:63]\n\t"
    "v_cvt_pk_bf16_f32 v224, v224, v225\n\t"
    "v_cvt_pk_bf16_f32 v225, v226, v227\n\t"
    "ds_write_b64 v144, v[224:225] offset:0\n\t"
    "v_pk_mul_f32 v[228:229], v[96:97], s[62:63]\n\t"
    "v_pk_mul_f32 v[230:231], v[98:99], s[62:63]\n\t"
    "v_cvt_pk_bf16_f32 v228, v228, v229\n\t"
    "v_cvt_pk_bf16_f32 v229, v230, v231\n\t"
    "ds_write_b64 v144, v[228:229] offset:2112\n\t"
    "v_pk_mul_f32 v[224:225], v[116:117], s[62:63]\n\t"
    "v_pk_mul_f32 v[226:227], v[118:119], s[62:63]\n\t"
    "v_cvt_pk_bf16_f32 v224, v224, v225\n\t"
    "v_cvt_pk_bf16_f32 v225, v226, v227\n\t"
    "ds_write_b64 v145, v[224:225] offset:0\n\t"
    "v_pk_mul_f32 v[228:229], v[100:101], s[62:63]\n\t"
    "v_pk_mul_f32 v[230:231], v[102:103], s[62:63]\n\t"
    "v_cvt_pk_bf16_f32 v228, v228, v229\n\t"
    "v_cvt_pk_bf16_f32 v229, v230, v231\n\t"
    "ds_write_b64 v145, v[228:229] offset:2112\n\t"
    "v_pk_mul_f32 v[224:225], v[120:121], s[62:63]\n\t"
    "v_pk_mul_f32 v[226:227], v[122:123], s[62:63]\n\t"
    "v_cvt_pk_bf16_f32 v224, v224, v225\n\t"
    "v_cvt_pk_bf16_f32 v225, v226, v227\n\t"
    "ds_write_b64 v146, v[224:225] offset:0\n\t"
    "v_pk_mul_f32 v[228:229], v[104:105], s[62:63]\n\t"
    "v_pk_mul_f32 v[230:231], v[106:107], s[62:63]\n\t"
    "v_cvt_pk_bf16_f32 v228, v228, v229\n\t"
    "v_cvt_pk_bf16_f32 v229, v230, v231\n\t"
    "ds_write_b64 v146, v[228:229] offset:2112\n\t"
    "v_pk_mul_f32 v[224:225], v[124:125], s[62:63]\n\t"
    "v_pk_mul_f32 v[226:227], v[126:127], s[62:63]\n\t"
    "v_cvt_pk_bf16_f32 v224, v224, v225\n\t"
    "v_cvt_pk_bf16_f32 v225, v226, v227\n\t"
    "ds_write_b64 v147, v[224:225] offset:0\n\t"
    "v_pk_mul_f32 v[228:229], v[108:109], s[62:63]\n\t"
    "v_pk_mul_f32 v[230:231], v[110:111], s[62:63]\n\t"
    "v_cvt_pk_bf16_f32 v228, v228, v229\n\t"
    "v_cvt_pk_bf16_f32 v229, v230, v231\n\t"
    "ds_write_b64 v147, v[228:229] offset:2112\n\t"
    "v_lshlrev_b32 v82, 16, v201\n\t"
    "v_and_b32 v83, 0xffff0000, v201\n\t"
    "v_lshlrev_b32 v84, 16, v202\n\t"
    "v_and_b32 v85, 0xffff0000, v202\n\t"
    "v_lshlrev_b32 v86, 16, v203\n\t"
    "v_and_b32 v87, 0xffff0000, v203\n\t"
    "v_lshlrev_b32 v88, 16, v204\n\t"
    "v_and_b32 v89, 0xffff0000, v204\n\t"
    "v_lshlrev_b32 v90, 16, v205\n\t"
    "v_and_b32 v91, 0xffff0000, v205\n\t"
    "v_lshlrev_b32 v92, 16, v206\n\t"
    "v_and_b32 v93, 0xffff0000, v206\n\t"
    "v_lshlrev_b32 v94, 16, v207\n\t"
    "v_and_b32 v95, 0xffff0000, v207\n\t"
    "s_waitcnt lgkmcnt(0)\n\t"
    "s_barrier\n\t"
    "ds_read_b128 v[208:211], v148 offset:0\n\t"
    "ds_read_b128 v[212:215], v148 offset:64\n\t"
    "ds_read_b128 v[216:219], v149 offset:128\n\t"
    "ds_read_b128 v[220:223], v149 offset:192\n\t"
    "v_lshlrev_b32 v96, 16, v128\n\t"
    "v_and_b32 v97, 0xffff0000, v128\n\t"
    "v_lshlrev_b32 v98, 16, v129\n\t"
    "v_and_b32 v99, 0xffff0000, v129\n\t"
    "v_lshlrev_b32 v100, 16, v130\n\t"
    "v_and_b32 v101, 0xffff0000, v130\n\t"
    "v_lshlrev_b32 v102, 16, v131\n\t"
    "v_and_b32 v103, 0xffff0000, v131\n\t"
    "v_lshlrev_b32 v104, 16, v132\n\t"
    "v_and_b32 v105, 0xffff0000, v132\n\t"
    "v_lshlrev_b32 v106, 16, v133\n\t"
    "v_and_b32 v107, 0xffff0000, v133\n\t"
    "v_lshlrev_b32 v108, 16, v134\n\t"
    "v_and_b32 v109, 0xffff0000, v134\n\t"
    "v_lshlrev_b32 v110, 16, v135\n\t"
    "v_and_b32 v111, 0xffff0000, v135\n\t"
    "v_lshlrev_b32 v112, 16, v136\n\t"
    "v_and_b32 v113, 0xffff0000, v136\n\t"
    "v_lshlrev_b32 v114, 16, v137\n\t"
    "v_and_b32 v115, 0xffff0000, v137\n\t"
    "v_lshlrev_b32 v116, 16, v138\n\t"
    "v_and_b32 v117, 0xffff0000, v138\n\t"
    "v_lshlrev_b32 v118, 16, v139\n\t"
    "v_and_b32 v119, 0xffff0000, v139\n\t"
    "v_lshlrev_b32 v120, 16, v140\n\t"
    "v_and_b32 v121, 0xffff0000, v140\n\t"
    "v_lshlrev_b32 v122, 16, v141\n\t"
    "v_and_b32 v123, 0xffff0000, v141\n\t"
    "v_lshlrev_b32 v124, 16, v142\n\t"
    "v_and_b32 v125, 0xffff0000, v142\n\t"
    "v_lshlrev_b32 v126, 16, v143\n\t"
    "v_and_b32 v127, 0xffff0000, v143\n\t"
    "s_waitcnt lgkmcnt(3)\n\t"
    "v_lshlrev_b32 v128, 16, v208\n\t"
    "v_and_b32 v129, 0xffff0000, v208\n\t"
    "v_lshlrev_b32 v130, 16, v209\n\t"
    "v_and_b32 v131, 0xffff0000, v209\n\t"
    "v_lshlrev_b32 v132, 16, v210\n\t"
    "v_and_b32 v133, 0xffff0000, v210\n\t"
    "v_lshlrev_b32 v134, 16, v211\n\t"
    "v_and_b32 v135, 0xffff0000, v211\n\t"
    "s_waitcnt lgkmcnt(2)\n\t"
    "v_lshlrev_b32 v136, 16, v212\n\t"
    "v_and_b32 v137, 0xffff0000, v212\n\t"
    "v_lshlrev_b32 v138, 16, v213\n\t"
    "v_and_b32 v139, 0xffff0000, v213\n\t"
    "v_lshlrev_b32 v140, 16, v214\n\t"
    "v_and_b32 v141, 0xffff0000, v214\n\t"
    "v_lshlrev_b32 v142, 16, v215\n\t"
    "v_and_b32 v143, 0xffff0000, v215\n\t"
    "s_waitcnt lgkmcnt(1)\n\t"
    "v_lshlrev_b32 v144, 16, v216\n\t"
    "v_and_b32 v145, 0xffff0000, v216\n\t"
    "v_lshlrev_b32 v146, 16, v217\n\t"
    "v_and_b32 v147, 0xffff0000, v217\n\t"
    "v_lshlrev_b32 v148, 16, v218\n\t"
    "v_and_b32 v149, 0xffff0000, v218\n\t"
    "v_lshlrev_b32 v150, 16, v219\n\t"
    "v_and_b32 v151, 0xffff0000, v219\n\t"
    "s_waitcnt lgkmcnt(0)\n\t"
    "v_lshlrev_b32 v152, 16, v220\n\t"
    "v_and_b32 v153, 0xffff0000, v220\n\t"
    "v_lshlrev_b32 v154, 16, v221\n\t"
    "v_and_b32 v155, 0xffff0000, v221\n\t"
    "v_lshlrev_b32 v156, 16, v222\n\t"
    "v_and_b32 v157, 0xffff0000, v222\n\t"
    "v_lshlrev_b32 v158, 16, v223\n\t"
    "v_and_b32 v159, 0xffff0000, v223\n\t"
    "s_cmp_eq_u32 s38, 0\n\t"
    "s_cbranch_scc1 Le26_%=\n\t"
    "s_branch Lw25_%=\n\t"
    "Lt24_%=:\n\t"
    "v_mov_b64 v[250:251], 0\n\t"
    "s_mov_b64 exec, s[38:39]\n\t"
    "global_atomic_or_x2 v[250:251], v249, v[250:251], s[20:21] sc0 sc1\n\t"
    "s_mov_b64 exec, -1\n\t"
    "Lw25_%=:\n\t"
    "s_waitcnt vmcnt(0)\n\t"
    "v_cmp_eq_u64 vcc, s[74:75], v[250:251]\n\t"
    "s_sub_u32 s37, s37, 1\n\t"
    "s_cselect_b32 s40, -1, 0\n\t"
    "s_or_b32 s40, s40, vcc_lo\n\t"
    "s_and_b32 s40, s40, s38\n\t"
    "s_lshr_b32 vcc_lo, s40, 1\n\t"
    "s_and_b32 s40, s40, vcc_lo\n\t"
    "s_and_b32 s40, s40, 0x55555555\n\t"
    "s_cbranch_scc1 Ln27_%=\n\t"
    "s_sleep 2\n\t"
    "s_branch Lt24_%=\n\t"
    "Ln27_%=:\n\t"
    "s_bitcmp1_b32 s40, 0\n\t"
    "s_cbranch_scc0 Lk33_%=\n\t"
    "global_load_dwordx4 v[0:3], v244, s[50:51] offset:-4096 sc1\n\t"
    "global_load_dwordx4 v[16:19], v244, s[50:51] offset:0 sc1\n\t"
    "global_load_dwordx4 v[32:35], v243, s[50:51] offset:-4096 sc1\n\t"
    "global_load_dwordx4 v[48:51], v243, s[50:51] offset:0 sc1\n\t"
    "Lk33_%=:\n\t"
    "s_bitcmp1_b32 s40, 2\n\t"
    "s_cbranch_scc0 Lk34_%=\n\t"
    "global_load_dwordx4 v[4:7], v244, s[52:53] offset:-4096 sc1\n\t"
    "global_load_dwordx4 v[20:23], v244, s[52:53] offset:0 sc1\n\t"
    "global_load_dwordx4 v[36:39], v243, s[52:53] offset:-4096 sc1\n\t"
    "global_load_dwordx4 v[52:55], v243, s[52:53] offset:0 sc1\n\t"
    "Lk34_%=:\n\t"
    "s_bitcmp1_b32 s40, 4\n\t"
    "s_cbranch_scc0 Lk35_%=\n\t"
    "global_load_dwordx4 v[8:11], v244, s[54:55] offset:-4096 sc1\n\t"
    "global_load_dwordx4 v[24:27], v244, s[54:55] offset:0 sc1\n\t"
    "global_load_dwordx4 v[40:43], v243, s[54:55] offset:-4096 sc1\n\t"
    "global_load_dwordx4 v[56:59], v243, s[54:55] offset:0 sc1\n\t"
    "Lk35_%=:\n\t"
    "s_bitcmp1_b32 s40, 6\n\t"
    "s_cbranch_scc0 Lk36_%=\n\t"
    "global_load_dwordx4 v[12:15], v244, s[56:57] offset:-4096 sc1\n\t"
    "global_load_dwordx4 v[28:31], v244, s[56:57] offset:0 sc1\n\t"
    "global_load_dwordx4 v[44:47], v243, s[56:57] offset:-4096 sc1\n\t"
    "global_load_dwordx4 v[60:63], v243, s[56:57] offset:0 sc1\n\t"
    "Lk36_%=:\n\t"
    "s_mul_i32 s40, s40, 3\n\t"
    "s_andn2_b32 s38, s38, s40\n\t"
    "s_cbranch_scc1 Lt24_%=\n\t"
    "Le26_%=:\n\t"
    "s_and_b32 vcc_lo, s40, 0xf\n\t"
    "s_cbranch_scc0 Lk37_%=\n\t"
    "s_and_b32 vcc_lo, s40, 0xf0\n\t"
    "s_cbranch_scc0 Lk38_%=\n\t"
    "s_waitcnt vmcnt(0)\n\t"
    "Lk37_%=:\n\t"
    "v_dot2_f32_bf16 v96, v0, s60, v96\n\t"
    "v_dot2_f32_bf16 v97, v0, s61, v97\n\t"
    "v_dot2_f32_bf16 v98, v1, s60, v98\n\t"
    "v_dot2_f32_bf16 v99, v1, s61, v99\n\t"
    "v_dot2_f32_bf16 v100, v2, s60, v100\n\t"
    "v_dot2_f32_bf16 v101, v2, s61, v101\n\t"
    "v_dot2_f32_bf16 v102, v3, s60, v102\n\t"
    "v_dot2_f32_bf16 v103, v3, s61, v103\n\t"
    "v_dot2_f32_bf16 v104, v16, s60, v104\n\t"
    "v_dot2_f32_bf16 v105, v16, s61, v105\n\t"
    "v_dot2_f32_bf16 v106, v17, s60, v106\n\t"
    "v_dot2_f32_bf16 v107, v17, s61, v107\n\t"
    "v_dot2_f32_bf16 v108, v18, s60, v108\n\t"
    "v_dot2_f32_bf16 v109, v18, s61, v109\n\t"
    "v_dot2_f32_bf16 v110, v19, s60, v110\n\t"
    "v_dot2_f32_bf16 v111, v19, s61, v111\n\t"
    "v_dot2_f32_bf16 v112, v32, s60, v112\n\t"
    "v_dot2_f32_bf16 v113, v32, s61, v113\n\t"
    "v_dot2_f32_bf16 v114, v33, s60, v114\n\t"
    "v_dot2_f32_bf16 v115, v33, s61, v115\n\t"
    "v_dot2_f32_bf16 v116, v34, s60, v116\n\t"
    "v_dot2_f32_bf16 v117, v34, s61, v117\n\t"
    "v_dot2_f32_bf16 v118, v35, s60, v118\n\t"
    "v_dot2_f32_bf16 v119, v35, s61, v119\n\t"
    "v_dot2_f32_bf16 v120, v48, s60, v120\n\t"
    "v_dot2_f32_bf16 v121, v48, s61, v121\n\t"
    "v_dot2_f32_bf16 v122, v49, s60, v122\n\t"
    "v_dot2_f32_bf16 v123, v49, s61, v123\n\t"
    "v_dot2_f32_bf16 v124, v50, s60, v124\n\t"
    "v_dot2_f32_bf16 v125, v50, s61, v125\n\t"
    "v_dot2_f32_bf16 v126, v51, s60, v126\n\t"
    "v_dot2_f32_bf16 v127, v51, s61, v127\n\t"
    "v_dot2_f32_bf16 v96, v4, s60, v96\n\t"
    "v_dot2_f32_bf16 v97, v4, s61, v97\n\t"
    "v_dot2_f32_bf16 v98, v5, s60, v98\n\t"
    "v_dot2_f32_bf16 v99, v5, s61, v99\n\t"
    "v_dot2_f32_bf16 v100, v6, s60, v100\n\t"
    "v_dot2_f32_bf16 v101, v6, s61, v101\n\t"
    "v_dot2_f32_bf16 v102, v7, s60, v102\n\t"
    "v_dot2_f32_bf16 v103, v7, s61, v103\n\t"
    "v_dot2_f32_bf16 v104, v20, s60, v104\n\t"
    "v_dot2_f32_bf16 v105, v20, s61, v105\n\t"
    "v_dot2_f32_bf16 v106, v21, s60, v106\n\t"
    "v_dot2_f32_bf16 v107, v21, s61, v107\n\t"
    "v_dot2_f32_bf16 v108, v22, s60, v108\n\t"
    "v_dot2_f32_bf16 v109, v22, s61, v109\n\t"
    "v_dot2_f32_bf16 v110, v23, s60, v110\n\t"
    "v_dot2_f32_bf16 v111, v23, s61, v111\n\t"
    "v_dot2_f32_bf16 v112, v36, s60, v112\n\t"
    "v_dot2_f32_bf16 v113, v36, s61, v113\n\t"
    "v_dot2_f32_bf16 v114, v37, s60, v114\n\t"
    "v_dot2_f32_bf16 v115, v37, s61, v115\n\t"
    "v_dot2_f32_bf16 v116, v38, s60, v116\n\t"
    "v_dot2_f32_bf16 v117, v38, s61, v117\n\t"
    "v_dot2_f32_bf16 v118, v39, s60, v118\n\t"
    "v_dot2_f32_bf16 v119, v39, s61, v119\n\t"
    "v_dot2_f32_bf16 v120, v52, s60, v120\n\t"
    "v_dot2_f32_bf16 v121, v52, s61, v121\n\t"
    "v_dot2_f32_bf16 v122, v53, s60, v122\n\t"
    "v_dot2_f32_bf16 v123, v53, s61, v123\n\t"
    "v_dot2_f32_bf16 v124, v54, s60, v124\n\t"
    "v_dot2_f32_bf16 v125, v54, s61, v125\n\t"
    "v_dot2_f32_bf16 v126, v55, s60, v126\n\t"
    "v_dot2_f32_bf16 v127, v55, s61, v127\n\t"
    "s_waitcnt vmcnt(0)\n\t"
    "v_dot2_f32_bf16 v160, v8, s60, 0\n\t"
    "v_dot2_f32_bf16 v161, v8, s61, 0\n\t"
    "v_dot2_f32_bf16 v162, v9, s60, 0\n\t"
    "v_dot2_f32_bf16 v163, v9, s61, 0\n\t"
    "v_dot2_f32_bf16 v164, v10, s60, 0\n\t"
    "v_dot2_f32_bf16 v165, v10, s61, 0\n\t"
    "v_dot2_f32_bf16 v166, v11, s60, 0\n\t"
    "v_dot2_f32_bf16 v167, v11, s61, 0\n\t"
    "v_dot2_f32_bf16 v168, v24, s60, 0\n\t"
    "v_dot2_f32_bf16 v169, v24, s61, 0\n\t"
    "v_dot2_f32_bf16 v170, v25, s60, 0\n\t"
    "v_dot2_f32_bf16 v171, v25, s61, 0\n\t"
    "v_dot2_f32_bf16 v172, v26, s60, 0\n\t"
    "v_dot2_f32_bf16 v173, v26, s61, 0\n\t"
    "v_dot2_f32_bf16 v174, v27, s60, 0\n\t"
    "v_dot2_f32_bf16 v175, v27, s61, 0\n\t"
    "v_dot2_f32_bf16 v176, v40, s60, 0\n\t"
    "v_dot2_f32_bf16 v177, v40, s61, 0\n\t"
    "v_dot2_f32_bf16 v178, v41, s60, 0\n\t"
    "v_dot2_f32_bf16 v179, v41, s61, 0\n\t"
    "v_dot2_f32_bf16 v180, v42, s60, 0\n\t"
    "v_dot2_f32_bf16 v181, v42, s61, 0\n\t"
    "v_dot2_f32_bf16 v182, v43, s60, 0\n\t"
    "v_dot2_f32_bf16 v183, v43, s61, 0\n\t"
    "v_dot2_f32_bf16 v184, v56, s60, 0\n\t"
    "v_dot2_f32_bf16 v185, v56, s61, 0\n\t"
    "v_dot2_f32_bf16 v186, v57, s60, 0\n\t"
    "v_dot2_f32_bf16 v187, v57, s61, 0\n\t"
    "v_dot2_f32_bf16 v188, v58, s60, 0\n\t"
    "v_dot2_f32_bf16 v189, v58, s61, 0\n\t"
    "v_dot2_f32_bf16 v190, v59, s60, 0\n\t"
    "v_dot2_f32_bf16 v191, v59, s61, 0\n\t"
    "v_dot2_f32_bf16 v160, v12, s60, v160\n\t"
    "v_dot2_f32_bf16 v161, v12, s61, v161\n\t"
    "v_dot2_f32_bf16 v162, v13, s60, v162\n\t"
    "v_dot2_f32_bf16 v163, v13, s61, v163\n\t"
    "v_dot2_f32_bf16 v164, v14, s60, v164\n\t"
    "v_dot2_f32_bf16 v165, v14, s61, v165\n\t"
    "v_dot2_f32_bf16 v166, v15, s60, v166\n\t"
    "v_dot2_f32_bf16 v167, v15, s61, v167\n\t"
    "v_dot2_f32_bf16 v168, v28, s60, v168\n\t"
    "v_dot2_f32_bf16 v169, v28, s61, v169\n\t"
    "v_dot2_f32_bf16 v170, v29, s60, v170\n\t"
    "v_dot2_f32_bf16 v171, v29, s61, v171\n\t"
    "v_dot2_f32_bf16 v172, v30, s60, v172\n\t"
    "v_dot2_f32_bf16 v173, v30, s61, v173\n\t"
    "v_dot2_f32_bf16 v174, v31, s60, v174\n\t"
    "v_dot2_f32_bf16 v175, v31, s61, v175\n\t"
    "v_dot2_f32_bf16 v176, v44, s60, v176\n\t"
    "v_dot2_f32_bf16 v177, v44, s61, v177\n\t"
    "v_dot2_f32_bf16 v178, v45, s60, v178\n\t"
    "v_dot2_f32_bf16 v179, v45, s61, v179\n\t"
    "v_dot2_f32_bf16 v180, v46, s60, v180\n\t"
    "v_dot2_f32_bf16 v181, v46, s61, v181\n\t"
    "v_dot2_f32_bf16 v182, v47, s60, v182\n\t"
    "v_dot2_f32_bf16 v183, v47, s61, v183\n\t"
    "v_dot2_f32_bf16 v184, v60, s60, v184\n\t"
    "v_dot2_f32_bf16 v185, v60, s61, v185\n\t"
    "v_dot2_f32_bf16 v186, v61, s60, v186\n\t"
    "v_dot2_f32_bf16 v187, v61, s61, v187\n\t"
    "v_dot2_f32_bf16 v188, v62, s60, v188\n\t"
    "v_dot2_f32_bf16 v189, v62, s61, v189\n\t"
    "v_dot2_f32_bf16 v190, v63, s60, v190\n\t"
    "v_dot2_f32_bf16 v191, v63, s61, v191\n\t"
    "Lk39_%=:\n\t"
    "v_pk_add_f32 v[96:97], v[96:97], v[160:161]\n\t"
    "v_pk_add_f32 v[98:99], v[98:99], v[162:163]\n\t"
    "v_pk_add_f32 v[100:101], v[100:101], v[164:165]\n\t"
    "v_pk_add_f32 v[102:103], v[102:103], v[166:167]\n\t"
    "v_pk_mul_f32 v[0:1], v[96:97], s[72:73]\n\t"
    "v_pk_mul_f32 v[2:3], v[98:99], s[72:73]\n\t"
    "v_pk_mul_f32 v[4:5], v[100:101], s[72:73]\n\t"
    "v_pk_mul_f32 v[6:7], v[102:103], s[72:73]\n\t"
    "v_exp_f32 v0, v0\n\t"
    "v_exp_f32 v1, v1\n\t"
    "v_exp_f32 v2, v2\n\t"
    "v_exp_f32 v3, v3\n\t"
    "v_exp_f32 v4, v4\n\t"
    "v_exp_f32 v5, v5\n\t"
    "v_exp_f32 v6, v6\n\t"
    "v_exp_f32 v7, v7\n\t"
    "v_pk_mul_f32 v[96:97], v[96:97], v[128:129]\n\t"
    "v_pk_mul_f32 v[98:99], v[98:99], v[130:131]\n\t"
    "v_pk_mul_f32 v[100:101], v[100:101], v[132:133]\n\t"
    "v_pk_mul_f32 v[102:103], v[102:103], v[134:135]\n\t"
    "v_pk_add_f32 v[0:1], v[0:1], 1.0 op_sel_hi:[1,0]\n\t"
    "v_pk_add_f32 v[2:3], v[2:3], 1.0 op_sel_hi:[1,0]\n\t"
    "v_pk_add_f32 v[4:5], v[4:5], 1.0 op_sel_hi:[1,0]\n\t"
    "v_pk_add_f32 v[6:7], v[6:7], 1.0 op_sel_hi:[1,0]\n\t"
    "v_rcp_f32 v0, v0\n\t"
    "v_rcp_f32 v1, v1\n\t"
    "v_rcp_f32 v2, v2\n\t"
    "v_rcp_f32 v3, v3\n\t"
    "v_rcp_f32 v4, v4\n\t"
    "v_rcp_f32 v5, v5\n\t"
    "v_rcp_f32 v6, v6\n\t"
    "v_rcp_f32 v7, v7\n\t"
    "v_pk_fma_f32 v[0:1], v[0:1], v[96:97], v[64:65]\n\t"
    "v_pk_fma_f32 v[2:3], v[2:3], v[98:99], v[66:67]\n\t"
    "v_pk_fma_f32 v[4:5], v[4:5], v[100:101], v[68:69]\n\t"
    "v_pk_fma_f32 v[6:7], v[6:7], v[102:103], v[70:71]\n\t"
    "v_cvt_pk_bf16_f32 v208, v0, v1\n\t"
    "v_cvt_pk_bf16_f32 v209, v2, v3\n\t"
    "v_cvt_pk_bf16_f32 v210, v4, v5\n\t"
    "v_cvt_pk_bf16_f32 v211, v6, v7\n\t"
    "global_store_dwordx4 v247, v[208:211], s[48:49] offset:0 sc1\n\t"
    "v_pk_add_f32 v[104:105], v[104:105], v[168:169]\n\t"
    "v_pk_add_f32 v[106:107], v[106:107], v[170:171]\n\t"
    "v_pk_add_f32 v[108:109], v[108:109], v[172:173]\n\t"
    "v_pk_add_f32 v[110:111], v[110:111], v[174:175]\n\t"
    "v_pk_mul_f32 v[16:17], v[104:105], s[72:73]\n\t"
    "v_pk_mul_f32 v[18:19], v[106:107], s[72:73]\n\t"
    "v_pk_mul_f32 v[20:21], v[108:109], s[72:73]\n\t"
    "v_pk_mul_f32 v[22:23], v[110:111], s[72:73]\n\t"
    "v_exp_f32 v16, v16\n\t"
    "v_exp_f32 v17, v17\n\t"
    "v_exp_f32 v18, v18\n\t"
    "v_exp_f32 v19, v19\n\t"
    "v_exp_f32 v20, v20\n\t"
    "v_exp_f32 v21, v21\n\t"
    "v_exp_f32 v22, v22\n\t"
    "v_exp_f32 v23, v23\n\t"
    "v_pk_mul_f32 v[104:105], v[104:105], v[136:137]\n\t"
    "v_pk_mul_f32 v[106:107], v[106:107], v[138:139]\n\t"
    "v_pk_mul_f32 v[108:109], v[108:109], v[140:141]\n\t"
    "v_pk_mul_f32 v[110:111], v[110:111], v[142:143]\n\t"
    "v_pk_add_f32 v[16:17], v[16:17], 1.0 op_sel_hi:[1,0]\n\t"
    "v_pk_add_f32 v[18:19], v[18:19], 1.0 op_sel_hi:[1,0]\n\t"
    "v_pk_add_f32 v[20:21], v[20:21], 1.0 op_sel_hi:[1,0]\n\t"
    "v_pk_add_f32 v[22:23], v[22:23], 1.0 op_sel_hi:[1,0]\n\t"
    "v_rcp_f32 v16, v16\n\t"
    "v_rcp_f32 v17, v17\n\t"
    "v_rcp_f32 v18, v18\n\t"
    "v_rcp_f32 v19, v19\n\t"
    "v_rcp_f32 v20, v20\n\t"
    "v_rcp_f32 v21, v21\n\t"
    "v_rcp_f32 v22, v22\n\t"
    "v_rcp_f32 v23, v23\n\t"
    "v_pk_fma_f32 v[16:17], v[16:17], v[104:105], v[72:73]\n\t"
    "v_pk_fma_f32 v[18:19], v[18:19], v[106:107], v[74:75]\n\t"
    "v_pk_fma_f32 v[20:21], v[20:21], v[108:109], v[76:77]\n\t"
    "v_pk_fma_f32 v[22:23], v[22:23], v[110:111], v[78:79]\n\t"
    "v_cvt_pk_bf16_f32 v212, v16, v17\n\t"
    "v_cvt_pk_bf16_f32 v213, v18, v19\n\t"
    "v_cvt_pk_bf16_f32 v214, v20, v21\n\t"
    "v_cvt_pk_bf16_f32 v215, v22, v23\n\t"
    "global_store_dwordx4 v247, v[212:215], s[48:49] offset:64 sc1\n\t"
    "v_pk_add_f32 v[112:113], v[112:113], v[176:177]\n\t"
    "v_pk_add_f32 v[114:115], v[114:115], v[178:179]\n\t"
    "v_pk_add_f32 v[116:117], v[116:117], v[180:181]\n\t"
    "v_pk_add_f32 v[118:119], v[118:119], v[182:183]\n\t"
    "v_pk_mul_f32 v[0:1], v[112:113], s[72:73]\n\t"
    "v_pk_mul_f32 v[2:3], v[114:115], s[72:73]\n\t"
    "v_pk_mul_f32 v[4:5], v[116:117], s[72:73]\n\t"
    "v_pk_mul_f32 v[6:7], v[118:119], s[72:73]\n\t"
    "v_exp_f32 v0, v0\n\t"
    "v_exp_f32 v1, v1\n\t"
    "v_exp_f32 v2, v2\n\t"
    "v_exp_f32 v3, v3\n\t"
    "v_exp_f32 v4, v4\n\t"
    "v_exp_f32 v5, v5\n\t"
    "v_exp_f32 v6, v6\n\t"
    "v_exp_f32 v7, v7\n\t"
    "v_pk_mul_f32 v[112:113], v[112:113], v[144:145]\n\t"
    "v_pk_mul_f32 v[114:115], v[114:115], v[146:147]\n\t"
    "v_pk_mul_f32 v[116:117], v[116:117], v[148:149]\n\t"
    "v_pk_mul_f32 v[118:119], v[118:119], v[150:151]\n\t"
    "v_pk_add_f32 v[0:1], v[0:1], 1.0 op_sel_hi:[1,0]\n\t"
    "v_pk_add_f32 v[2:3], v[2:3], 1.0 op_sel_hi:[1,0]\n\t"
    "v_pk_add_f32 v[4:5], v[4:5], 1.0 op_sel_hi:[1,0]\n\t"
    "v_pk_add_f32 v[6:7], v[6:7], 1.0 op_sel_hi:[1,0]\n\t"
    "v_rcp_f32 v0, v0\n\t"
    "v_rcp_f32 v1, v1\n\t"
    "v_rcp_f32 v2, v2\n\t"
    "v_rcp_f32 v3, v3\n\t"
    "v_rcp_f32 v4, v4\n\t"
    "v_rcp_f32 v5, v5\n\t"
    "v_rcp_f32 v6, v6\n\t"
    "v_rcp_f32 v7, v7\n\t"
    "v_pk_fma_f32 v[0:1], v[0:1], v[112:113], v[80:81]\n\t"
    "v_pk_fma_f32 v[2:3], v[2:3], v[114:115], v[82:83]\n\t"
    "v_pk_fma_f32 v[4:5], v[4:5], v[116:117], v[84:85]\n\t"
    "v_pk_fma_f32 v[6:7], v[6:7], v[118:119], v[86:87]\n\t"
    "v_cvt_pk_bf16_f32 v216, v0, v1\n\t"
    "v_cvt_pk_bf16_f32 v217, v2, v3\n\t"
    "v_cvt_pk_bf16_f32 v218, v4, v5\n\t"
    "v_cvt_pk_bf16_f32 v219, v6, v7\n\t"
    "global_store_dwordx4 v247, v[216:219], s[48:49] offset:128 sc1\n\t"
    "v_pk_add_f32 v[120:121], v[120:121], v[184:185]\n\t"
    "v_pk_add_f32 v[122:123], v[122:123], v[186:187]\n\t"
    "v_pk_add_f32 v[124:125], v[124:125], v[188:189]\n\t"
    "v_pk_add_f32 v[126:127], v[126:127], v[190:191]\n\t"
    "v_pk_mul_f32 v[16:17], v[120:121], s[72:73]\n\t"
    "v_pk_mul_f32 v[18:19], v[122:123], s[72:73]\n\t"
    "v_pk_mul_f32 v[20:21], v[124:125], s[72:73]\n\t"
    "v_pk_mul_f32 v[22:23], v[126:127], s[72:73]\n\t"
    "v_exp_f32 v16, v16\n\t"
    "v_exp_f32 v17, v17\n\t"
    "v_exp_f32 v18, v18\n\t"
    "v_exp_f32 v19, v19\n\t"
    "v_exp_f32 v20, v20\n\t"
    "v_exp_f32 v21, v21\n\t"
    "v_exp_f32 v22, v22\n\t"
    "v_exp_f32 v23, v23\n\t"
    "v_pk_mul_f32 v[120:121], v[120:121], v[152:153]\n\t"
    "v_pk_mul_f32 v[122:123], v[122:123], v[154:155]\n\t"
    "v_pk_mul_f32 v[124:125], v[124:125], v[156:157]\n\t"
    "v_pk_mul_f32 v[126:127], v[126:127], v[158:159]\n\t"
    "v_pk_add_f32 v[16:17], v[16:17], 1.0 op_sel_hi:[1,0]\n\t"
    "v_pk_add_f32 v[18:19], v[18:19], 1.0 op_sel_hi:[1,0]\n\t"
    "v_pk_add_f32 v[20:21], v[20:21], 1.0 op_sel_hi:[1,0]\n\t"
    "v_pk_add_f32 v[22:23], v[22:23], 1.0 op_sel_hi:[1,0]\n\t"
    "v_rcp_f32 v16, v16\n\t"
    "v_rcp_f32 v17, v17\n\t"
    "v_rcp_f32 v18, v18\n\t"
    "v_rcp_f32 v19, v19\n\t"
    "v_rcp_f32 v20, v20\n\t"
    "v_rcp_f32 v21, v21\n\t"
    "v_rcp_f32 v22, v22\n\t"
    "v_rcp_f32 v23, v23\n\t"
    "v_pk_fma_f32 v[16:17], v[16:17], v[120:121], v[88:89]\n\t"
    "v_pk_fma_f32 v[18:19], v[18:19], v[122:123], v[90:91]\n\t"
    "v_pk_fma_f32 v[20:21], v[20:21], v[124:125], v[92:93]\n\t"
    "v_pk_fma_f32 v[22:23], v[22:23], v[126:127], v[94:95]\n\t"
    "v_cvt_pk_bf16_f32 v220, v16, v17\n\t"
    "v_cvt_pk_bf16_f32 v221, v18, v19\n\t"
    "v_cvt_pk_bf16_f32 v222, v20, v21\n\t"
    "v_cvt_pk_bf16_f32 v223, v22, v23\n\t"
    "global_store_dwordx4 v247, v[220:223], s[48:49] offset:192 sc1\n\t"
    "s_endpgm\n\t"
    "Lk38_%=:\n\t"
    "v_dot2_f32_bf16 v160, v8, s60, 0\n\t"
    "v_dot2_f32_bf16 v161, v8, s61, 0\n\t"
    "v_dot2_f32_bf16 v162, v9, s60, 0\n\t"
    "v_dot2_f32_bf16 v163, v9, s61, 0\n\t"
    "v_dot2_f32_bf16 v164, v10, s60, 0\n\t"
    "v_dot2_f32_bf16 v165, v10, s61, 0\n\t"
    "v_dot2_f32_bf16 v166, v11, s60, 0\n\t"
    "v_dot2_f32_bf16 v167, v11, s61, 0\n\t"
    "v_dot2_f32_bf16 v168, v24, s60, 0\n\t"
    "v_dot2_f32_bf16 v169, v24, s61, 0\n\t"
    "v_dot2_f32_bf16 v170, v25, s60, 0\n\t"
    "v_dot2_f32_bf16 v171, v25, s61, 0\n\t"
    "v_dot2_f32_bf16 v172, v26, s60, 0\n\t"
    "v_dot2_f32_bf16 v173, v26, s61, 0\n\t"
    "v_dot2_f32_bf16 v174, v27, s60, 0\n\t"
    "v_dot2_f32_bf16 v175, v27, s61, 0\n\t"
    "v_dot2_f32_bf16 v176, v40, s60, 0\n\t"
    "v_dot2_f32_bf16 v177, v40, s61, 0\n\t"
    "v_dot2_f32_bf16 v178, v41, s60, 0\n\t"
    "v_dot2_f32_bf16 v179, v41, s61, 0\n\t"
    "v_dot2_f32_bf16 v180, v42, s60, 0\n\t"
    "v_dot2_f32_bf16 v181, v42, s61, 0\n\t"
    "v_dot2_f32_bf16 v182, v43, s60, 0\n\t"
    "v_dot2_f32_bf16 v183, v43, s61, 0\n\t"
    "v_dot2_f32_bf16 v184, v56, s60, 0\n\t"
    "v_dot2_f32_bf16 v185, v56, s61, 0\n\t"
    "v_dot2_f32_bf16 v186, v57, s60, 0\n\t"
    "v_dot2_f32_bf16 v187, v57, s61, 0\n\t"
    "v_dot2_f32_bf16 v188, v58, s60, 0\n\t"
    "v_dot2_f32_bf16 v189, v58, s61, 0\n\t"
    "v_dot2_f32_bf16 v190, v59, s60, 0\n\t"
    "v_dot2_f32_bf16 v191, v59, s61, 0\n\t"
    "Lpad12_%=:\n\t"
    "s_cbranch_execz LretB_%=\n\t"
    "v_dot2_f32_bf16 v160, v12, s60, v160\n\t"
    "v_dot2_f32_bf16 v161, v12, s61, v161\n\t"
    "v_dot2_f32_bf16 v162, v13, s60, v162\n\t"
    "v_dot2_f32_bf16 v163, v13, s61, v163\n\t"
    "v_dot2_f32_bf16 v164, v14, s60, v164\n\t"
    "v_dot2_f32_bf16 v165, v14, s61, v165\n\t"
    "v_dot2_f32_bf16 v166, v15, s60, v166\n\t"
    "v_dot2_f32_bf16 v167, v15, s61, v167\n\t"
    "v_dot2_f32_bf16 v168, v28, s60, v168\n\t"
    "v_dot2_f32_bf16 v169, v28, s61, v169\n\t"
    "v_dot2_f32_bf16 v170, v29, s60, v170\n\t"
    "v_dot2_f32_bf16 v171, v29, s61, v171\n\t"
    "v_dot2_f32_bf16 v172, v30, s60, v172\n\t"
    "v_dot2_f32_bf16 v173, v30, s61, v173\n\t"
    "v_dot2_f32_bf16 v174, v31, s60, v174\n\t"
    "v_dot2_f32_bf16 v175, v31, s61, v175\n\t"
    "v_dot2_f32_bf16 v176, v44, s60, v176\n\t"
    "v_dot2_f32_bf16 v177, v44, s61, v177\n\t"
    "v_dot2_f32_bf16 v178, v45, s60, v178\n\t"
    "v_dot2_f32_bf16 v179, v45, s61, v179\n\t"
    "v_dot2_f32_bf16 v180, v46, s60, v180\n\t"
    "v_dot2_f32_bf16 v181, v46, s61, v181\n\t"
    "v_dot2_f32_bf16 v182, v47, s60, v182\n\t"
    "v_dot2_f32_bf16 v183, v47, s61, v183\n\t"
    "v_dot2_f32_bf16 v184, v60, s60, v184\n\t"
    "v_dot2_f32_bf16 v185, v60, s61, v185\n\t"
    "v_dot2_f32_bf16 v186, v61, s60, v186\n\t"
    "v_dot2_f32_bf16 v187, v61, s61, v187\n\t"
    "v_dot2_f32_bf16 v188, v62, s60, v188\n\t"
    "v_dot2_f32_bf16 v189, v62, s61, v189\n\t"
    "v_dot2_f32_bf16 v190, v63, s60, v190\n\t"
    "v_dot2_f32_bf16 v191, v63, s61, v191\n\t"
    "s_waitcnt vmcnt(0)\n\t"
    "v_dot2_f32_bf16 v96, v0, s60, v96\n\t"
    "v_dot2_f32_bf16 v97, v0, s61, v97\n\t"
    "v_dot2_f32_bf16 v98, v1, s60, v98\n\t"
    "v_dot2_f32_bf16 v99, v1, s61, v99\n\t"
    "v_dot2_f32_bf16 v100, v2, s60, v100\n\t"
    "v_dot2_f32_bf16 v101, v2, s61, v101\n\t"
    "v_dot2_f32_bf16 v102, v3, s60, v102\n\t"
    "v_dot2_f32_bf16 v103, v3, s61, v103\n\t"
    "v_dot2_f32_bf16 v104, v16, s60, v104\n\t"
    "v_dot2_f32_bf16 v105, v16, s61, v105\n\t"
    "v_dot2_f32_bf16 v106, v17, s60, v106\n\t"
    "v_dot2_f32_bf16 v107, v17, s61, v107\n\t"
    "v_dot2_f32_bf16 v108, v18, s60, v108\n\t"
    "v_dot2_f32_bf16 v109, v18, s61, v109\n\t"
    "v_dot2_f32_bf16 v110, v19, s60, v110\n\t"
    "v_dot2_f32_bf16 v111, v19, s61, v111\n\t"
    "v_dot2_f32_bf16 v112, v32, s60, v112\n\t"
    "v_dot2_f32_bf16 v113, v32, s61, v113\n\t"
    "v_dot2_f32_bf16 v114, v33, s60, v114\n\t"
    "v_dot2_f32_bf16 v115, v33, s61, v115\n\t"
    "v_dot2_f32_bf16 v116, v34, s60, v116\n\t"
    "v_dot2_f32_bf16 v117, v34, s61, v117\n\t"
    "v_dot2_f32_bf16 v118, v35, s60, v118\n\t"
    "v_dot2_f32_bf16 v119, v35, s61, v119\n\t"
    "v_dot2_f32_bf16 v120, v48, s60, v120\n\t"
    "v_dot2_f32_bf16 v121, v48, s61, v121\n\t"
    "v_dot2_f32_bf16 v122, v49, s60, v122\n\t"
    "v_dot2_f32_bf16 v123, v49, s61, v123\n\t"
    "v_dot2_f32_bf16 v124, v50, s60, v124\n\t"
    "v_dot2_f32_bf16 v125, v50, s61, v125\n\t"
    "v_dot2_f32_bf16 v126, v51, s60, v126\n\t"
    "v_dot2_f32_bf16 v127, v51, s61, v127\n\t"
    "v_dot2_f32_bf16 v96, v4, s60, v96\n\t"
    "v_dot2_f32_bf16 v97, v4, s61, v97\n\t"
    "v_dot2_f32_bf16 v98, v5, s60, v98\n\t"
    "v_dot2_f32_bf16 v99, v5, s61, v99\n\t"
    "v_dot2_f32_bf16 v100, v6, s60, v100\n\t"
    "v_dot2_f32_bf16 v101, v6, s61, v101\n\t"
    "v_dot2_f32_bf16 v102, v7, s60, v102\n\t"
    "v_dot2_f32_bf16 v103, v7, s61, v103\n\t"
    "v_dot2_f32_bf16 v104, v20, s60, v104\n\t"
    "v_dot2_f32_bf16 v105, v20, s61, v105\n\t"
    "v_dot2_f32_bf16 v106, v21, s60, v106\n\t"
    "v_dot2_f32_bf16 v107, v21, s61, v107\n\t"
    "v_dot2_f32_bf16 v108, v22, s60, v108\n\t"
    "v_dot2_f32_bf16 v109, v22, s61, v109\n\t"
    "v_dot2_f32_bf16 v110, v23, s60, v110\n\t"
    "v_dot2_f32_bf16 v111, v23, s61, v111\n\t"
    "v_dot2_f32_bf16 v112, v36, s60, v112\n\t"
    "v_dot2_f32_bf16 v113, v36, s61, v113\n\t"
    "v_dot2_f32_bf16 v114, v37, s60, v114\n\t"
    "v_dot2_f32_bf16 v115, v37, s61, v115\n\t"
    "v_dot2_f32_bf16 v116, v38, s60, v116\n\t"
    "v_dot2_f32_bf16 v117, v38, s61, v117\n\t"
    "v_dot2_f32_bf16 v118, v39, s60, v118\n\t"
    "v_dot2_f32_bf16 v119, v39, s61, v119\n\t"
    "v_dot2_f32_bf16 v120, v52, s60, v120\n\t"
    "v_dot2_f32_bf16 v121, v52, s61, v121\n\t"
    "v_dot2_f32_bf16 v122, v53, s60, v122\n\t"
    "v_dot2_f32_bf16 v123, v53, s61, v123\n\t"
    "v_dot2_f32_bf16 v124, v54, s60, v124\n\t"
    "v_dot2_f32_bf16 v125, v54, s61, v125\n\t"
    "v_dot2_f32_bf16 v126, v55, s60, v126\n\t"
    "v_dot2_f32_bf16 v127, v55, s61, v127\n\t"
    "s_nop 2\n\t"
    "s_branch Lk39_%=\n\t"
    "Ls1_%=:\n\t"
    "s_mov_b32 s37, 0x10000\n\t"
    "Lp3_%=:\n\t"
    "s_sleep 3\n\t"
    "v_mov_b64 v[250:251], 0\n\t"
    "s_mov_b64 exec, 0xff\n\t"
    "global_atomic_or_x2 v[250:251], v249, v[250:251], s[20:21] sc0 sc1\n\t"
    "s_mov_b64 exec, -1\n\t"
    "s_sub_u32 s37, s37, 1\n\t"
    "s_waitcnt vmcnt(0)\n\t"
    "s_cbranch_scc1 Lb2_%=\n\t"
    "v_cmp_ne_u64 vcc, s[74:75], v[250:251]\n\t"
    "s_nop 1\n\t"
    "s_and_b64 vcc, vcc, 0xff\n\t"
    "s_cbranch_scc1 Lp3_%=\n\t"
    "s_branch Lb2_%=\n\t"
    "Ls4_%=:\n\t"
    "s_mov_b32 s37, 0x10000\n\t"
    "Lp6_%=:\n\t"
    "s_sleep 3\n\t"
    "global_load_dwordx2 v[250:251], v249, s[20:21] sc1\n\t"
    "s_sub_u32 s37, s37, 1\n\t"
    "s_waitcnt vmcnt(0)\n\t"
    "v_mov_b32 v230, v250\n\t"
    "v_and_b32 v250, 0xffff0000, v250\n\t"
    "s_cbranch_scc1 Lb5_%=\n\t"
    "v_cmp_ne_u64 vcc, s[74:75], v[250:251]\n\t"
    "s_nop 1\n\t"
    "s_and_b64 vcc, vcc, -1\n\t"
    "s_cbranch_scc1 Lp6_%=\n\t"
    "s_branch Lb5_%=\n\t"
    "Ls12_%=:\n\t"
    "s_mov_b32 s37, 0x10000\n\t"
    "Lp14_%=:\n\t"
    "s_sleep 3\n\t"
    "v_mov_b64 v[250:251], 0\n\t"
    "s_mov_b64 exec, 0x7f\n\t"
    "global_atomic_or_x2 v[250:251], v249, v[250:251], s[20:21] sc0 sc1\n\t"
    "s_mov_b64 exec, -1\n\t"
    "s_sub_u32 s37, s37, 1\n\t"
    "s_waitcnt vmcnt(0)\n\t"
    "s_cbranch_scc1 Lb13_%=\n\t"
    "v_cmp_ne_u64 vcc, s[74:75], v[250:251]\n\t"
    "s_nop 1\n\t"
    "s_and_b64 vcc, vcc, 0x7f\n\t"
    "s_cbranch_scc1 Lp14_%=\n\t"
    "s_branch Lb13_%=\n\t"
    "Ls15_%=:\n\t"
    "s_mov_b32 s37, 0x10000\n\t"
    "Lp17_%=:\n\t"
    "s_sleep 3\n\t"
    "v_mov_b64 v[250:251], 0\n\t"
    "s_mov_b64 exec, 0x7f\n\t"
    "global_atomic_or_x2 v[250:251], v249, v[250:251], s[20:21] sc0 sc1\n\t"
    "s_mov_b64 exec, -1\n\t"
    "s_sub_u32 s37, s37, 1\n\t"
    "s_waitcnt vmcnt(0)\n\t"
    "s_cbranch_scc1 Lb16_%=\n\t"
    "v_cmp_ne_u64 vcc, s[74:75], v[250:251]\n\t"
    "s_nop 1\n\t"
    "s_and_b64 vcc, vcc, 0x7f\n\t"
    "s_cbranch_scc1 Lp17_%=\n\t"
    "s_branch Lb16_%=\n\t"
    "Ls18_%=:\n\t"
    "s_mov_b32 s37, 0x10000\n\t"
    "Lp20_%=:\n\t"
    "s_sleep 3\n\t"
    "v_mov_b64 v[250:251], 0\n\t"
    "s_mov_b64 exec, 0x7f\n\t"
    "global_atomic_or_x2 v[250:251], v249, v[250:251], s[20:21] sc0 sc1\n\t"
    "s_mov_b64 exec, -1\n\t"
    "s_sub_u32 s37, s37, 1\n\t"
    "s_waitcnt vmcnt(0)\n\t"
    "s_cbranch_scc1 Lb19_%=\n\t"
    "v_cmp_ne_u64 vcc, s[74:75], v[250:251]\n\t"
    "s_nop 1\n\t"
    "s_and_b64 vcc, vcc, 0x7f\n\t"
    "s_cbranch_scc1 Lp20_%=\n\t"
    "s_branch Lb19_%=\n\t"
    "Ls21_%=:\n\t"
    "s_mov_b32 s37, 0x10000\n\t"
    "Lp23_%=:\n\t"
    "s_sleep 3\n\t"
    "global_load_dwordx2 v[250:251], v176, s[20:21] sc1\n\t"
    "s_sub_u32 s37, s37, 1\n\t"
    "s_waitcnt vmcnt(0)\n\t"
    "s_cbranch_scc1 Lb22_%=\n\t"
    "v_cmp_ne_u64 vcc, s[74:75], v[250:251]\n\t"
    "s_nop 1\n\t"
    "s_and_b64 vcc, vcc, -1\n\t"
    "s_cbranch_scc1 Lp23_%=\n\t"
    "s_branch Lb22_%=\n\t"
    "Ls40_%=:\n\t"
    "s_mov_b32 s37, 0x10000\n\t"
    "Lp42_%=:\n\t"
    "s_sleep 8\n\t"
    "global_load_dwordx2 v[250:251], v249, s[20:21] sc1\n\t"
    "s_sub_u32 s37, s37, 1\n\t"
    "s_waitcnt vmcnt(0)\n\t"
    "v_mov_b32 v230, v250\n\t"
    "v_and_b32 v250, 0xffff0000, v250\n\t"
    "s_cbranch_scc1 Lb41_%=\n\t"
    "v_cmp_ne_u64 vcc, s[74:75], v[250:251]\n\t"
    "s_nop 1\n\t"
    "s_and_b64 vcc, vcc, -1\n\t"
    "s_cbranch_scc1 Lp42_%=\n\t"
    "s_branch Lb41_%=\n\t"
    "Lspare_%=:\n\t"
    "v_mov_b32 v110, v111\n\t"
    "v_pk_maximum3_f16 v110, v128, v129, v110\n\t"
    "v_pk_minimum3_f16 v111, v128, v129, v111\n\t"
    "v_pk_maximum3_f16 v110, v130, v131, v110\n\t"
    "v_pk_minimum3_f16 v111, v130, v131, v111\n\t"
    "v_pk_maximum3_f16 v110, v132, v133, v110\n\t"
    "v_pk_minimum3_f16 v111, v132, v133, v111\n\t"
    "v_pk_maximum3_f16 v110, v134, v135, v110\n\t"
    "v_pk_minimum3_f16 v111, v134, v135, v111\n\t"
    "v_pk_maximum3_f16 v110, v136, v137, v110\n\t"
    "v_pk_minimum3_f16 v111, v136, v137, v111\n\t"
    "v_pk_maximum3_f16 v110, v138, v139, v110\n\t"
    "v_pk_minimum3_f16 v111, v138, v139, v111\n\t"
    "v_pk_maximum3_f16 v110, v140, v141, v110\n\t"
    "v_pk_minimum3_f16 v111, v140, v141, v111\n\t"
    "v_pk_maximum3_f16 v110, v142, v143, v110\n\t"
    "v_pk_minimum3_f16 v111, v142, v143, v111\n\t"
    "v_and_b32 v110, 0x7fff7fff, v110\n\t"
    "v_and_b32 v111, 0x7fff7fff, v111\n\t"
    "v_pk_max_u16 v110, v110, v111\n\t"
    "v_lshrrev_b32 v111, 16, v110\n\t"
    "v_and_b32 v110, 0xffff, v110\n\t"
    "v_max_u32 v110, v110, v111\n\t"
    "s_nop 1\n\t"
    "v_max_u32_dpp v110, v110, v110 quad_perm:[1,0,3,2] row_mask:0xf bank_mask:0xf\n\t"
    "s_nop 1\n\t"
    "v_max_u32_dpp v110, v110, v110 quad_perm:[2,3,0,1] row_mask:0xf bank_mask:0xf\n\t"
    "s_nop 1\n\t"
    "v_max_u32_dpp v110, v110, v110 row_half_mirror row_mask:0xf bank_mask:0xf\n\t"
    "s_nop 1\n\t"
    "v_max_u32_dpp v110, v110, v110 row_mirror row_mask:0xf bank_mask:0xf\n\t"
    "v_mov_b32 v111, v110\n\t"
    "s_nop 1\n\t"
    "v_permlane16_swap_b32 v110, v111\n\t"
    "v_max_u32 v110, v110, v111\n\t"
    "v_mov_b32 v111, v110\n\t"
    "s_nop 1\n\t"
    "v_permlane32_swap_b32 v110, v111\n\t"
    "v_max_u32 v110, v110, v111\n\t"
    "v_lshrrev_b32 v110, 7, v110\n\t"
    "s_lshl_b32 s38, s22, 2\n\t"
    "s_add_u32 s38, s38, 152320\n\t"
    "v_mov_b32 v111, s38\n\t"
    "ds_write_b32 v111, v110\n\t"
    "s_waitcnt lgkmcnt(0)\n\t"
    "v_lshrrev_b32 v90, 4, v245\n\t"
    "v_and_b32 v91, 15, v245\n\t"
    "v_lshlrev_b32 v89, 7, v91\n\t"
    "v_lshl_or_b32 v89, v90, 11, v89\n\t"
    "v_min_u32 v91, 13, v91\n\t"
    "v_mul_u32_u24 v88, 7168, v90\n\t"
    "v_lshl_add_u32 v88, v91, 7, v88\n\t"
    "s_mul_i32 s38, s27, 28672\n\t"
    "s_mul_i32 s39, s25, 1792\n\t"
    "s_add_u32 s38, s38, s39\n\t"
    "s_add_u32 s38, s38, 0xe0000\n\t"
    "s_add_u32 s54, s10, s38\n\t"
    "s_addc_u32 s55, s11, 0\n\t"
    "s_lshl_b32 s38, s27, 13\n\t"
    "s_mul_i32 s39, s25, 1835008\n\t"
    "s_add_u32 s38, s38, s39\n\t"
    "s_add_u32 s56, s14, s38\n\t"
    "s_addc_u32 s57, s15, 0\n\t"
    "s_and_b32 s38, s27, 15\n\t"
    "s_lshl_b32 s38, s38, 2\n\t"
    "s_add_u32 s38, s38, s25\n\t"
    "s_lshl_b32 s38, s38, 18\n\t"
    "s_lshr_b32 s39, s38, 8\n\t"
    "s_xor_b32 s39, s39, s30\n\t"
    "s_and_b32 s39, s39, 0x2000\n\t"
    "s_or_b32 s38, s38, s39\n\t"
    "s_lshr_b32 s39, s30, 3\n\t"
    "s_add_u32 s38, s38, s39\n\t"
    "s_and_b32 s39, s22, 1\n\t"
    "s_lshl_b32 s39, s39, 13\n\t"
    "s_xor_b32 s38, s38, s39\n\t"
    "s_add_u32 s38, s38, 21200896\n\t"
    "s_xor_b32 s39, s38, 0x2000\n\t"
    "s_mov_b32 s41, 0\n\t"
    "global_load_dword a127, v88, s[54:55]\n\t"
    "s_add_u32 s54, s54, 0xe0000\n\t"
    "s_addc_u32 s55, s55, 0\n\t"
    "global_load_dword a127, v88, s[54:55]\n\t"
    "s_add_u32 s54, s54, 0xe0000\n\t"
    "s_addc_u32 s55, s55, 0\n\t"
    "global_load_dword a127, v88, s[54:55]\n\t"
    "s_add_u32 s54, s54, 0xe0000\n\t"
    "s_addc_u32 s55, s55, 0\n\t"
    "s_waitcnt lgkmcnt(0)\n\t"
    "s_memtime s[46:47]\n\t"
    "s_waitcnt lgkmcnt(0)\n\t"
    "s_load_dword s40, s[20:21], s38 glc\n\t"
    "s_waitcnt lgkmcnt(0)\n\t"
    "s_memtime s[48:49]\n\t"
    "s_waitcnt lgkmcnt(0)\n\t"
    "s_load_dword s40, s[20:21], s39 glc\n\t"
    "s_waitcnt lgkmcnt(0)\n\t"
    "s_memtime s[52:53]\n\t"
    "s_waitcnt lgkmcnt(0)\n\t"
    "s_lshl_b32 s40, s48, 1\n\t"
    "s_sub_u32 s40, s40, s46\n\t"
    "s_sub_u32 s40, s40, s52\n\t"
    "s_max_i32 s40, s40, -400\n\t"
    "s_min_i32 s40, s40, 400\n\t"
    "s_add_u32 s41, s41, s40\n\t"
    "s_xor_b32 s38, s38, 16512\n\t"
    "s_xor_b32 s39, s38, 0x2000\n\t"
    "s_memtime s[46:47]\n\t"
    "s_waitcnt lgkmcnt(0)\n\t"
    "s_load_dword s40, s[20:21], s38 glc\n\t"
    "s_waitcnt lgkmcnt(0)\n\t"
    "s_memtime s[48:49]\n\t"
    "s_waitcnt lgkmcnt(0)\n\t"
    "s_load_dword s40, s[20:21], s39 glc\n\t"
    "s_waitcnt lgkmcnt(0)\n\t"
    "s_memtime s[52:53]\n\t"
    "s_waitcnt lgkmcnt(0)\n\t"
    "s_lshl_b32 s40, s48, 1\n\t"
    "s_sub_u32 s40, s40, s46\n\t"
    "s_sub_u32 s40, s40, s52\n\t"
    "s_max_i32 s40, s40, -400\n\t"
    "s_min_i32 s40, s40, 400\n\t"
    "s_add_u32 s41, s41, s40\n\t"
    "s_xor_b32 s38, s38, 6400\n\t"
    "s_xor_b32 s39, s38, 0x2000\n\t"
    "s_lshl_b32 s38, s22, 2\n\t"
    "s_add_u32 s38, s38, 152352\n\t"
    "v_mov_b32 v16, s38\n\t"
    "v_mov_b32 v17, s41\n\t"
    "ds_write_b32 v16, v17\n\t"
    "s_waitcnt lgkmcnt(0)\n\t"
    "s_waitcnt vmcnt(3)\n\t"
    "s_barrier\n\t"
    "v_mov_b32 v128, 152320\n\t"
    "ds_read_b128 v[128:131], v128\n\t"
    "s_waitcnt lgkmcnt(0)\n\t"
    "v_max3_u32 v128, v128, v129, v130\n\t"
    "v_max_u32 v128, v128, v131\n\t"
    "v_mov_b32 v16, 152352\n\t"
    "ds_read_b128 v[16:19], v16\n\t"
    "s_waitcnt lgkmcnt(0)\n\t"
    "v_add_u32 v16, v16, v18\n\t"
    "v_sub_u32 v16, v17, v16\n\t"
    "v_add_u32 v16, v16, v19\n\t"
    "v_lshrrev_b32 v16, 31, v16\n\t"
    "v_lshl_or_b32 v128, v16, 8, v128\n\t"
    "v_or_b32 v128, v128, v254\n\t"
    "v_mov_b32 v129, v255\n\t"
    "s_mov_b64 exec, s[58:59]\n\t"
    "global_store_dwordx2 v248, v[128:129], s[42:43] offset:0 sc1\n\t"
    "s_mov_b64 exec, -1\n\t"
    "global_load_dword a127, v88, s[54:55]\n\t"
    "s_add_u32 s54, s54, 0xe0000\n\t"
    "s_addc_u32 s55, s55, 0\n\t"
    "global_load_dword a127, v88, s[54:55]\n\t"
    "s_add_u32 s54, s54, 0xe0000\n\t"
    "s_addc_u32 s55, s55, 0\n\t"
    "s_mov_b64 exec, 0\n\t"
    "s_cmp_eq_u32 s22, 0\n\t"
    "s_cbranch_scc1 Lpad5_%=\n\t"
    "s_cmp_eq_u32 s22, 1\n\t"
    "s_cbranch_scc1 Lpad6_%=\n\t"
    "s_cmp_eq_u32 s22, 2\n\t"
    "s_cbranch_scc1 Lpad7_%=\n\t"
    "s_branch Lpad8_%=\n\t"
    "Lspare2_%=:\n\t"
    "global_load_dword a127, v88, s[54:55]\n\t"
    "s_add_u32 s54, s54, 0xe0000\n\t"
    "s_addc_u32 s55, s55, 0\n\t"
    "global_load_dword a127, v88, s[54:55]\n\t"
    "s_add_u32 s54, s54, 0xe0000\n\t"
    "s_addc_u32 s55, s55, 0\n\t"
    "global_load_dword a127, v89, s[56:57]\n\t"
    "s_add_u32 s56, s56, 0x40000\n\t"
    "s_addc_u32 s57, s57, 0\n\t"
    "global_load_dword a127, v89, s[56:57]\n\t"
    "s_add_u32 s56, s56, 0x40000\n\t"
    "s_addc_u32 s57, s57, 0\n\t"
    "global_load_dword a127, v89, s[56:57]\n\t"
    "s_add_u32 s56, s56, 0x40000\n\t"
    "s_addc_u32 s57, s57, 0\n\t"
    "s_mul_i32 s38, s25, 896\n\t"
    "s_lshl_b32 s39, s22, 3\n\t"
    "s_add_u32 s38, s38, s39\n\t"
    "s_lshl_b32 s38, s38, 11\n\t"
    "s_lshl_b32 s39, s23, 8\n\t"
    "s_add_u32 s38, s38, s39\n\t"
    "s_add_u32 s96, s14, s38\n\t"
    "s_addc_u32 s97, s15, 0\n\t"
    "s_and_b32 s97, s97, 0xffff\n\t"
    "s_mov_b32 s98, 0x1b3900\n\t"
    "s_mov_b32 s99, 0x20000\n\t"
    "v_and_b32 v227, 31, v245\n\t"
    "v_subrev_u32 v224, 4, v227\n\t"
    "v_mul_u32_u24 v225, 43, v224\n\t"
    "v_lshrrev_b32 v225, 8, v225\n\t"
    "v_mul_u32_u24 v226, 6, v225\n\t"
    "v_sub_u32 v224, v224, v226\n\t"
    "v_add3_u32 v224, v224, s26, 1\n\t"
    "v_subrev_u32 v226, 7, v224\n\t"
    "v_min_u32 v224, v224, v226\n\t"
    "v_lshl_add_u32 v224, v224, 2, v225\n\t"
    "v_lshl_add_u32 v225, s26, 2, v227\n\t"
    "v_cmp_gt_u32 vcc, 4, v227\n\t"
    "s_nop 1\n\t"
    "v_cndmask_b32 v228, v224, v225, vcc\n\t"
    "v_mul_u32_u24 v225, 37, v227\n\t"
    "v_lshrrev_b32 v225, 8, v225\n\t"
    "v_mul_u32_u24 v226, 7, v225\n\t"
    "v_sub_u32 v224, v227, v226\n\t"
    "v_lshl_add_u32 v224, v224, 2, v225\n\t"
    "s_cmp_eq_u32 s26, 7\n\t"
    "s_cselect_b64 vcc, -1, 0\n\t"
    "v_cndmask_b32 v228, v228, v224, vcc\n\t"
    "v_lshlrev_b32 v228, 12, v228\n\t"
    "v_and_b32 v224, 0xffffe000, v228\n\t"
    "v_add_u32 v224, v224, v228\n\t"
    "v_cmp_gt_u32 vcc, 32, v245\n\t"
    "s_nop 1\n\t"
    "v_cndmask_b32 v246, v228, v224, vcc\n\t"
    "v_lshrrev_b32 v249, 1, v245\n\t"
    "v_and_b32 v224, 1, v245\n\t"
    "v_lshl_add_u32 v249, v249, 3, v224\n\t"
    "s_lshl_b32 s38, s22, 7\n\t"
    "s_add_u32 s38, s38, 131072\n\t"
    "v_lshl_add_u32 v249, v249, 6, s38\n\t"
    "global_load_dwordx2 v[250:251], v249, s[20:21] sc1\n\t"
    "v_lshrrev_b32 v225, 3, v245\n\t"
    "v_and_b32 v227, 0x70, v240\n\t"
    "v_lshl_or_b32 v227, v225, 18, v227\n\t"
    "v_and_b32 v226, 3, v245\n\t"
    "v_mad_u32_u24 v228, v226, 36, v225\n\t"
    "v_bfe_u32 v226, v245, 2, 1\n\t"
    "v_mul_u32_u24 v226, 4608, v226\n\t"
    "v_add_u32 v228, v228, v226\n\t"
    "s_mul_i32 s38, s22, 1152\n\t"
    "s_add_u32 s38, s38, 115200\n\t"
    "v_lshl_add_u32 v228, v228, 2, s38\n\t"
    "s_lshl_b32 s38, s22, 16\n\t"
    "s_lshl_b32 s39, s29, 7\n\t"
    "s_add_u32 s38, s38, s39\n\t"
    "s_add_u32 s38, s38, 0x800000\n\t"
    "s_add_u32 s42, s20, s38\n\t"
    "s_addc_u32 s43, s21, 0\n\t"
    "s_waitcnt vmcnt(0)\n\t"
    "v_mov_b32 v230, v250\n\t"
    "v_and_b32 v250, 0xffff0000, v250\n\t"
    "v_cmp_ne_u64 vcc, s[74:75], v[250:251]\n\t"
    "s_nop 1\n\t"
    "s_and_b64 vcc, vcc, -1\n\t"
    "s_cbranch_scc1 Ls40_%=\n\t"
    "Lb41_%=:\n\t"
    "v_and_b32 v231, 0x100, v230\n\t"
    "v_cmp_lt_u32 vcc, 0, v231\n\t"
    "s_nop 1\n\t"
    "s_bcnt1_i32_b64 s6, vcc\n\t"
    "v_and_b32 v230, 0xff, v230\n\t"
    "s_nop 1\n\t"
    "v_max_u32_dpp v230, v230, v230 quad_perm:[1,0,3,2] row_mask:0xf bank_mask:0xf\n\t"
    "s_nop 1\n\t"
    "v_max_u32_dpp v230, v230, v230 quad_perm:[2,3,0,1] row_mask:0xf bank_mask:0xf\n\t"
    "s_nop 1\n\t"
    "v_max_u32_dpp v230, v230, v230 row_half_mirror row_mask:0xf bank_mask:0xf\n\t"
    "s_nop 1\n\t"
    "v_max_u32_dpp v230, v230, v230 row_mirror row_mask:0xf bank_mask:0xf\n\t"
    "v_mov_b32 v231, v230\n\t"
    "s_nop 1\n\t"
    "v_permlane16_swap_b32 v230, v231\n\t"
    "v_max_u32 v230, v230, v231\n\t"
    "v_mov_b32 v231, v230\n\t"
    "s_nop 1\n\t"
    "v_permlane32_swap_b32 v230, v231\n\t"
    "v_max_u32 v230, v230, v231\n\t"
    "v_lshl_or_b32 v230, v230, 8, s6\n\t"
    "s_lshl_b32 s38, s22, 2\n\t"
    "s_add_u32 s38, s38, 152336\n\t"
    "v_mov_b32 v231, s38\n\t"
    "ds_write_b32 v231, v230\n\t"
    "global_load_dwordx4 v[128:131], v227, s[42:43] sc1\n\t"
    "s_add_u32 s42, s42, 0x200000\n\t"
    "s_addc_u32 s43, s43, 0\n\t"
    "global_load_dwordx4 v[132:135], v227, s[42:43] sc1\n\t"
    "s_add_u32 s42, s42, 0x200000\n\t"
    "s_addc_u32 s43, s43, 0\n\t"
    "global_load_dwordx4 v[136:139], v227, s[42:43] sc1\n\t"
    "s_add_u32 s42, s42, 0x200000\n\t"
    "s_addc_u32 s43, s43, 0\n\t"
    "global_load_dwordx4 v[140:143], v227, s[42:43] sc1\n\t"
    "s_sub_u32 s42, s42, 0x5f8000\n\t"
    "s_subb_u32 s43, s43, 0\n\t"
    "global_load_dwordx4 v[144:147], v227, s[42:43] sc1\n\t"
    "s_add_u32 s42, s42, 0x200000\n\t"
    "s_addc_u32 s43, s43, 0\n\t"
    "global_load_dwordx4 v[148:151], v227, s[42:43] sc1\n\t"
    "s_add_u32 s42, s42, 0x200000\n\t"
    "s_addc_u32 s43, s43, 0\n\t"
    "global_load_dwordx4 v[152:155], v227, s[42:43] sc1\n\t"
    "s_add_u32 s42, s42, 0x200000\n\t"
    "s_addc_u32 s43, s43, 0\n\t"
    "global_load_dwordx4 v[156:159], v227, s[42:43] sc1\n\t"
    "s_cmp_eq_u32 s29, 0\n\t"
    "s_cbranch_scc1 Lt43_%=\n\t"
    "Lr44_%=:\n\t"
    "v_readlane_b32 s24, v246, 32\n\t"
    "s_add_u32 m0, s31, 32896\n\t"
    "s_nop 0\n\t"
    "s_lshl_b32 s35, s24, 4\n\t"
    "buffer_load_dwordx4 v243, s[96:99], s35 offen sc1 lds\n\t"
    "buffer_load_dwordx4 v244, s[96:99], s35 offen offset:1024 sc1 lds\n\t"
    "v_readlane_b32 s24, v246, 33\n\t"
    "s_add_u32 m0, s31, 41120\n\t"
    "s_nop 0\n\t"
    "s_lshl_b32 s35, s24, 4\n\t"
    "buffer_load_dwordx4 v243, s[96:99], s35 offen sc1 lds\n\t"
    "buffer_load_dwordx4 v244, s[96:99], s35 offen offset:1024 sc1 lds\n\t"
    "v_readlane_b32 s24, v246, 34\n\t"
    "s_add_u32 m0, s31, 49344\n\t"
    "s_nop 0\n\t"
    "s_lshl_b32 s35, s24, 4\n\t"
    "buffer_load_dwordx4 v243, s[96:99], s35 offen sc1 lds\n\t"
    "buffer_load_dwordx4 v244, s[96:99], s35 offen offset:1024 sc1 lds\n\t"
    "v_readlane_b32 s24, v246, 35\n\t"
    "s_add_u32 m0, s31, 57568\n\t"
    "s_nop 0\n\t"
    "s_lshl_b32 s35, s24, 4\n\t"
    "buffer_load_dwordx4 v243, s[96:99], s35 offen sc1 lds\n\t"
    "buffer_load_dwordx4 v244, s[96:99], s35 offen offset:1024 sc1 lds\n\t"
    "global_load_dword a127, v89, s[56:57]\n\t"
    "s_add_u32 s56, s56, 0x40000\n\t"
    "s_addc_u32 s57, s57, 0\n\t"
    "v_and_b32 v2, 31, v245\n\t"
    "v_lshrrev_b32 v3, 5, v245\n\t"
    "v_mad_u32_u24 v2, v2, 36, v3\n\t"
    "s_lshr_b32 s38, s22, 1\n\t"
    "s_mul_i32 s38, s38, 4604\n\t"
    "s_lshl_b32 s39, s22, 1\n\t"
    "s_add_u32 s38, s38, s39\n\t"
    "v_lshl_add_u32 v2, v2, 2, s38\n\t"
    "s_waitcnt vmcnt(16)\n\t"
    "ds_write_b32 v228, v128 offset:0\n\t"
    "ds_write_b32 v228, v129 offset:4608\n\t"
    "ds_write_b32 v228, v130 offset:9216\n\t"
    "ds_write_b32 v228, v131 offset:13824\n\t"
    "s_waitcnt vmcnt(15)\n\t"
    "ds_write_b32 v228, v132 offset:32\n\t"
    "ds_write_b32 v228, v133 offset:4640\n\t"
    "ds_write_b32 v228, v134 offset:9248\n\t"
    "ds_write_b32 v228, v135 offset:13856\n\t"
    "s_waitcnt vmcnt(14)\n\t"
    "ds_write_b32 v228, v136 offset:64\n\t"
    "ds_write_b32 v228, v137 offset:4672\n\t"
    "ds_write_b32 v228, v138 offset:9280\n\t"
    "ds_write_b32 v228, v139 offset:13888\n\t"
    "s_waitcnt vmcnt(13)\n\t"
    "ds_write_b32 v228, v140 offset:96\n\t"
    "ds_write_b32 v228, v141 offset:4704\n\t"
    "ds_write_b32 v228, v142 offset:9312\n\t"
    "ds_write_b32 v228, v143 offset:13920\n\t"
    "s_waitcnt vmcnt(12)\n\t"
    "ds_write_b32 v228, v144 offset:576\n\t"
    "ds_write_b32 v228, v145 offset:5184\n\t"
    "ds_write_b32 v228, v146 offset:9792\n\t"
    "ds_write_b32 v228, v147 offset:14400\n\t"
    "s_waitcnt vmcnt(11)\n\t"
    "ds_write_b32 v228, v148 offset:608\n\t"
    "ds_write_b32 v228, v149 offset:5216\n\t"
    "ds_write_b32 v228, v150 offset:9824\n\t"
    "ds_write_b32 v228, v151 offset:14432\n\t"
    "s_waitcnt vmcnt(10)\n\t"
    "ds_write_b32 v228, v152 offset:640\n\t"
    "ds_write_b32 v228, v153 offset:5248\n\t"
    "ds_write_b32 v228, v154 offset:9856\n\t"
    "ds_write_b32 v228, v155 offset:14464\n\t"
    "s_waitcnt vmcnt(9)\n\t"
    "ds_write_b32 v228, v156 offset:672\n\t"
    "ds_write_b32 v228, v157 offset:5280\n\t"
    "ds_write_b32 v228, v158 offset:9888\n\t"
    "ds_write_b32 v228, v159 offset:14496\n\t"
    "ds_write_b16 v2, v16 offset:0\n\t"
    "ds_write_b16 v2, v17 offset:8\n\t"
    "ds_write_b16 v2, v18 offset:16\n\t"
    "ds_write_b16 v2, v19 offset:24\n\t"
    "ds_write_b16 v2, v20 offset:32\n\t"
    "ds_write_b16 v2, v21 offset:40\n\t"
    "ds_write_b16 v2, v22 offset:48\n\t"
    "ds_write_b16 v2, v23 offset:56\n\t"
    "ds_write_b16 v2, v24 offset:64\n\t"
    "ds_write_b16 v2, v25 offset:72\n\t"
    "ds_write_b16 v2, v26 offset:80\n\t"
    "ds_write_b16 v2, v27 offset:88\n\t"
    "ds_write_b16 v2, v28 offset:96\n\t"
    "ds_write_b16 v2, v29 offset:104\n\t"
    "ds_write_b16 v2, v30 offset:112\n\t"
    "ds_write_b16 v2, v31 offset:120\n\t"
    "s_waitcnt lgkmcnt(0)\n\t"
    "s_barrier\n\t"
    "global_load_dword a127, v89, s[56:57]\n\t"
    "s_add_u32 s56, s56, 0x40000\n\t"
    "s_addc_u32 s57, s57, 0\n\t"
    "global_load_dword a127, v89, s[56:57]\n\t"
    "s_add_u32 s56, s56, 0x40000\n\t"
    "s_addc_u32 s57, s57, 0\n\t"
    "s_cmp_eq_u32 s29, 8\n\t"
    "s_cselect_b64 exec, s[58:59], 0\n\t"
    "global_store_dwordx2 v248, v[254:255], s[20:21] sc1\n\t"
    "s_mov_b64 exec, -1\n\t"
    "v_mov_b32 v228, 152336\n\t"
    "ds_read_b128 v[228:231], v228\n\t"
    "s_waitcnt lgkmcnt(0)\n\t"
    "v_add3_u32 v227, v228, v229, v230\n\t"
    "v_add_u32 v227, v227, v231\n\t"
    "s_nop 0\n\t"
    "v_readfirstlane_b32 s6, v227\n\t"
    "s_nop 1\n\t"
    "s_and_b32 s6, s6, 0xff\n\t"
    "s_cmp_gt_u32 s6, 14\n\t"
    "s_cselect_b32 s6, 0x2000, 0\n\t"
    "s_xor_b32 s30, s30, s6\n\t"
    "s_lshl_b32 s6, s6, 8\n\t"
    "s_add_u32 s4, s4, s6\n\t"
    "s_addc_u32 s5, s5, 0\n\t"
    "v_max3_u32 v228, v228, v229, v230\n\t"
    "v_max_u32 v228, v228, v231\n\t"
    "s_nop 0\n\t"
    "v_readfirstlane_b32 s62, v228\n\t"
    "s_nop 1\n\t"
    "s_lshr_b32 s62, s62, 8\n\t"
    "s_max_u32 s62, s62, 29\n\t"
    "s_mov_b32 s60, 0x04000400\n\t"
    "s_sub_u32 s72, s62, 29\n\t"
    "s_mul_i32 s72, s72, 0x800080\n\t"
    "s_mov_b32 s73, 0x7fff7fff\n\t"
    "s_sub_u32 s62, s62, 1\n\t"
    "s_lshl_b32 s62, s62, 23\n\t"
    "s_mov_b32 s63, s62\n\t"
    "v_and_b32 v224, 31, v245\n\t"
    "v_lshrrev_b32 v225, 5, v245\n\t"
    "v_mul_u32_u24 v224, 144, v224\n\t"
    "v_lshl_add_u32 v225, v225, 4, v224\n\t"
    "s_mul_i32 s38, s22, 4608\n\t"
    "s_add_u32 s38, s38, 115200\n\t"
    "v_add_u32 v224, s38, v225\n\t"
    "s_and_b32 s39, s22, 1\n\t"
    "s_mul_i32 s39, s39, 9216\n\t"
    "s_add_u32 s39, s39, 133632\n\t"
    "s_lshl_b32 s38, s29, 2\n\t"
    "s_add_u32 s38, s38, s22\n\t"
    "s_cmp_eq_u32 s38, 2\n\t"
    "s_cselect_b32 s39, 0, s39\n\t"
    "v_add_u32 v225, s39, v225\n\t"
    "ds_read_b128 v[128:131], v225 offset:0\n\t"
    "ds_read_b128 v[144:147], v225 offset:4608\n\t"
    "ds_read_b128 v[132:135], v225 offset:32\n\t"
    "ds_read_b128 v[148:151], v225 offset:4640\n\t"
    "ds_read_b128 v[136:139], v225 offset:64\n\t"
    "ds_read_b128 v[152:155], v225 offset:4672\n\t"
    "ds_read_b128 v[140:143], v225 offset:96\n\t"
    "ds_read_b128 v[156:159], v225 offset:4704\n\t"
    "ds_read_b128 v[208:211], v224 offset:0\n\t"
    "ds_read_b128 v[212:215], v224 offset:32\n\t"
    "ds_read_b128 v[216:219], v224 offset:64\n\t"
    "ds_read_b128 v[220:223], v224 offset:96\n\t"
    "s_waitcnt lgkmcnt(11)\n\t"
    "v_and_b32 v226, s73, v128\n\t"
    "v_pk_sub_u16 v226, v226, s72 clamp\n\t"
    "v_pk_lshlrev_b16 v226, 3, v226 op_sel_hi:[0,1]\n\t"
    "v_bfi_b32 v226, s73, v226, v128\n\t"
    "s_waitcnt lgkmcnt(10)\n\t"
    "v_and_b32 v227, s73, v144\n\t"
    "v_pk_sub_u16 v227, v227, s72 clamp\n\t"
    "v_pk_lshlrev_b16 v227, 3, v227 op_sel_hi:[0,1]\n\t"
    "v_bfi_b32 v227, s73, v227, v144\n\t"
    "v_pk_mul_f16 v226, v226, s60\n\t"
    "v_pk_fma_f16 v128, v227, s60, v226\n\t"
    "v_pk_fma_f16 v144, v227, s60, v226 neg_lo:[1,0,0] neg_hi:[1,0,0]\n\t"
    "v_and_b32 v228, s73, v129\n\t"
    "v_pk_sub_u16 v228, v228, s72 clamp\n\t"
    "v_pk_lshlrev_b16 v228, 3, v228 op_sel_hi:[0,1]\n\t"
    "v_bfi_b32 v228, s73, v228, v129\n\t"
    "v_and_b32 v229, s73, v145\n\t"
    "v_pk_sub_u16 v229, v229, s72 clamp\n\t"
    "v_pk_lshlrev_b16 v229, 3, v229 op_sel_hi:[0,1]\n\t"
    "v_bfi_b32 v229, s73, v229, v145\n\t"
    "v_pk_mul_f16 v228, v228, s60\n\t"
    "v_pk_fma_f16 v129, v229, s60, v228\n\t"
    "v_pk_fma_f16 v145, v229, s60, v228 neg_lo:[1,0,0] neg_hi:[1,0,0]\n\t"
    "v_and_b32 v226, s73, v130\n\t"
    "v_pk_sub_u16 v226, v226, s72 clamp\n\t"
    "v_pk_lshlrev_b16 v226, 3, v226 op_sel_hi:[0,1]\n\t"
    "v_bfi_b32 v226, s73, v226, v130\n\t"
    "v_and_b32 v227, s73, v146\n\t"
    "v_pk_sub_u16 v227, v227, s72 clamp\n\t"
    "v_pk_lshlrev_b16 v227, 3, v227 op_sel_hi:[0,1]\n\t"
    "v_bfi_b32 v227, s73, v227, v146\n\t"
    "v_pk_mul_f16 v226, v226, s60\n\t"
    "v_pk_fma_f16 v130, v227, s60, v226\n\t"
    "v_pk_fma_f16 v146, v227, s60, v226 neg_lo:[1,0,0] neg_hi:[1,0,0]\n\t"
    "v_and_b32 v228, s73, v131\n\t"
    "v_pk_sub_u16 v228, v228, s72 clamp\n\t"
    "v_pk_lshlrev_b16 v228, 3, v228 op_sel_hi:[0,1]\n\t"
    "v_bfi_b32 v228, s73, v228, v131\n\t"
    "v_and_b32 v229, s73, v147\n\t"
    "v_pk_sub_u16 v229, v229, s72 clamp\n\t"
    "v_pk_lshlrev_b16 v229, 3, v229 op_sel_hi:[0,1]\n\t"
    "v_bfi_b32 v229, s73, v229, v147\n\t"
    "v_pk_mul_f16 v228, v228, s60\n\t"
    "v_pk_fma_f16 v131, v229, s60, v228\n\t"
    "v_pk_fma_f16 v147, v229, s60, v228 neg_lo:[1,0,0] neg_hi:[1,0,0]\n\t"
    "s_waitcnt lgkmcnt(9)\n\t"
    "v_and_b32 v226, s73, v132\n\t"
    "v_pk_sub_u16 v226, v226, s72 clamp\n\t"
    "v_pk_lshlrev_b16 v226, 3, v226 op_sel_hi:[0,1]\n\t"
    "v_bfi_b32 v226, s73, v226, v132\n\t"
    "s_waitcnt lgkmcnt(8)\n\t"
    "v_and_b32 v227, s73, v148\n\t"
    "v_pk_sub_u16 v227, v227, s72 clamp\n\t"
    "v_pk_lshlrev_b16 v227, 3, v227 op_sel_hi:[0,1]\n\t"
    "v_bfi_b32 v227, s73, v227, v148\n\t"
    "v_pk_mul_f16 v226, v226, s60\n\t"
    "v_pk_fma_f16 v132, v227, s60, v226\n\t"
    "v_pk_fma_f16 v148, v227, s60, v226 neg_lo:[1,0,0] neg_hi:[1,0,0]\n\t"
    "v_and_b32 v228, s73, v133\n\t"
    "v_pk_sub_u16 v228, v228, s72 clamp\n\t"
    "v_pk_lshlrev_b16 v228, 3, v228 op_sel_hi:[0,1]\n\t"
    "v_bfi_b32 v228, s73, v228, v133\n\t"
    "v_and_b32 v229, s73, v149\n\t"
    "v_pk_sub_u16 v229, v229, s72 clamp\n\t"
    "v_pk_lshlrev_b16 v229, 3, v229 op_sel_hi:[0,1]\n\t"
    "v_bfi_b32 v229, s73, v229, v149\n\t"
    "v_pk_mul_f16 v228, v228, s60\n\t"
    "v_pk_fma_f16 v133, v229, s60, v228\n\t"
    "v_pk_fma_f16 v149, v229, s60, v228 neg_lo:[1,0,0] neg_hi:[1,0,0]\n\t"
    "v_and_b32 v226, s73, v134\n\t"
    "v_pk_sub_u16 v226, v226, s72 clamp\n\t"
    "v_pk_lshlrev_b16 v226, 3, v226 op_sel_hi:[0,1]\n\t"
    "v_bfi_b32 v226, s73, v226, v134\n\t"
    "v_and_b32 v227, s73, v150\n\t"
    "v_pk_sub_u16 v227, v227, s72 clamp\n\t"
    "v_pk_lshlrev_b16 v227, 3, v227 op_sel_hi:[0,1]\n\t"
    "v_bfi_b32 v227, s73, v227, v150\n\t"
    "v_pk_mul_f16 v226, v226, s60\n\t"
    "v_pk_fma_f16 v134, v227, s60, v226\n\t"
    "v_pk_fma_f16 v150, v227, s60, v226 neg_lo:[1,0,0] neg_hi:[1,0,0]\n\t"
    "v_and_b32 v228, s73, v135\n\t"
    "v_pk_sub_u16 v228, v228, s72 clamp\n\t"
    "v_pk_lshlrev_b16 v228, 3, v228 op_sel_hi:[0,1]\n\t"
    "v_bfi_b32 v228, s73, v228, v135\n\t"
    "v_and_b32 v229, s73, v151\n\t"
    "v_pk_sub_u16 v229, v229, s72 clamp\n\t"
    "v_pk_lshlrev_b16 v229, 3, v229 op_sel_hi:[0,1]\n\t"
    "v_bfi_b32 v229, s73, v229, v151\n\t"
    "v_pk_mul_f16 v228, v228, s60\n\t"
    "v_pk_fma_f16 v135, v229, s60, v228\n\t"
    "v_pk_fma_f16 v151, v229, s60, v228 neg_lo:[1,0,0] neg_hi:[1,0,0]\n\t"
    "s_waitcnt lgkmcnt(7)\n\t"
    "v_and_b32 v226, s73, v136\n\t"
    "v_pk_sub_u16 v226, v226, s72 clamp\n\t"
    "v_pk_lshlrev_b16 v226, 3, v226 op_sel_hi:[0,1]\n\t"
    "v_bfi_b32 v226, s73, v226, v136\n\t"
    "s_waitcnt lgkmcnt(6)\n\t"
    "v_and_b32 v227, s73, v152\n\t"
    "v_pk_sub_u16 v227, v227, s72 clamp\n\t"
    "v_pk_lshlrev_b16 v227, 3, v227 op_sel_hi:[0,1]\n\t"
    "v_bfi_b32 v227, s73, v227, v152\n\t"
    "v_pk_mul_f16 v226, v226, s60\n\t"
    "v_pk_fma_f16 v136, v227, s60, v226\n\t"
    "v_pk_fma_f16 v152, v227, s60, v226 neg_lo:[1,0,0] neg_hi:[1,0,0]\n\t"
    "v_and_b32 v228, s73, v137\n\t"
    "v_pk_sub_u16 v228, v228, s72 clamp\n\t"
    "v_pk_lshlrev_b16 v228, 3, v228 op_sel_hi:[0,1]\n\t"
    "v_bfi_b32 v228, s73, v228, v137\n\t"
    "v_and_b32 v229, s73, v153\n\t"
    "v_pk_sub_u16 v229, v229, s72 clamp\n\t"
    "v_pk_lshlrev_b16 v229, 3, v229 op_sel_hi:[0,1]\n\t"
    "v_bfi_b32 v229, s73, v229, v153\n\t"
    "v_pk_mul_f16 v228, v228, s60\n\t"
    "v_pk_fma_f16 v137, v229, s60, v228\n\t"
    "v_pk_fma_f16 v153, v229, s60, v228 neg_lo:[1,0,0] neg_hi:[1,0,0]\n\t"
    "v_and_b32 v226, s73, v138\n\t"
    "v_pk_sub_u16 v226, v226, s72 clamp\n\t"
    "v_pk_lshlrev_b16 v226, 3, v226 op_sel_hi:[0,1]\n\t"
    "v_bfi_b32 v226, s73, v226, v138\n\t"
    "v_and_b32 v227, s73, v154\n\t"
    "v_pk_sub_u16 v227, v227, s72 clamp\n\t"
    "v_pk_lshlrev_b16 v227, 3, v227 op_sel_hi:[0,1]\n\t"
    "v_bfi_b32 v227, s73, v227, v154\n\t"
    "v_pk_mul_f16 v226, v226, s60\n\t"
    "v_pk_fma_f16 v138, v227, s60, v226\n\t"
    "v_pk_fma_f16 v154, v227, s60, v226 neg_lo:[1,0,0] neg_hi:[1,0,0]\n\t"
    "v_and_b32 v228, s73, v139\n\t"
    "v_pk_sub_u16 v228, v228, s72 clamp\n\t"
    "v_pk_lshlrev_b16 v228, 3, v228 op_sel_hi:[0,1]\n\t"
    "v_bfi_b32 v228, s73, v228, v139\n\t"
    "v_and_b32 v229, s73, v155\n\t"
    "v_pk_sub_u16 v229, v229, s72 clamp\n\t"
    "v_pk_lshlrev_b16 v229, 3, v229 op_sel_hi:[0,1]\n\t"
    "v_bfi_b32 v229, s73, v229, v155\n\t"
    "v_pk_mul_f16 v228, v228, s60\n\t"
    "v_pk_fma_f16 v139, v229, s60, v228\n\t"
    "v_pk_fma_f16 v155, v229, s60, v228 neg_lo:[1,0,0] neg_hi:[1,0,0]\n\t"
    "s_waitcnt lgkmcnt(5)\n\t"
    "v_and_b32 v226, s73, v140\n\t"
    "v_pk_sub_u16 v226, v226, s72 clamp\n\t"
    "v_pk_lshlrev_b16 v226, 3, v226 op_sel_hi:[0,1]\n\t"
    "v_bfi_b32 v226, s73, v226, v140\n\t"
    "s_waitcnt lgkmcnt(4)\n\t"
    "v_and_b32 v227, s73, v156\n\t"
    "v_pk_sub_u16 v227, v227, s72 clamp\n\t"
    "v_pk_lshlrev_b16 v227, 3, v227 op_sel_hi:[0,1]\n\t"
    "v_bfi_b32 v227, s73, v227, v156\n\t"
    "v_pk_mul_f16 v226, v226, s60\n\t"
    "v_pk_fma_f16 v140, v227, s60, v226\n\t"
    "v_pk_fma_f16 v156, v227, s60, v226 neg_lo:[1,0,0] neg_hi:[1,0,0]\n\t"
    "v_and_b32 v228, s73, v141\n\t"
    "v_pk_sub_u16 v228, v228, s72 clamp\n\t"
    "v_pk_lshlrev_b16 v228, 3, v228 op_sel_hi:[0,1]\n\t"
    "v_bfi_b32 v228, s73, v228, v141\n\t"
    "v_and_b32 v229, s73, v157\n\t"
    "v_pk_sub_u16 v229, v229, s72 clamp\n\t"
    "v_pk_lshlrev_b16 v229, 3, v229 op_sel_hi:[0,1]\n\t"
    "v_bfi_b32 v229, s73, v229, v157\n\t"
    "v_pk_mul_f16 v228, v228, s60\n\t"
    "v_pk_fma_f16 v141, v229, s60, v228\n\t"
    "v_pk_fma_f16 v157, v229, s60, v228 neg_lo:[1,0,0] neg_hi:[1,0,0]\n\t"
    "v_and_b32 v226, s73, v142\n\t"
    "v_pk_sub_u16 v226, v226, s72 clamp\n\t"
    "v_pk_lshlrev_b16 v226, 3, v226 op_sel_hi:[0,1]\n\t"
    "v_bfi_b32 v226, s73, v226, v142\n\t"
    "v_and_b32 v227, s73, v158\n\t"
    "v_pk_sub_u16 v227, v227, s72 clamp\n\t"
    "v_pk_lshlrev_b16 v227, 3, v227 op_sel_hi:[0,1]\n\t"
    "v_bfi_b32 v227, s73, v227, v158\n\t"
    "v_pk_mul_f16 v226, v226, s60\n\t"
    "v_pk_fma_f16 v142, v227, s60, v226\n\t"
    "v_pk_fma_f16 v158, v227, s60, v226 neg_lo:[1,0,0] neg_hi:[1,0,0]\n\t"
    "v_and_b32 v228, s73, v143\n\t"
    "v_pk_sub_u16 v228, v228, s72 clamp\n\t"
    "v_pk_lshlrev_b16 v228, 3, v228 op_sel_hi:[0,1]\n\t"
    "v_bfi_b32 v228, s73, v228, v143\n\t"
    "v_and_b32 v229, s73, v159\n\t"
    "v_pk_sub_u16 v229, v229, s72 clamp\n\t"
    "v_pk_lshlrev_b16 v229, 3, v229 op_sel_hi:[0,1]\n\t"
    "v_bfi_b32 v229, s73, v229, v159\n\t"
    "v_pk_mul_f16 v228, v228, s60\n\t"
    "v_pk_fma_f16 v143, v229, s60, v228\n\t"
    "v_pk_fma_f16 v159, v229, s60, v228 neg_lo:[1,0,0] neg_hi:[1,0,0]\n\t"
    "global_load_dword a127, v89, s[56:57]\n\t"
    "s_add_u32 s56, s56, 0x40000\n\t"
    "s_addc_u32 s57, s57, 0\n\t"
    "s_and_b32 s38, s22, 1\n\t"
    "s_or_b32 s38, s38, s29\n\t"
    "s_cbranch_scc0 Lu45_%=\n\t"
    "Lv46_%=:\n\t"
    "v_pk_fma_f16 v128, v144, v176, v128 op_sel_hi:[1,0,1]\n\t"
    "v_pk_mul_f16 v144, v144, v176 op_sel:[0,1] op_sel_hi:[1,1]\n\t"
    "v_pk_fma_f16 v129, v145, v177, v129 op_sel_hi:[1,0,1]\n\t"
    "v_pk_mul_f16 v145, v145, v177 op_sel:[0,1] op_sel_hi:[1,1]\n\t"
    "v_pk_fma_f16 v130, v146, v178, v130 op_sel_hi:[1,0,1]\n\t"
    "v_pk_mul_f16 v146, v146, v178 op_sel:[0,1] op_sel_hi:[1,1]\n\t"
    "v_pk_fma_f16 v131, v147, v179, v131 op_sel_hi:[1,0,1]\n\t"
    "v_pk_mul_f16 v147, v147, v179 op_sel:[0,1] op_sel_hi:[1,1]\n\t"
    "v_pk_fma_f16 v132, v148, v180, v132 op_sel_hi:[1,0,1]\n\t"
    "v_pk_mul_f16 v148, v148, v180 op_sel:[0,1] op_sel_hi:[1,1]\n\t"
    "v_pk_fma_f16 v133, v149, v181, v133 op_sel_hi:[1,0,1]\n\t"
    "v_pk_mul_f16 v149, v149, v181 op_sel:[0,1] op_sel_hi:[1,1]\n\t"
    "v_pk_fma_f16 v134, v150, v182, v134 op_sel_hi:[1,0,1]\n\t"
    "v_pk_mul_f16 v150, v150, v182 op_sel:[0,1] op_sel_hi:[1,1]\n\t"
    "v_pk_fma_f16 v135, v151, v183, v135 op_sel_hi:[1,0,1]\n\t"
    "v_pk_mul_f16 v151, v151, v183 op_sel:[0,1] op_sel_hi:[1,1]\n\t"
    "v_pk_fma_f16 v136, v152, v184, v136 op_sel_hi:[1,0,1]\n\t"
    "v_pk_mul_f16 v152, v152, v184 op_sel:[0,1] op_sel_hi:[1,1]\n\t"
    "v_pk_fma_f16 v137, v153, v185, v137 op_sel_hi:[1,0,1]\n\t"
    "v_pk_mul_f16 v153, v153, v185 op_sel:[0,1] op_sel_hi:[1,1]\n\t"
    "v_pk_fma_f16 v138, v154, v186, v138 op_sel_hi:[1,0,1]\n\t"
    "v_pk_mul_f16 v154, v154, v186 op_sel:[0,1] op_sel_hi:[1,1]\n\t"
    "v_pk_fma_f16 v139, v155, v187, v139 op_sel_hi:[1,0,1]\n\t"
    "v_pk_mul_f16 v155, v155, v187 op_sel:[0,1] op_sel_hi:[1,1]\n\t"
    "v_pk_fma_f16 v140, v156, v188, v140 op_sel_hi:[1,0,1]\n\t"
    "v_pk_mul_f16 v156, v156, v188 op_sel:[0,1] op_sel_hi:[1,1]\n\t"
    "v_pk_fma_f16 v141, v157, v189, v141 op_sel_hi:[1,0,1]\n\t"
    "v_pk_mul_f16 v157, v157, v189 op_sel:[0,1] op_sel_hi:[1,1]\n\t"
    "v_pk_fma_f16 v142, v158, v190, v142 op_sel_hi:[1,0,1]\n\t"
    "v_pk_mul_f16 v158, v158, v190 op_sel:[0,1] op_sel_hi:[1,1]\n\t"
    "v_pk_fma_f16 v143, v159, v191, v143 op_sel_hi:[1,0,1]\n\t"
    "v_pk_mul_f16 v159, v159, v191 op_sel:[0,1] op_sel_hi:[1,1]\n\t"
    "s_lshl_b32 s38, s22, 12\n\t"
    "s_add_u32 s39, s38, 133632\n\t"
    "v_add_u32 v241, s39, v240\n\t"
    "s_xor_b32 s39, s38, 0x2000\n\t"
    "s_add_u32 s39, s39, 133632\n\t"
    "v_add_u32 v242, s39, v240\n\t"
    "s_lshl_b32 s38, s22, 12\n\t"
    "s_xor_b32 s38, s38, s30\n\t"
    "s_and_b32 s39, s29, 255\n\t"
    "s_lshl_b32 s39, s39, 14\n\t"
    "s_add_u32 s38, s38, s39\n\t"
    "s_add_u32 s38, s38, 0x1000000\n\t"
    "s_add_u32 s50, s20, s38\n\t"
    "s_addc_u32 s51, s21, 0\n\t"
    "v_and_b32 v249, 7, v245\n\t"
    "v_min_u32 v249, 6, v249\n\t"
    "s_and_b32 s38, s2, 0xc7\n\t"
    "s_lshl_b32 s38, s38, 2\n\t"
    "s_add_u32 s38, s38, s22\n\t"
    "s_lshl_b32 s38, s38, 9\n\t"
    "s_add_u32 s38, s38, 262144\n\t"
    "s_add_u32 s38, s38, s6\n\t"
    "v_lshl_add_u32 v249, v249, 14, s38\n\t"
    "s_lshl_b32 s38, s29, 6\n\t"
    "s_add_u32 s38, s38, 0x1c000\n\t"
    "s_add_u32 s52, s20, s38\n\t"
    "s_addc_u32 s53, s21, 0\n\t"
    "s_and_b32 s41, s27, 15\n\t"
    "s_lshl_b32 s41, s41, 2\n\t"
    "s_add_u32 s41, s41, s25\n\t"
    "s_lshl_b32 s41, s41, 18\n\t"
    "s_lshr_b32 s38, s41, 8\n\t"
    "s_xor_b32 s38, s38, s30\n\t"
    "s_and_b32 s38, s38, 0x2000\n\t"
    "s_or_b32 s41, s41, s38\n\t"
    "s_add_u32 s41, s41, 20971520\n\t"
    "s_lshl_b32 s38, s26, 15\n\t"
    "s_add_u32 s38, s38, s41\n\t"
    "s_add_u32 s38, s38, 0x1000\n\t"
    "s_add_u32 s46, s20, s38\n\t"
    "s_addc_u32 s47, s21, 0\n\t"
    "s_add_u32 s64, s20, s41\n\t"
    "s_addc_u32 s65, s21, 0\n\t"
    "s_and_b32 s65, s65, 0xffff\n\t"
    "s_mov_b32 s66, 0x36000\n\t"
    "s_mov_b32 s67, 0x20000\n\t"
    "s_waitcnt vmcnt(4)\n\t"
    "s_waitcnt lgkmcnt(0)\n\t"
    "s_barrier\n\t"
    "v_mfma_f32_32x32x16_f16 v[0:15], v[208:211], a[224:227], 0\n\t"
    "v_mfma_f32_32x32x16_f16 v[16:31], v[208:211], a[240:243], 0\n\t"
    "v_mfma_f32_32x32x16_f16 v[0:15], v[212:215], a[228:231], v[0:15]\n\t"
    "v_mfma_f32_32x32x16_f16 v[16:31], v[212:215], a[244:247], v[16:31]\n\t"
    "v_mfma_f32_32x32x16_f16 v[0:15], v[216:219], a[232:235], v[0:15]\n\t"
    "v_mfma_f32_32x32x16_f16 v[16:31], v[216:219], a[248:251], v[16:31]\n\t"
    "v_mfma_f32_32x32x16_f16 v[0:15], v[220:223], a[236:239], v[0:15]\n\t"
    "v_mfma_f32_32x32x16_f16 v[16:31], v[220:223], a[252:255], v[16:31]\n\t"
    "s_nop 11\n\t"
    "v_cvt_pk_f16_f32 v80, v0, v16\n\t"
    "v_cvt_pk_f16_f32 v81, v1, v17\n\t"
    "v_pk_mul_f16 v32, v80, v160 op_sel_hi:[1,0]\n\t"
    "v_pk_mul_f16 v33, v81, v161 op_sel_hi:[1,0]\n\t"
    "v_pk_fma_f16 v32, v80, v160, v32 op_sel:[1,1,0] op_sel_hi:[0,1,1] neg_hi:[1,0,0]\n\t"
    "v_pk_fma_f16 v33, v81, v161, v33 op_sel:[1,1,0] op_sel_hi:[0,1,1] neg_hi:[1,0,0]\n\t"
    "v_cvt_pk_f16_f32 v82, v2, v18\n\t"
    "v_cvt_pk_f16_f32 v83, v3, v19\n\t"
    "v_pk_mul_f16 v34, v82, v162 op_sel_hi:[1,0]\n\t"
    "v_pk_mul_f16 v35, v83, v163 op_sel_hi:[1,0]\n\t"
    "v_pk_fma_f16 v34, v82, v162, v34 op_sel:[1,1,0] op_sel_hi:[0,1,1] neg_hi:[1,0,0]\n\t"
    "v_pk_fma_f16 v35, v83, v163, v35 op_sel:[1,1,0] op_sel_hi:[0,1,1] neg_hi:[1,0,0]\n\t"
    "v_cvt_pk_f16_f32 v80, v4, v20\n\t"
    "v_cvt_pk_f16_f32 v81, v5, v21\n\t"
    "v_mfma_f32_32x32x16_f16 v[48:63], a[224:227], v[32:35], 0\n\t"
    "v_pk_mul_f16 v36, v80, v164 op_sel_hi:[1,0]\n\t"
    "v_pk_mul_f16 v37, v81, v165 op_sel_hi:[1,0]\n\t"
    "v_pk_fma_f16 v36, v80, v164, v36 op_sel:[1,1,0] op_sel_hi:[0,1,1] neg_hi:[1,0,0]\n\t"
    "v_pk_fma_f16 v37, v81, v165, v37 op_sel:[1,1,0] op_sel_hi:[0,1,1] neg_hi:[1,0,0]\n\t"
    "v_cvt_pk_f16_f32 v82, v6, v22\n\t"
    "v_cvt_pk_f16_f32 v83, v7, v23\n\t"
    "v_mfma_f32_32x32x16_f16 v[64:79], a[240:243], v[32:35], 0\n\t"
    "v_pk_mul_f16 v38, v82, v166 op_sel_hi:[1,0]\n\t"
    "v_pk_mul_f16 v39, v83, v167 op_sel_hi:[1,0]\n\t"
    "v_pk_fma_f16 v38, v82, v166, v38 op_sel:[1,1,0] op_sel_hi:[0,1,1] neg_hi:[1,0,0]\n\t"
    "v_pk_fma_f16 v39, v83, v167, v39 op_sel:[1,1,0] op_sel_hi:[0,1,1] neg_hi:[1,0,0]\n\t"
    "v_cvt_pk_f16_f32 v80, v8, v24\n\t"
    "v_cvt_pk_f16_f32 v81, v9, v25\n\t"
    "v_mfma_f32_32x32x16_f16 v[48:63], a[228:231], v[36:39], v[48:63]\n\t"
    "v_pk_mul_f16 v40, v80, v168 op_sel_hi:[1,0]\n\t"
    "v_pk_mul_f16 v41, v81, v169 op_sel_hi:[1,0]\n\t"
    "v_pk_fma_f16 v40, v80, v168, v40 op_sel:[1,1,0] op_sel_hi:[0,1,1] neg_hi:[1,0,0]\n\t"
    "v_pk_fma_f16 v41, v81, v169, v41 op_sel:[1,1,0] op_sel_hi:[0,1,1] neg_hi:[1,0,0]\n\t"
    "v_cvt_pk_f16_f32 v82, v10, v26\n\t"
    "v_cvt_pk_f16_f32 v83, v11, v27\n\t"
    "v_mfma_f32_32x32x16_f16 v[64:79], a[244:247], v[36:39], v[64:79]\n\t"
    "v_pk_mul_f16 v42, v82, v170 op_sel_hi:[1,0]\n\t"
    "v_pk_mul_f16 v43, v83, v171 op_sel_hi:[1,0]\n\t"
    "v_pk_fma_f16 v42, v82, v170, v42 op_sel:[1,1,0] op_sel_hi:[0,1,1] neg_hi:[1,0,0]\n\t"
    "v_pk_fma_f16 v43, v83, v171, v43 op_sel:[1,1,0] op_sel_hi:[0,1,1] neg_hi:[1,0,0]\n\t"
    "v_cvt_pk_f16_f32 v80, v12, v28\n\t"
    "v_cvt_pk_f16_f32 v81, v13, v29\n\t"
    "v_mfma_f32_32x32x16_f16 v[48:63], a[232:235], v[40:43], v[48:63]\n\t"
    "v_pk_mul_f16 v44, v80, v172 op_sel_hi:[1,0]\n\t"
    "v_pk_mul_f16 v45, v81, v173 op_sel_hi:[1,0]\n\t"
    "v_pk_fma_f16 v44, v80, v172, v44 op_sel:[1,1,0] op_sel_hi:[0,1,1] neg_hi:[1,0,0]\n\t"
    "v_pk_fma_f16 v45, v81, v173, v45 op_sel:[1,1,0] op_sel_hi:[0,1,1] neg_hi:[1,0,0]\n\t"
    "v_cvt_pk_f16_f32 v82, v14, v30\n\t"
    "v_cvt_pk_f16_f32 v83, v15, v31\n\t"
    "v_mfma_f32_32x32x16_f16 v[64:79], a[248:251], v[40:43], v[64:79]\n\t"
    "v_pk_mul_f16 v46, v82, v174 op_sel_hi:[1,0]\n\t"
    "v_pk_mul_f16 v47, v83, v175 op_sel_hi:[1,0]\n\t"
    "v_pk_fma_f16 v46, v82, v174, v46 op_sel:[1,1,0] op_sel_hi:[0,1,1] neg_hi:[1,0,0]\n\t"
    "v_pk_fma_f16 v47, v83, v175, v47 op_sel:[1,1,0] op_sel_hi:[0,1,1] neg_hi:[1,0,0]\n\t"
    "s_nop 1\n\t"
    "v_mfma_f32_32x32x16_f16 v[48:63], a[236:239], v[44:47], v[48:63]\n\t"
    "v_mfma_f32_32x32x16_f16 v[64:79], a[252:255], v[44:47], v[64:79]\n\t"
    "s_nop 7\n\t"
    "s_nop 3\n\t"
    "s_branch Ljoin_%=\n\t"
    "Lx7_%=:\n\t"
    "v_and_b32 v3, 31, v245\n\t"
    "v_lshrrev_b32 v4, 5, v245\n\t"
    "v_lshl_add_u32 v3, v4, 7, v3\n\t"
    "s_lshl_b32 s38, s22, 12\n\t"
    "s_add_u32 s38, s38, 133632\n\t"
    "v_sub_u32 v0, 1024, v3\n\t"
    "v_and_b32 v0, 1023, v0\n\t"
    "v_lshrrev_b32 v1, 8, v0\n\t"
    "v_lshl_add_u32 v1, v1, 10, s38\n\t"
    "v_and_b32 v2, 31, v0\n\t"
    "v_lshl_add_u32 v1, v2, 4, v1\n\t"
    "v_bfe_u32 v2, v0, 7, 1\n\t"
    "v_lshl_add_u32 v1, v2, 9, v1\n\t"
    "v_bfe_u32 v2, v0, 5, 2\n\t"
    "v_lshl_add_u32 v1, v2, 2, v1\n\t"
    "ds_read_b32 v48, v1\n\t"
    "v_sub_u32 v0, 992, v3\n\t"
    "v_and_b32 v0, 1023, v0\n\t"
    "v_lshrrev_b32 v1, 8, v0\n\t"
    "v_lshl_add_u32 v1, v1, 10, s38\n\t"
    "v_and_b32 v2, 31, v0\n\t"
    "v_lshl_add_u32 v1, v2, 4, v1\n\t"
    "v_bfe_u32 v2, v0, 7, 1\n\t"
    "v_lshl_add_u32 v1, v2, 9, v1\n\t"
    "v_bfe_u32 v2, v0, 5, 2\n\t"
    "v_lshl_add_u32 v1, v2, 2, v1\n\t"
    "ds_read_b32 v49, v1\n\t"
    "v_sub_u32 v0, 960, v3\n\t"
    "v_and_b32 v0, 1023, v0\n\t"
    "v_lshrrev_b32 v1, 8, v0\n\t"
    "v_lshl_add_u32 v1, v1, 10, s38\n\t"
    "v_and_b32 v2, 31, v0\n\t"
    "v_lshl_add_u32 v1, v2, 4, v1\n\t"
    "v_bfe_u32 v2, v0, 7, 1\n\t"
    "v_lshl_add_u32 v1, v2, 9, v1\n\t"
    "v_bfe_u32 v2, v0, 5, 2\n\t"
    "v_lshl_add_u32 v1, v2, 2, v1\n\t"
    "ds_read_b32 v50, v1\n\t"
    "v_sub_u32 v0, 928, v3\n\t"
    "v_and_b32 v0, 1023, v0\n\t"
    "v_lshrrev_b32 v1, 8, v0\n\t"
    "v_lshl_add_u32 v1, v1, 10, s38\n\t"
    "v_and_b32 v2, 31, v0\n\t"
    "v_lshl_add_u32 v1, v2, 4, v1\n\t"
    "v_bfe_u32 v2, v0, 7, 1\n\t"
    "v_lshl_add_u32 v1, v2, 9, v1\n\t"
    "v_bfe_u32 v2, v0, 5, 2\n\t"
    "v_lshl_add_u32 v1, v2, 2, v1\n\t"
    "ds_read_b32 v51, v1\n\t"
    "v_sub_u32 v0, 768, v3\n\t"
    "v_and_b32 v0, 1023, v0\n\t"
    "v_lshrrev_b32 v1, 8, v0\n\t"
    "v_lshl_add_u32 v1, v1, 10, s38\n\t"
    "v_and_b32 v2, 31, v0\n\t"
    "v_lshl_add_u32 v1, v2, 4, v1\n\t"
    "v_bfe_u32 v2, v0, 7, 1\n\t"
    "v_lshl_add_u32 v1, v2, 9, v1\n\t"
    "v_bfe_u32 v2, v0, 5, 2\n\t"
    "v_lshl_add_u32 v1, v2, 2, v1\n\t"
    "ds_read_b32 v52, v1\n\t"
    "v_sub_u32 v0, 736, v3\n\t"
    "v_and_b32 v0, 1023, v0\n\t"
    "v_lshrrev_b32 v1, 8, v0\n\t"
    "v_lshl_add_u32 v1, v1, 10, s38\n\t"
    "v_and_b32 v2, 31, v0\n\t"
    "v_lshl_add_u32 v1, v2, 4, v1\n\t"
    "v_bfe_u32 v2, v0, 7, 1\n\t"
    "v_lshl_add_u32 v1, v2, 9, v1\n\t"
    "v_bfe_u32 v2, v0, 5, 2\n\t"
    "v_lshl_add_u32 v1, v2, 2, v1\n\t"
    "ds_read_b32 v53, v1\n\t"
    "v_sub_u32 v0, 704, v3\n\t"
    "v_and_b32 v0, 1023, v0\n\t"
    "v_lshrrev_b32 v1, 8, v0\n\t"
    "v_lshl_add_u32 v1, v1, 10, s38\n\t"
    "v_and_b32 v2, 31, v0\n\t"
    "v_lshl_add_u32 v1, v2, 4, v1\n\t"
    "v_bfe_u32 v2, v0, 7, 1\n\t"
    "v_lshl_add_u32 v1, v2, 9, v1\n\t"
    "v_bfe_u32 v2, v0, 5, 2\n\t"
    "v_lshl_add_u32 v1, v2, 2, v1\n\t"
    "ds_read_b32 v54, v1\n\t"
    "v_sub_u32 v0, 672, v3\n\t"
    "v_and_b32 v0, 1023, v0\n\t"
    "v_lshrrev_b32 v1, 8, v0\n\t"
    "v_lshl_add_u32 v1, v1, 10, s38\n\t"
    "v_and_b32 v2, 31, v0\n\t"
    "v_lshl_add_u32 v1, v2, 4, v1\n\t"
    "v_bfe_u32 v2, v0, 7, 1\n\t"
    "v_lshl_add_u32 v1, v2, 9, v1\n\t"
    "v_bfe_u32 v2, v0, 5, 2\n\t"
    "v_lshl_add_u32 v1, v2, 2, v1\n\t"
    "ds_read_b32 v55, v1\n\t"
    "v_sub_u32 v0, 512, v3\n\t"
    "v_and_b32 v0, 1023, v0\n\t"
    "v_lshrrev_b32 v1, 8, v0\n\t"
    "v_lshl_add_u32 v1, v1, 10, s38\n\t"
    "v_and_b32 v2, 31, v0\n\t"
    "v_lshl_add_u32 v1, v2, 4, v1\n\t"
    "v_bfe_u32 v2, v0, 7, 1\n\t"
    "v_lshl_add_u32 v1, v2, 9, v1\n\t"
    "v_bfe_u32 v2, v0, 5, 2\n\t"
    "v_lshl_add_u32 v1, v2, 2, v1\n\t"
    "ds_read_b32 v56, v1\n\t"
    "v_sub_u32 v0, 480, v3\n\t"
    "v_and_b32 v0, 1023, v0\n\t"
    "v_lshrrev_b32 v1, 8, v0\n\t"
    "v_lshl_add_u32 v1, v1, 10, s38\n\t"
    "v_and_b32 v2, 31, v0\n\t"
    "v_lshl_add_u32 v1, v2, 4, v1\n\t"
    "v_bfe_u32 v2, v0, 7, 1\n\t"
    "v_lshl_add_u32 v1, v2, 9, v1\n\t"
    "v_bfe_u32 v2, v0, 5, 2\n\t"
    "v_lshl_add_u32 v1, v2, 2, v1\n\t"
    "ds_read_b32 v57, v1\n\t"
    "v_sub_u32 v0, 448, v3\n\t"
    "v_and_b32 v0, 1023, v0\n\t"
    "v_lshrrev_b32 v1, 8, v0\n\t"
    "v_lshl_add_u32 v1, v1, 10, s38\n\t"
    "v_and_b32 v2, 31, v0\n\t"
    "v_lshl_add_u32 v1, v2, 4, v1\n\t"
    "v_bfe_u32 v2, v0, 7, 1\n\t"
    "v_lshl_add_u32 v1, v2, 9, v1\n\t"
    "v_bfe_u32 v2, v0, 5, 2\n\t"
    "v_lshl_add_u32 v1, v2, 2, v1\n\t"
    "ds_read_b32 v58, v1\n\t"
    "v_sub_u32 v0, 416, v3\n\t"
    "v_and_b32 v0, 1023, v0\n\t"
    "v_lshrrev_b32 v1, 8, v0\n\t"
    "v_lshl_add_u32 v1, v1, 10, s38\n\t"
    "v_and_b32 v2, 31, v0\n\t"
    "v_lshl_add_u32 v1, v2, 4, v1\n\t"
    "v_bfe_u32 v2, v0, 7, 1\n\t"
    "v_lshl_add_u32 v1, v2, 9, v1\n\t"
    "v_bfe_u32 v2, v0, 5, 2\n\t"
    "v_lshl_add_u32 v1, v2, 2, v1\n\t"
    "ds_read_b32 v59, v1\n\t"
    "v_sub_u32 v0, 256, v3\n\t"
    "v_and_b32 v0, 1023, v0\n\t"
    "v_lshrrev_b32 v1, 8, v0\n\t"
    "v_lshl_add_u32 v1, v1, 10, s38\n\t"
    "v_and_b32 v2, 31, v0\n\t"
    "v_lshl_add_u32 v1, v2, 4, v1\n\t"
    "v_bfe_u32 v2, v0, 7, 1\n\t"
    "v_lshl_add_u32 v1, v2, 9, v1\n\t"
    "v_bfe_u32 v2, v0, 5, 2\n\t"
    "v_lshl_add_u32 v1, v2, 2, v1\n\t"
    "ds_read_b32 v60, v1\n\t"
    "v_sub_u32 v0, 224, v3\n\t"
    "v_and_b32 v0, 1023, v0\n\t"
    "v_lshrrev_b32 v1, 8, v0\n\t"
    "v_lshl_add_u32 v1, v1, 10, s38\n\t"
    "v_and_b32 v2, 31, v0\n\t"
    "v_lshl_add_u32 v1, v2, 4, v1\n\t"
    "v_bfe_u32 v2, v0, 7, 1\n\t"
    "v_lshl_add_u32 v1, v2, 9, v1\n\t"
    "v_bfe_u32 v2, v0, 5, 2\n\t"
    "v_lshl_add_u32 v1, v2, 2, v1\n\t"
    "ds_read_b32 v61, v1\n\t"
    "v_sub_u32 v0, 192, v3\n\t"
    "v_and_b32 v0, 1023, v0\n\t"
    "v_lshrrev_b32 v1, 8, v0\n\t"
    "v_lshl_add_u32 v1, v1, 10, s38\n\t"
    "v_and_b32 v2, 31, v0\n\t"
    "v_lshl_add_u32 v1, v2, 4, v1\n\t"
    "v_bfe_u32 v2, v0, 7, 1\n\t"
    "v_lshl_add_u32 v1, v2, 9, v1\n\t"
    "v_bfe_u32 v2, v0, 5, 2\n\t"
    "v_lshl_add_u32 v1, v2, 2, v1\n\t"
    "ds_read_b32 v62, v1\n\t"
    "v_sub_u32 v0, 160, v3\n\t"
    "v_and_b32 v0, 1023, v0\n\t"
    "v_lshrrev_b32 v1, 8, v0\n\t"
    "v_lshl_add_u32 v1, v1, 10, s38\n\t"
    "v_and_b32 v2, 31, v0\n\t"
    "v_lshl_add_u32 v1, v2, 4, v1\n\t"
    "v_bfe_u32 v2, v0, 7, 1\n\t"
    "v_lshl_add_u32 v1, v2, 9, v1\n\t"
    "v_bfe_u32 v2, v0, 5, 2\n\t"
    "v_lshl_add_u32 v1, v2, 2, v1\n\t"
    "ds_read_b32 v63, v1\n\t"
    "s_waitcnt lgkmcnt(0)\n\t"
    "v_alignbit_b32 v48, v48, v48, 16\n\t"
    "v_alignbit_b32 v49, v49, v49, 16\n\t"
    "v_alignbit_b32 v50, v50, v50, 16\n\t"
    "v_alignbit_b32 v51, v51, v51, 16\n\t"
    "v_alignbit_b32 v52, v52, v52, 16\n\t"
    "v_alignbit_b32 v53, v53, v53, 16\n\t"
    "v_alignbit_b32 v54, v54, v54, 16\n\t"
    "v_alignbit_b32 v55, v55, v55, 16\n\t"
    "v_alignbit_b32 v56, v56, v56, 16\n\t"
    "v_alignbit_b32 v57, v57, v57, 16\n\t"
    "v_alignbit_b32 v58, v58, v58, 16\n\t"
    "v_alignbit_b32 v59, v59, v59, 16\n\t"
    "v_alignbit_b32 v60, v60, v60, 16\n\t"
    "v_alignbit_b32 v61, v61, v61, 16\n\t"
    "v_alignbit_b32 v62, v62, v62, 16\n\t"
    "v_alignbit_b32 v63, v63, v63, 16\n\t"
    "s_nop 1\n\t"
    "s_branch Ly8_%=\n\t"
    "Ld9_%=:\n\t"
    "s_waitcnt vmcnt(0)\n\t"
    "v_mov_b64 v[250:251], 0\n\t"
    "s_mov_b64 exec, 0x7f\n\t"
    "global_atomic_or_x2 v[250:251], v249, v[250:251], s[20:21] sc0 sc1\n\t"
    "s_mov_b64 exec, -1\n\t"
    "s_waitcnt vmcnt(0)\n\t"
    "v_cmp_ne_u64 vcc, s[74:75], v[250:251]\n\t"
    "s_nop 1\n\t"
    "s_and_b64 vcc, vcc, 0x7f\n\t"
    "s_cbranch_scc1 Ls12_%=\n\t"
    "Lb13_%=:\n\t"
    "Lg11_%=:\n\t"
    "v_readlane_b32 s24, v246, 0\n\t"
    "s_nop 1\n\t"
    "s_mov_b32 s34, s24\n\t"
    "buffer_load_dwordx4 a[0:3], v240, s[64:67], s34 offen offset:0 sc1\n\t"
    "buffer_load_dwordx4 a[4:7], v240, s[64:67], s34 offen offset:1024 sc1\n\t"
    "buffer_load_dwordx4 a[8:11], v240, s[64:67], s34 offen offset:2048 sc1\n\t"
    "buffer_load_dwordx4 a[12:15], v240, s[64:67], s34 offen offset:3072 sc1\n\t"
    "v_readlane_b32 s24, v246, 1\n\t"
    "s_nop 1\n\t"
    "s_mov_b32 s34, s24\n\t"
    "buffer_load_dwordx4 a[16:19], v240, s[64:67], s34 offen offset:0 sc1\n\t"
    "buffer_load_dwordx4 a[20:23], v240, s[64:67], s34 offen offset:1024 sc1\n\t"
    "buffer_load_dwordx4 a[24:27], v240, s[64:67], s34 offen offset:2048 sc1\n\t"
    "buffer_load_dwordx4 a[28:31], v240, s[64:67], s34 offen offset:3072 sc1\n\t"
    "v_readlane_b32 s24, v246, 2\n\t"
    "s_nop 1\n\t"
    "s_mov_b32 s34, s24\n\t"
    "buffer_load_dwordx4 v[176:179], v240, s[64:67], s34 offen offset:0 sc1\n\t"
    "buffer_load_dwordx4 v[180:183], v240, s[64:67], s34 offen offset:1024 sc1\n\t"
    "buffer_load_dwordx4 v[184:187], v240, s[64:67], s34 offen offset:2048 sc1\n\t"
    "buffer_load_dwordx4 v[188:191], v240, s[64:67], s34 offen offset:3072 sc1\n\t"
    "v_readlane_b32 s24, v246, 3\n\t"
    "s_nop 1\n\t"
    "s_mov_b32 s34, s24\n\t"
    "buffer_load_dwordx4 a[48:51], v240, s[64:67], s34 offen offset:0 sc1\n\t"
    "buffer_load_dwordx4 a[52:55], v240, s[64:67], s34 offen offset:1024 sc1\n\t"
    "buffer_load_dwordx4 a[56:59], v240, s[64:67], s34 offen offset:2048 sc1\n\t"
    "buffer_load_dwordx4 a[60:63], v240, s[64:67], s34 offen offset:3072 sc1\n\t"
    "s_waitcnt vmcnt(12)\n\t"
    "s_branch Le10_%=\n\t"
    "Lt43_%=:\n\t"
    "s_lshl_b32 s38, s22, 14\n\t"
    "s_add_u32 s38, s38, 0x1f1000\n\t"
    "s_add_u32 s44, s20, s38\n\t"
    "s_addc_u32 s45, s21, 0\n\t"
    "v_lshrrev_b32 v1, 2, v245\n\t"
    "v_and_b32 v2, 3, v245\n\t"
    "v_lshlrev_b32 v2, 1, v2\n\t"
    "v_lshl_or_b32 v1, v1, 6, v2\n\t"
    "v_add_u32 v2, 8192, v1\n\t"
    "global_load_ushort v16, v1, s[44:45] offset:-4096 sc1\n\t"
    "global_load_ushort v17, v1, s[44:45] offset:-3072 sc1\n\t"
    "global_load_ushort v18, v1, s[44:45] offset:-2048 sc1\n\t"
    "global_load_ushort v19, v1, s[44:45] offset:-1024 sc1\n\t"
    "global_load_ushort v20, v1, s[44:45] offset:0 sc1\n\t"
    "global_load_ushort v21, v1, s[44:45] offset:1024 sc1\n\t"
    "global_load_ushort v22, v1, s[44:45] offset:2048 sc1\n\t"
    "global_load_ushort v23, v1, s[44:45] offset:3072 sc1\n\t"
    "global_load_ushort v24, v2, s[44:45] offset:-4096 sc1\n\t"
    "global_load_ushort v25, v2, s[44:45] offset:-3072 sc1\n\t"
    "global_load_ushort v26, v2, s[44:45] offset:-2048 sc1\n\t"
    "global_load_ushort v27, v2, s[44:45] offset:-1024 sc1\n\t"
    "global_load_ushort v28, v2, s[44:45] offset:0 sc1\n\t"
    "global_load_ushort v29, v2, s[44:45] offset:1024 sc1\n\t"
    "global_load_ushort v30, v2, s[44:45] offset:2048 sc1\n\t"
    "global_load_ushort v31, v2, s[44:45] offset:3072 sc1\n\t"
    "s_waitcnt vmcnt(0)\n\t"
    "s_branch Lr44_%=\n\t"
    "Lu45_%=:\n\t"
    "s_lshl_b32 s38, s22, 12\n\t"
    "s_add_u32 s38, s38, 9216\n\t"
    "v_add_u32 v1, s38, v240\n\t"
    "ds_write_b128 v1, v[128:131] offset:0\n\t"
    "ds_write_b128 v1, v[132:135] offset:1024\n\t"
    "ds_write_b128 v1, v[136:139] offset:2048\n\t"
    "ds_write_b128 v1, v[140:143] offset:3072\n\t"
    "ds_write_b128 v1, v[144:147] offset:4096\n\t"
    "ds_write_b128 v1, v[148:151] offset:5120\n\t"
    "ds_write_b128 v1, v[152:155] offset:6144\n\t"
    "ds_write_b128 v1, v[156:159] offset:7168\n\t"
    "v_and_b32 v3, 31, v245\n\t"
    "v_lshrrev_b32 v4, 5, v245\n\t"
    "v_lshl_add_u32 v3, v4, 7, v3\n\t"
    "s_waitcnt lgkmcnt(0)\n\t"
    "v_sub_u32 v0, 1024, v3\n\t"
    "v_and_b32 v0, 1023, v0\n\t"
    "v_lshrrev_b32 v1, 8, v0\n\t"
    "v_lshl_add_u32 v1, v1, 10, s38\n\t"
    "v_and_b32 v2, 31, v0\n\t"
    "v_lshl_add_u32 v1, v2, 4, v1\n\t"
    "v_bfe_u32 v2, v0, 7, 1\n\t"
    "v_lshl_add_u32 v1, v2, 9, v1\n\t"
    "v_bfe_u32 v2, v0, 5, 2\n\t"
    "v_lshl_add_u32 v1, v2, 2, v1\n\t"
    "ds_read_b32 v16, v1\n\t"
    "ds_read_b32 v32, v1 offset:4096\n\t"
    "v_sub_u32 v0, 992, v3\n\t"
    "v_and_b32 v0, 1023, v0\n\t"
    "v_lshrrev_b32 v1, 8, v0\n\t"
    "v_lshl_add_u32 v1, v1, 10, s38\n\t"
    "v_and_b32 v2, 31, v0\n\t"
    "v_lshl_add_u32 v1, v2, 4, v1\n\t"
    "v_bfe_u32 v2, v0, 7, 1\n\t"
    "v_lshl_add_u32 v1, v2, 9, v1\n\t"
    "v_bfe_u32 v2, v0, 5, 2\n\t"
    "v_lshl_add_u32 v1, v2, 2, v1\n\t"
    "ds_read_b32 v17, v1\n\t"
    "ds_read_b32 v33, v1 offset:4096\n\t"
    "v_sub_u32 v0, 960, v3\n\t"
    "v_and_b32 v0, 1023, v0\n\t"
    "v_lshrrev_b32 v1, 8, v0\n\t"
    "v_lshl_add_u32 v1, v1, 10, s38\n\t"
    "v_and_b32 v2, 31, v0\n\t"
    "v_lshl_add_u32 v1, v2, 4, v1\n\t"
    "v_bfe_u32 v2, v0, 7, 1\n\t"
    "v_lshl_add_u32 v1, v2, 9, v1\n\t"
    "v_bfe_u32 v2, v0, 5, 2\n\t"
    "v_lshl_add_u32 v1, v2, 2, v1\n\t"
    "ds_read_b32 v18, v1\n\t"
    "ds_read_b32 v34, v1 offset:4096\n\t"
    "v_sub_u32 v0, 928, v3\n\t"
    "v_and_b32 v0, 1023, v0\n\t"
    "v_lshrrev_b32 v1, 8, v0\n\t"
    "v_lshl_add_u32 v1, v1, 10, s38\n\t"
    "v_and_b32 v2, 31, v0\n\t"
    "v_lshl_add_u32 v1, v2, 4, v1\n\t"
    "v_bfe_u32 v2, v0, 7, 1\n\t"
    "v_lshl_add_u32 v1, v2, 9, v1\n\t"
    "v_bfe_u32 v2, v0, 5, 2\n\t"
    "v_lshl_add_u32 v1, v2, 2, v1\n\t"
    "ds_read_b32 v19, v1\n\t"
    "ds_read_b32 v35, v1 offset:4096\n\t"
    "v_sub_u32 v0, 768, v3\n\t"
    "v_and_b32 v0, 1023, v0\n\t"
    "v_lshrrev_b32 v1, 8, v0\n\t"
    "v_lshl_add_u32 v1, v1, 10, s38\n\t"
    "v_and_b32 v2, 31, v0\n\t"
    "v_lshl_add_u32 v1, v2, 4, v1\n\t"
    "v_bfe_u32 v2, v0, 7, 1\n\t"
    "v_lshl_add_u32 v1, v2, 9, v1\n\t"
    "v_bfe_u32 v2, v0, 5, 2\n\t"
    "v_lshl_add_u32 v1, v2, 2, v1\n\t"
    "ds_read_b32 v20, v1\n\t"
    "ds_read_b32 v36, v1 offset:4096\n\t"
    "v_sub_u32 v0, 736, v3\n\t"
    "v_and_b32 v0, 1023, v0\n\t"
    "v_lshrrev_b32 v1, 8, v0\n\t"
    "v_lshl_add_u32 v1, v1, 10, s38\n\t"
    "v_and_b32 v2, 31, v0\n\t"
    "v_lshl_add_u32 v1, v2, 4, v1\n\t"
    "v_bfe_u32 v2, v0, 7, 1\n\t"
    "v_lshl_add_u32 v1, v2, 9, v1\n\t"
    "v_bfe_u32 v2, v0, 5, 2\n\t"
    "v_lshl_add_u32 v1, v2, 2, v1\n\t"
    "ds_read_b32 v21, v1\n\t"
    "ds_read_b32 v37, v1 offset:4096\n\t"
    "v_sub_u32 v0, 704, v3\n\t"
    "v_and_b32 v0, 1023, v0\n\t"
    "v_lshrrev_b32 v1, 8, v0\n\t"
    "v_lshl_add_u32 v1, v1, 10, s38\n\t"
    "v_and_b32 v2, 31, v0\n\t"
    "v_lshl_add_u32 v1, v2, 4, v1\n\t"
    "v_bfe_u32 v2, v0, 7, 1\n\t"
    "v_lshl_add_u32 v1, v2, 9, v1\n\t"
    "v_bfe_u32 v2, v0, 5, 2\n\t"
    "v_lshl_add_u32 v1, v2, 2, v1\n\t"
    "ds_read_b32 v22, v1\n\t"
    "ds_read_b32 v38, v1 offset:4096\n\t"
    "v_sub_u32 v0, 672, v3\n\t"
    "v_and_b32 v0, 1023, v0\n\t"
    "v_lshrrev_b32 v1, 8, v0\n\t"
    "v_lshl_add_u32 v1, v1, 10, s38\n\t"
    "v_and_b32 v2, 31, v0\n\t"
    "v_lshl_add_u32 v1, v2, 4, v1\n\t"
    "v_bfe_u32 v2, v0, 7, 1\n\t"
    "v_lshl_add_u32 v1, v2, 9, v1\n\t"
    "v_bfe_u32 v2, v0, 5, 2\n\t"
    "v_lshl_add_u32 v1, v2, 2, v1\n\t"
    "ds_read_b32 v23, v1\n\t"
    "ds_read_b32 v39, v1 offset:4096\n\t"
    "v_sub_u32 v0, 512, v3\n\t"
    "v_and_b32 v0, 1023, v0\n\t"
    "v_lshrrev_b32 v1, 8, v0\n\t"
    "v_lshl_add_u32 v1, v1, 10, s38\n\t"
    "v_and_b32 v2, 31, v0\n\t"
    "v_lshl_add_u32 v1, v2, 4, v1\n\t"
    "v_bfe_u32 v2, v0, 7, 1\n\t"
    "v_lshl_add_u32 v1, v2, 9, v1\n\t"
    "v_bfe_u32 v2, v0, 5, 2\n\t"
    "v_lshl_add_u32 v1, v2, 2, v1\n\t"
    "ds_read_b32 v24, v1\n\t"
    "ds_read_b32 v40, v1 offset:4096\n\t"
    "v_sub_u32 v0, 480, v3\n\t"
    "v_and_b32 v0, 1023, v0\n\t"
    "v_lshrrev_b32 v1, 8, v0\n\t"
    "v_lshl_add_u32 v1, v1, 10, s38\n\t"
    "v_and_b32 v2, 31, v0\n\t"
    "v_lshl_add_u32 v1, v2, 4, v1\n\t"
    "v_bfe_u32 v2, v0, 7, 1\n\t"
    "v_lshl_add_u32 v1, v2, 9, v1\n\t"
    "v_bfe_u32 v2, v0, 5, 2\n\t"
    "v_lshl_add_u32 v1, v2, 2, v1\n\t"
    "ds_read_b32 v25, v1\n\t"
    "ds_read_b32 v41, v1 offset:4096\n\t"
    "v_sub_u32 v0, 448, v3\n\t"
    "v_and_b32 v0, 1023, v0\n\t"
    "v_lshrrev_b32 v1, 8, v0\n\t"
    "v_lshl_add_u32 v1, v1, 10, s38\n\t"
    "v_and_b32 v2, 31, v0\n\t"
    "v_lshl_add_u32 v1, v2, 4, v1\n\t"
    "v_bfe_u32 v2, v0, 7, 1\n\t"
    "v_lshl_add_u32 v1, v2, 9, v1\n\t"
    "v_bfe_u32 v2, v0, 5, 2\n\t"
    "v_lshl_add_u32 v1, v2, 2, v1\n\t"
    "ds_read_b32 v26, v1\n\t"
    "ds_read_b32 v42, v1 offset:4096\n\t"
    "v_sub_u32 v0, 416, v3\n\t"
    "v_and_b32 v0, 1023, v0\n\t"
    "v_lshrrev_b32 v1, 8, v0\n\t"
    "v_lshl_add_u32 v1, v1, 10, s38\n\t"
    "v_and_b32 v2, 31, v0\n\t"
    "v_lshl_add_u32 v1, v2, 4, v1\n\t"
    "v_bfe_u32 v2, v0, 7, 1\n\t"
    "v_lshl_add_u32 v1, v2, 9, v1\n\t"
    "v_bfe_u32 v2, v0, 5, 2\n\t"
    "v_lshl_add_u32 v1, v2, 2, v1\n\t"
    "ds_read_b32 v27, v1\n\t"
    "ds_read_b32 v43, v1 offset:4096\n\t"
    "v_sub_u32 v0, 256, v3\n\t"
    "v_and_b32 v0, 1023, v0\n\t"
    "v_lshrrev_b32 v1, 8, v0\n\t"
    "v_lshl_add_u32 v1, v1, 10, s38\n\t"
    "v_and_b32 v2, 31, v0\n\t"
    "v_lshl_add_u32 v1, v2, 4, v1\n\t"
    "v_bfe_u32 v2, v0, 7, 1\n\t"
    "v_lshl_add_u32 v1, v2, 9, v1\n\t"
    "v_bfe_u32 v2, v0, 5, 2\n\t"
    "v_lshl_add_u32 v1, v2, 2, v1\n\t"
    "ds_read_b32 v28, v1\n\t"
    "ds_read_b32 v44, v1 offset:4096\n\t"
    "v_sub_u32 v0, 224, v3\n\t"
    "v_and_b32 v0, 1023, v0\n\t"
    "v_lshrrev_b32 v1, 8, v0\n\t"
    "v_lshl_add_u32 v1, v1, 10, s38\n\t"
    "v_and_b32 v2, 31, v0\n\t"
    "v_lshl_add_u32 v1, v2, 4, v1\n\t"
    "v_bfe_u32 v2, v0, 7, 1\n\t"
    "v_lshl_add_u32 v1, v2, 9, v1\n\t"
    "v_bfe_u32 v2, v0, 5, 2\n\t"
    "v_lshl_add_u32 v1, v2, 2, v1\n\t"
    "ds_read_b32 v29, v1\n\t"
    "ds_read_b32 v45, v1 offset:4096\n\t"
    "v_sub_u32 v0, 192, v3\n\t"
    "v_and_b32 v0, 1023, v0\n\t"
    "v_lshrrev_b32 v1, 8, v0\n\t"
    "v_lshl_add_u32 v1, v1, 10, s38\n\t"
    "v_and_b32 v2, 31, v0\n\t"
    "v_lshl_add_u32 v1, v2, 4, v1\n\t"
    "v_bfe_u32 v2, v0, 7, 1\n\t"
    "v_lshl_add_u32 v1, v2, 9, v1\n\t"
    "v_bfe_u32 v2, v0, 5, 2\n\t"
    "v_lshl_add_u32 v1, v2, 2, v1\n\t"
    "ds_read_b32 v30, v1\n\t"
    "ds_read_b32 v46, v1 offset:4096\n\t"
    "v_sub_u32 v0, 160, v3\n\t"
    "v_and_b32 v0, 1023, v0\n\t"
    "v_lshrrev_b32 v1, 8, v0\n\t"
    "v_lshl_add_u32 v1, v1, 10, s38\n\t"
    "v_and_b32 v2, 31, v0\n\t"
    "v_lshl_add_u32 v1, v2, 4, v1\n\t"
    "v_bfe_u32 v2, v0, 7, 1\n\t"
    "v_lshl_add_u32 v1, v2, 9, v1\n\t"
    "v_bfe_u32 v2, v0, 5, 2\n\t"
    "v_lshl_add_u32 v1, v2, 2, v1\n\t"
    "ds_read_b32 v31, v1\n\t"
    "ds_read_b32 v47, v1 offset:4096\n\t"
    "s_waitcnt lgkmcnt(0)\n\t"
    "s_mov_b64 exec, 1\n\t"
    "v_xor_b32 v32, 0x80008000, v32\n\t"
    "s_mov_b64 exec, -1\n\t"
    "v_pk_add_f16 v128, v128, v16 neg_hi:[0,1]\n\t"
    "v_pk_add_f16 v144, v144, v32 neg_lo:[0,1]\n\t"
    "v_pk_add_f16 v129, v129, v17 neg_hi:[0,1]\n\t"
    "v_pk_add_f16 v145, v145, v33 neg_lo:[0,1]\n\t"
    "v_pk_add_f16 v130, v130, v18 neg_hi:[0,1]\n\t"
    "v_pk_add_f16 v146, v146, v34 neg_lo:[0,1]\n\t"
    "v_pk_add_f16 v131, v131, v19 neg_hi:[0,1]\n\t"
    "v_pk_add_f16 v147, v147, v35 neg_lo:[0,1]\n\t"
    "v_pk_add_f16 v132, v132, v20 neg_hi:[0,1]\n\t"
    "v_pk_add_f16 v148, v148, v36 neg_lo:[0,1]\n\t"
    "v_pk_add_f16 v133, v133, v21 neg_hi:[0,1]\n\t"
    "v_pk_add_f16 v149, v149, v37 neg_lo:[0,1]\n\t"
    "v_pk_add_f16 v134, v134, v22 neg_hi:[0,1]\n\t"
    "v_pk_add_f16 v150, v150, v38 neg_lo:[0,1]\n\t"
    "v_pk_add_f16 v135, v135, v23 neg_hi:[0,1]\n\t"
    "v_pk_add_f16 v151, v151, v39 neg_lo:[0,1]\n\t"
    "v_pk_add_f16 v136, v136, v24 neg_hi:[0,1]\n\t"
    "v_pk_add_f16 v152, v152, v40 neg_lo:[0,1]\n\t"
    "v_pk_add_f16 v137, v137, v25 neg_hi:[0,1]\n\t"
    "v_pk_add_f16 v153, v153, v41 neg_lo:[0,1]\n\t"
    "v_pk_add_f16 v138, v138, v26 neg_hi:[0,1]\n\t"
    "v_pk_add_f16 v154, v154, v42 neg_lo:[0,1]\n\t"
    "v_pk_add_f16 v139, v139, v27 neg_hi:[0,1]\n\t"
    "v_pk_add_f16 v155, v155, v43 neg_lo:[0,1]\n\t"
    "v_pk_add_f16 v140, v140, v28 neg_hi:[0,1]\n\t"
    "v_pk_add_f16 v156, v156, v44 neg_lo:[0,1]\n\t"
    "v_pk_add_f16 v141, v141, v29 neg_hi:[0,1]\n\t"
    "v_pk_add_f16 v157, v157, v45 neg_lo:[0,1]\n\t"
    "v_pk_add_f16 v142, v142, v30 neg_hi:[0,1]\n\t"
    "v_pk_add_f16 v158, v158, v46 neg_lo:[0,1]\n\t"
    "v_pk_add_f16 v143, v143, v31 neg_hi:[0,1]\n\t"
    "v_pk_add_f16 v159, v159, v47 neg_lo:[0,1]\n\t"
    "v_mov_b32 v0, 0x38003800\n\t"
    "v_pk_mul_f16 v128, v128, v0\n\t"
    "v_pk_mul_f16 v129, v129, v0\n\t"
    "v_pk_mul_f16 v130, v130, v0\n\t"
    "v_pk_mul_f16 v131, v131, v0\n\t"
    "v_pk_mul_f16 v132, v132, v0\n\t"
    "v_pk_mul_f16 v133, v133, v0\n\t"
    "v_pk_mul_f16 v134, v134, v0\n\t"
    "v_pk_mul_f16 v135, v135, v0\n\t"
    "v_pk_mul_f16 v136, v136, v0\n\t"
    "v_pk_mul_f16 v137, v137, v0\n\t"
    "v_pk_mul_f16 v138, v138, v0\n\t"
    "v_pk_mul_f16 v139, v139, v0\n\t"
    "v_pk_mul_f16 v140, v140, v0\n\t"
    "v_pk_mul_f16 v141, v141, v0\n\t"
    "v_pk_mul_f16 v142, v142, v0\n\t"
    "v_pk_mul_f16 v143, v143, v0\n\t"
    "v_pk_mul_f16 v144, v144, v0\n\t"
    "v_pk_mul_f16 v145, v145, v0\n\t"
    "v_pk_mul_f16 v146, v146, v0\n\t"
    "v_pk_mul_f16 v147, v147, v0\n\t"
    "v_pk_mul_f16 v148, v148, v0\n\t"
    "v_pk_mul_f16 v149, v149, v0\n\t"
    "v_pk_mul_f16 v150, v150, v0\n\t"
    "v_pk_mul_f16 v151, v151, v0\n\t"
    "v_pk_mul_f16 v152, v152, v0\n\t"
    "v_pk_mul_f16 v153, v153, v0\n\t"
    "v_pk_mul_f16 v154, v154, v0\n\t"
    "v_pk_mul_f16 v155, v155, v0\n\t"
    "v_pk_mul_f16 v156, v156, v0\n\t"
    "v_pk_mul_f16 v157, v157, v0\n\t"
    "v_pk_mul_f16 v158, v158, v0\n\t"
    "v_pk_mul_f16 v159, v159, v0\n\t"
    "s_branch Lv46_%=\n\t"
    ".p2align 12\n\t"
    "Lend_%=:\n\t"
    : "+{v0}"(tid)
    : "{s[0:1]}"(__builtin_amdgcn_kernarg_segment_ptr()), "{s2}"(blockIdx.x), "{s3}"(lds_base)
    : "v1","v2","v3","v4","v5","v6","v7","v8","v9","v10","v11","v12","v13","v14","v15","v16","v17","v18","v19","v20","v21","v22","v23","v24","v25","v26","v27","v28","v29","v30","v31","v32","v33","v34","v35","v36","v37","v38","v39","v40","v41","v42","v43","v44","v45","v46","v47","v48","v49","v50","v51","v52","v53","v54","v55","v56","v57","v58","v59","v60","v61","v62","v63","v64","v65","v66","v67","v68","v69","v70","v71","v72","v73","v74","v75","v76","v77","v78","v79","v80","v81","v82","v83","v84","v85","v86","v87","v88","v89","v90","v91","v92","v93","v94","v95","v96","v97","v98","v99","v100","v101","v102","v103","v104","v105","v106","v107","v108","v109","v110","v111","v112","v113","v114","v115","v116","v117","v118","v119","v120","v121","v122","v123","v124","v125","v126","v127","v128","v129","v130","v131","v132","v133","v134","v135","v136","v137","v138","v139","v140","v141","v142","v143","v144","v145","v146","v147","v148","v149","v150","v151","v152","v153","v154","v155","v156","v157","v158","v159","v160","v161","v162","v163","v164","v165","v166","v167","v168","v169","v170","v171","v172","v173","v174","v175","v176","v177","v178","v179","v180","v181","v182","v183","v184","v185","v186","v187","v188","v189","v190","v191","v192","v193","v194","v195","v196","v197","v198","v199","v200","v201","v202","v203","v204","v205","v206","v207","v208","v209","v210","v211","v212","v213","v214","v215","v216","v217","v218","v219","v220","v221","v222","v223","v224","v225","v226","v227","v228","v229","v230","v231","v232","v233","v234","v235","v236","v237","v238","v239","v240","v241","v242","v243","v244","v245","v246","v247","v248","v249","v250","v251","v252","v253","v254","v255","a0","a1","a2","a3","a4","a5","a6","a7","a8","a9","a10","a11","a12","a13","a14","a15","a16","a17","a18","a19","a20","a21","a22","a23","a24","a25","a26","a27","a28","a29","a30","a31","a32","a33","a34","a35","a36","a37","a38","a39","a40","a41","a42","a43","a44","a45","a46","a47","a48","a49","a50","a51","a52","a53","a54","a55","a56","a57","a58","a59","a60","a61","a62","a63","a64","a65","a66","a67","a68","a69","a70","a71","a72","a73","a74","a75","a76","a77","a78","a79","a80","a81","a82","a83","a84","a85","a86","a87","a88","a89","a90","a91","a92","a93","a94","a95","a96","a97","a98","a99","a100","a101","a102","a103","a104","a105","a106","a107","a108","a109","a110","a111","a112","a113","a114","a115","a116","a117","a118","a119","a120","a121","a122","a123","a124","a125","a126","a127","a128","a129","a130","a131","a132","a133","a134","a135","a136","a137","a138","a139","a140","a141","a142","a143","a144","a145","a146","a147","a148","a149","a150","a151","a152","a153","a154","a155","a156","a157","a158","a159","a160","a161","a162","a163","a164","a165","a166","a167","a168","a169","a170","a171","a172","a173","a174","a175","a176","a177","a178","a179","a180","a181","a182","a183","a184","a185","a186","a187","a188","a189","a190","a191","a192","a193","a194","a195","a196","a197","a198","a199","a200","a201","a202","a203","a204","a205","a206","a207","a208","a209","a210","a211","a212","a213","a214","a215","a216","a217","a218","a219","a220","a221","a222","a223","a224","a225","a226","a227","a228","a229","a230","a231","a232","a233","a234","a235","a236","a237","a238","a239","a240","a241","a242","a243","a244","a245","a246","a247","a248","a249","a250","a251","a252","a253","a254","a255","s4","s5","s6","s7","s8","s9","s10","s11","s12","s13","s14","s15","s16","s17","s18","s19","s20","s21","s22","s23","s24","s25","s26","s27","s28","s29","s30","s31","s34","s35","s36","s37","s38","s39","s40","s41","s42","s43","s44","s45","s46","s47","s48","s49","s50","s51","s52","s53","s54","s55","s56","s57","s58","s59","s60","s61","s62","s63","s64","s65","s66","s67","s68","s69","s70","s71","s72","s73","s74","s75","s76","s77","s78","s79","s80","s81","s82","s83","s84","s85","s86","s87","s88","s89","s90","s91","s92","s93","s94","s95","s96","s97","s98","s99","vcc","scc","memory");
}

extern "C" void kernel_launch(void* const* d_in, const int* in_sizes, int n_in, void* d_out, int out_size, void* d_ws, size_t ws_size,
                              hipStream_t stream) {
    fused_kernel<<<256, 256, 0, stream>>>(d_in[0], d_in[1], d_in[2], d_in[4], d_in[5], d_in[6], d_in[7], d_out, d_ws);
}
```
